```python
import torch

def _softmax(x, axis=-1):
    return torch.softmax(x, dim=axis)

def _gelu(x):
    return torch.nn.functional.gelu(x, approximate='tanh')

def _silu(x):
    return torch.nn.functional.silu(x)

def _sigmoid(x):
    return torch.sigmoid(x)

def _layernorm(x, axis=-1, eps=1e-05):
    m = x.mean(dim=axis, keepdim=True)
    v = x.var(dim=axis, keepdim=True, unbiased=False)
    return (x - m) / torch.sqrt(v + eps)

def _scatter_add(tgt, idx, src, axis=0):
    return tgt.clone().index_add_(axis, idx, src)

def _segment_sum(src, seg, n):
    out = torch.zeros((n,) + src.shape[1:], dtype=src.dtype)
    return out.index_add_(0, seg, src)

def _pad(x, pw):
    flat = tuple((v for pair in reversed(pw) for v in pair))
    return torch.nn.functional.pad(x, flat)
kernel_name = 'adaLN_cond__compressed_kv_cla__dense_activation_relu__decode'

def setup_inputs():

    _w = lambda a: (lambda t: t.to(torch.bfloat16) if t.is_floating_point() else t)(torch.as_tensor(a))

    def _A_setup():
        B, H, Sq, Sk, D, M = (1, 32, 1, 8192, 192, 640)
        Dc = 4 * D
        return {'x': _w((lambda *s: torch.randn(*s, dtype=torch.bfloat16) * 0.1)(B, Sq, M)), 'c_kv': _w((lambda *s: torch.randn(*s, dtype=torch.bfloat16) * 3.2)(B, Sk, Dc)), 'w_q': _w((lambda *s: torch.randn(*s, dtype=torch.bfloat16) * 1.6)(M, H, D) * M ** (-0.5)), 'w_uk': _w((lambda *s: torch.randn(*s, dtype=torch.bfloat16) * 1.6)(Dc, H, D) * Dc ** (-0.5)), 'w_uv': _w((lambda *s: torch.randn(*s, dtype=torch.bfloat16) * 3.2)(Dc, H, D) * Dc ** (-0.5)), 'w_o': _w((lambda *s: torch.randn(*s, dtype=torch.bfloat16) * 3.2)(H, D, M) * (H * D) ** (-0.5))}
    _A_in = _A_setup()

    def _B_setup():
        B, S, D, Dff = (1, 1, 640, 1536)
        return {'x': _w((lambda *s: torch.randn(*s, dtype=torch.bfloat16) * 0.1)(B, S, D)), 'w1': _w((lambda *s: torch.randn(*s, dtype=torch.bfloat16) * 0.1)(D, Dff)), 'w2': _w((lambda *s: torch.randn(*s, dtype=torch.bfloat16) * 0.1)(Dff, D))}
    _B_in = _B_setup()
    return {'A_x': _A_in['x'], 'A_c_kv': _A_in['c_kv'], 'A_w_q': _A_in['w_q'], 'A_w_uk': _A_in['w_uk'], 'A_w_uv': _A_in['w_uv'], 'A_w_o': _A_in['w_o'], 'B_x': _B_in['x'], 'B_w1': _B_in['w1'], 'B_w2': _B_in['w2'], 'c': _w((lambda *s: torch.randn(*s, dtype=torch.bfloat16) * 0.8)(1, 640) * 8.0), 'w_mod': _w((lambda *s: torch.randn(*s, dtype=torch.bfloat16) * 0.8)(640, 6 * 640) * 640 ** (-0.5) * 8.0)}

def reference(A_x, A_c_kv, A_w_q, A_w_uk, A_w_uv, A_w_o, B_x, B_w1, B_w2, c, w_mod):

    def _A_ref(x, c_kv, w_q, w_uk, w_uv, w_o):
        B, Sq, M = (x.shape[0], x.shape[1], x.shape[2])
        Sk = c_kv.shape[1]
        H, D = (w_q.shape[1], w_q.shape[2])
        q = torch.einsum('bqm,mhd->bhqd', x, w_q)
        k = torch.einsum('bkc,chd->bhkd', c_kv, w_uk)
        v = torch.einsum('bkc,chd->bhkd', c_kv, w_uv)
        scores = torch.einsum('bhqd,bhkd->bhqk', q, k) * D ** (-0.5)
        off = Sk - Sq
        qi = torch.arange(Sq)[:, None]
        kj = torch.arange(Sk)[None, :]
        cmask = kj <= qi + off
        scores = torch.where(cmask[None, None], scores, -1000000000.0)
        p = _softmax(scores, axis=-1)
        ctx = torch.einsum('bhqk,bhkd->bhqd', p, v)
        return torch.einsum('bhqd,hdm->bqm', ctx, w_o)

    def _B_ref(x, w1, w2):
        h = torch.einsum('bsd,df->bsf', x, w1)
        zero = torch.zeros_like(h)
        h = torch.maximum(h, zero)
        return torch.einsum('bsf,fd->bsd', h, w2)
    x = A_x
    mod = (_silu(c) @ w_mod).reshape(c.shape[0], 6, 1, -1)
    sa, ca, ga, sb, cb, gb = (mod[:, 0].clone(), mod[:, 1].clone(), mod[:, 2].clone(), mod[:, 3].clone(), mod[:, 4].clone(), mod[:, 5].clone())
    h = _layernorm(x, axis=-1) * (1 + ca) + sa
    x = x + ga * _A_ref(h, A_c_kv, A_w_q, A_w_uk, A_w_uv, A_w_o)
    h = _layernorm(x, axis=-1) * (1 + cb) + sb
    x = x + gb * _B_ref(h, B_w1, B_w2)
    return x
```

```cpp
#include <hip/hip_runtime.h>
#include <hip/hip_bf16.h>
#include <stdint.h>
#include <stdio.h>

typedef unsigned short u16;
__device__ __forceinline__ float bf2f(u16 v) { return __uint_as_float(((unsigned)v) << 16); }
__device__ __forceinline__ u16 f2bf(float f) {
    unsigned u = __float_as_uint(f);
    return (u16)((u + 0x7FFFu + ((u >> 16) & 1u)) >> 16);
}
__device__ __forceinline__ float rbf(float f) { return bf2f(f2bf(f)); }

constexpr int H = 32, SK = 8192, D = 192, M = 640, DC = 768, DFF = 1536;

struct WS {
    float mod[3840];
    float q[H * D];
    float qabs[H * DC];
    float sc[H * SK];
    float ctxlat[H * DC];
    float ctx[H * D];
    float x1[M];
    float h1[DFF];
};

__global__ void k_mod(const u16* c, const u16* w_mod, WS* ws) {
    int j = blockIdx.x * blockDim.x + threadIdx.x;
    if (j >= 3840) return;
    float acc = 0.f;
    for (int m = 0; m < M; ++m) {
        float cv = bf2f(c[m]);
        float s = rbf(cv / (1.f + __expf(-cv)));
        acc += s * bf2f(w_mod[m * 3840 + j]);
    }
    ws->mod[j] = rbf(acc);
}

__device__ void ln_mod(const float* xs, float* hs, const float* ss, const float* cc, float* red) {
    int t = threadIdx.x;
    float s = 0.f;
    for (int m = t; m < M; m += blockDim.x) s += xs[m];
    red[t] = s; __syncthreads();
    for (int o = blockDim.x / 2; o > 0; o >>= 1) { if (t < o) red[t] += red[t + o]; __syncthreads(); }
    float mean32 = red[0] / M; __syncthreads();
    float s2 = 0.f;
    for (int m = t; m < M; m += blockDim.x) { float d = xs[m] - mean32; s2 += d * d; }
    red[t] = s2; __syncthreads();
    for (int o = blockDim.x / 2; o > 0; o >>= 1) { if (t < o) red[t] += red[t + o]; __syncthreads(); }
    float var = rbf(red[0] / M); __syncthreads();
    float mean = rbf(mean32);
    float den = rbf(sqrtf(rbf(var + 1e-5f)));
    for (int m = t; m < M; m += blockDim.x) {
        float ln = rbf(rbf(xs[m] - mean) / den);
        hs[m] = rbf(rbf(ln * rbf(1.f + cc[m])) + ss[m]);
    }
    __syncthreads();
}

__global__ void k_q(const u16* x, const u16* w_q, WS* ws) {
    __shared__ float xs[M], hs[M], red[256];
    for (int m = threadIdx.x; m < M; m += blockDim.x) xs[m] = bf2f(x[m]);
    __syncthreads();
    ln_mod(xs, hs, ws->mod, ws->mod + 640, red);
    int j = blockIdx.x * blockDim.x + threadIdx.x;
    float acc = 0.f;
    for (int m = 0; m < M; ++m) acc += hs[m] * bf2f(w_q[m * (H * D) + j]);
    ws->q[j] = rbf(acc);
}

__global__ void k_qabs(const u16* w_uk, WS* ws) {
    int idx = blockIdx.x * blockDim.x + threadIdx.x;
    int h = idx / DC, c = idx % DC;
    float acc = 0.f;
    for (int d = 0; d < D; ++d) acc += ws->q[h * D + d] * bf2f(w_uk[(c * H + h) * D + d]);
    ws->qabs[idx] = acc;
}

__global__ void k_scores(const u16* ckv, WS* ws) {
    int idx = blockIdx.x * blockDim.x + threadIdx.x;
    int h = idx / SK, k = idx % SK;
    float acc = 0.f;
    for (int c = 0; c < DC; ++c) acc += ws->qabs[h * DC + c] * bf2f(ckv[k * DC + c]);
    float s = rbf(acc);
    s = rbf(s * 0.07216878364870322f);
    ws->sc[idx] = s;
}

__global__ void k_pv(const u16* ckv, WS* ws) {
    __shared__ float red[256];
    __shared__ float p[SK];
    int h = blockIdx.x, t = threadIdx.x;
    float mx = -3e38f;
    for (int k = t; k < SK; k += 256) mx = fmaxf(mx, ws->sc[h * SK + k]);
    red[t] = mx; __syncthreads();
    for (int o = 128; o > 0; o >>= 1) { if (t < o) red[t] = fmaxf(red[t], red[t + o]); __syncthreads(); }
    mx = red[0]; __syncthreads();
    float s = 0.f;
    for (int k = t; k < SK; k += 256) { float e = __expf(ws->sc[h * SK + k] - mx); p[k] = e; s += e; }
    red[t] = s; __syncthreads();
    for (int o = 128; o > 0; o >>= 1) { if (t < o) red[t] += red[t + o]; __syncthreads(); }
    s = red[0]; __syncthreads();
    for (int k = t; k < SK; k += 256) p[k] = rbf(p[k] / s);
    __syncthreads();
    for (int c = t; c < DC; c += 256) {
        float acc = 0.f;
        for (int k = 0; k < SK; ++k) acc += p[k] * bf2f(ckv[k * DC + c]);
        ws->ctxlat[h * DC + c] = acc;
    }
}

__global__ void k_ctx(const u16* w_uv, WS* ws) {
    int idx = blockIdx.x * blockDim.x + threadIdx.x;
    int h = idx / D, d = idx % D;
    float acc = 0.f;
    for (int c = 0; c < DC; ++c) acc += ws->ctxlat[h * DC + c] * bf2f(w_uv[(c * H + h) * D + d]);
    ws->ctx[idx] = rbf(acc);
}

__global__ void k_o(const u16* x, const u16* w_o, WS* ws) {
    int m = blockIdx.x * blockDim.x + threadIdx.x;
    if (m >= M) return;
    float acc = 0.f;
    for (int j = 0; j < H * D; ++j) acc += ws->ctx[j] * bf2f(w_o[j * M + m]);
    float a = rbf(acc);
    float g = ws->mod[1280 + m];
    ws->x1[m] = rbf(bf2f(x[m]) + rbf(g * a));
}

__global__ void k_h1(const u16* w1, WS* ws) {
    __shared__ float xs[M], hs[M], red[256];
    for (int m = threadIdx.x; m < M; m += blockDim.x) xs[m] = ws->x1[m];
    __syncthreads();
    ln_mod(xs, hs, ws->mod + 1920, ws->mod + 2560, red);
    int j = blockIdx.x * blockDim.x + threadIdx.x;
    float acc = 0.f;
    for (int m = 0; m < M; ++m) acc += hs[m] * bf2f(w1[m * DFF + j]);
    ws->h1[j] = fmaxf(rbf(acc), 0.f);
}

__global__ void k_out(const u16* w2, WS* ws, u16* out) {
    int m = blockIdx.x * blockDim.x + threadIdx.x;
    if (m >= M) return;
    float acc = 0.f;
    for (int j = 0; j < DFF; ++j) acc += ws->h1[j] * bf2f(w2[j * M + m]);
    float a = rbf(acc);
    float g = ws->mod[3200 + m];
    out[m] = f2bf(ws->x1[m] + rbf(g * a));
}

__global__ void k_dbg(const u16* x, const u16* c, const u16* ckv, WS* ws, u16* out) {
    if (threadIdx.x == 0) {
        printf("DBG x: %g %g %g %g  c: %g %g %g %g ckv: %g %g\n", bf2f(x[0]), bf2f(x[1]), bf2f(x[2]), bf2f(x[3]), bf2f(c[0]), bf2f(c[1]), bf2f(c[2]), bf2f(c[3]), bf2f(ckv[0]), bf2f(ckv[1]));
        printf("DBG out: %g %g %g %g\n", bf2f(out[0]), bf2f(out[1]), bf2f(out[2]), bf2f(out[3]));
    }
}

extern "C" void kernel_launch(void* const* d_in, const int* in_sizes, int n_in,
                              void* d_out, int out_size, void* d_ws, size_t ws_size,
                              hipStream_t stream) {
    const u16* x = (const u16*)d_in[0];
    const u16* ckv = (const u16*)d_in[1];
    const u16* w_q = (const u16*)d_in[2];
    const u16* w_uk = (const u16*)d_in[3];
    const u16* w_uv = (const u16*)d_in[4];
    const u16* w_o = (const u16*)d_in[5];
    const u16* w1 = (const u16*)d_in[7];
    const u16* w2 = (const u16*)d_in[8];
    const u16* c = (const u16*)d_in[9];
    const u16* w_mod = (const u16*)d_in[10];
    WS* ws = (WS*)d_ws;
    u16* out = (u16*)d_out;
    k_mod<<<15, 256, 0, stream>>>(c, w_mod, ws);
    k_q<<<24, 256, 0, stream>>>(x, w_q, ws);
    k_qabs<<<96, 256, 0, stream>>>(w_uk, ws);
    k_scores<<<1024, 256, 0, stream>>>(ckv, ws);
    k_pv<<<32, 256, 0, stream>>>(ckv, ws);
    k_ctx<<<24, 256, 0, stream>>>(w_uv, ws);
    k_o<<<3, 256, 0, stream>>>(x, w_o, ws);
    k_h1<<<6, 256, 0, stream>>>(w1, ws);
    k_out<<<3, 256, 0, stream>>>(w2, ws, out);
}
```

```cpp
#include <hip/hip_runtime.h>
#include <hip/hip_bf16.h>
#include <stdint.h>
#include <stdio.h>

typedef unsigned short u16;
__device__ __forceinline__ float bf2f(u16 v) { return __uint_as_float(((unsigned)v) << 16); }
__device__ __forceinline__ u16 f2bf(float f) {
    unsigned u = __float_as_uint(f);
    return (u16)((u + 0x7FFFu + ((u >> 16) & 1u)) >> 16);
}
__device__ __forceinline__ float rbf(float f) { return bf2f(f2bf(f)); }

constexpr int H = 32, SK = 8192, D = 192, M = 640, DC = 768, DFF = 1536;

struct WS {
    float mod[3840];
    float q[H * D];
    float qabs[H * DC];
    float sc[H * SK];
    float ctxlat[H * DC];
    float ctx[H * D];
    float x1[M];
    float h1[DFF];
};

__global__ void k_mod(const u16* c, const u16* w_mod, WS* ws) {
    int j = blockIdx.x * blockDim.x + threadIdx.x;
    if (j >= 3840) return;
    float acc = 0.f;
    for (int m = 0; m < M; ++m) {
        float cv = bf2f(c[m]);
        float s = rbf(cv / (1.f + __expf(-cv)));
        acc += s * bf2f(w_mod[m * 3840 + j]);
    }
    ws->mod[j] = rbf(acc);
}

__device__ void ln_mod(const float* xs, float* hs, const float* ss, const float* cc, float* red) {
    int t = threadIdx.x;
    float s = 0.f;
    for (int m = t; m < M; m += blockDim.x) s += xs[m];
    red[t] = s; __syncthreads();
    for (int o = blockDim.x / 2; o > 0; o >>= 1) { if (t < o) red[t] += red[t + o]; __syncthreads(); }
    float mean32 = red[0] / M; __syncthreads();
    float s2 = 0.f;
    for (int m = t; m < M; m += blockDim.x) { float d = xs[m] - mean32; s2 += d * d; }
    red[t] = s2; __syncthreads();
    for (int o = blockDim.x / 2; o > 0; o >>= 1) { if (t < o) red[t] += red[t + o]; __syncthreads(); }
    float var = rbf(red[0] / M); __syncthreads();
    float mean = rbf(mean32);
    float den = rbf(sqrtf(rbf(var + 1e-5f)));
    for (int m = t; m < M; m += blockDim.x) {
        float ln = rbf(rbf(xs[m] - mean) / den);
        hs[m] = rbf(rbf(ln * rbf(1.f + cc[m])) + ss[m]);
    }
    __syncthreads();
}

__global__ void k_q(const u16* x, const u16* w_q, WS* ws) {
    __shared__ float xs[M], hs[M], red[256];
    for (int m = threadIdx.x; m < M; m += blockDim.x) xs[m] = bf2f(x[m]);
    __syncthreads();
    ln_mod(xs, hs, ws->mod, ws->mod + 640, red);
    int j = blockIdx.x * blockDim.x + threadIdx.x;
    float acc = 0.f;
    for (int m = 0; m < M; ++m) acc += hs[m] * bf2f(w_q[m * (H * D) + j]);
    ws->q[j] = rbf(acc);
}

__global__ void k_qabs(const u16* w_uk, WS* ws) {
    int idx = blockIdx.x * blockDim.x + threadIdx.x;
    int h = idx / DC, c = idx % DC;
    float acc = 0.f;
    for (int d = 0; d < D; ++d) acc += ws->q[h * D + d] * bf2f(w_uk[(c * H + h) * D + d]);
    ws->qabs[idx] = acc;
}

__global__ void k_scores(const u16* ckv, WS* ws) {
    int idx = blockIdx.x * blockDim.x + threadIdx.x;
    int h = idx / SK, k = idx % SK;
    float acc = 0.f;
    for (int c = 0; c < DC; ++c) acc += ws->qabs[h * DC + c] * bf2f(ckv[k * DC + c]);
    float s = rbf(acc);
    s = rbf(s * 0.07216878364870322f);
    ws->sc[idx] = s;
}

__global__ void k_pv(const u16* ckv, WS* ws) {
    __shared__ float red[256];
    __shared__ float p[SK];
    int h = blockIdx.x, t = threadIdx.x;
    float mx = -3e38f;
    for (int k = t; k < SK; k += 256) mx = fmaxf(mx, ws->sc[h * SK + k]);
    red[t] = mx; __syncthreads();
    for (int o = 128; o > 0; o >>= 1) { if (t < o) red[t] = fmaxf(red[t], red[t + o]); __syncthreads(); }
    mx = red[0]; __syncthreads();
    float s = 0.f;
    for (int k = t; k < SK; k += 256) { float e = __expf(ws->sc[h * SK + k] - mx); p[k] = e; s += e; }
    red[t] = s; __syncthreads();
    for (int o = 128; o > 0; o >>= 1) { if (t < o) red[t] += red[t + o]; __syncthreads(); }
    s = red[0]; __syncthreads();
    for (int k = t; k < SK; k += 256) p[k] = rbf(p[k] / s);
    __syncthreads();
    for (int c = t; c < DC; c += 256) {
        float acc = 0.f;
        for (int k = 0; k < SK; ++k) acc += p[k] * bf2f(ckv[k * DC + c]);
        ws->ctxlat[h * DC + c] = acc;
    }
}

__global__ void k_ctx(const u16* w_uv, WS* ws) {
    int idx = blockIdx.x * blockDim.x + threadIdx.x;
    int h = idx / D, d = idx % D;
    float acc = 0.f;
    for (int c = 0; c < DC; ++c) acc += ws->ctxlat[h * DC + c] * bf2f(w_uv[(c * H + h) * D + d]);
    ws->ctx[idx] = rbf(acc);
}

__global__ void k_o(const u16* x, const u16* w_o, WS* ws) {
    int m = blockIdx.x * blockDim.x + threadIdx.x;
    if (m >= M) return;
    float acc = 0.f;
    for (int j = 0; j < H * D; ++j) acc += ws->ctx[j] * bf2f(w_o[j * M + m]);
    float a = rbf(acc);
    float g = ws->mod[1280 + m];
    ws->x1[m] = rbf(bf2f(x[m]) + rbf(g * a));
}

__global__ void k_h1(const u16* w1, WS* ws) {
    __shared__ float xs[M], hs[M], red[256];
    for (int m = threadIdx.x; m < M; m += blockDim.x) xs[m] = ws->x1[m];
    __syncthreads();
    ln_mod(xs, hs, ws->mod + 1920, ws->mod + 2560, red);
    int j = blockIdx.x * blockDim.x + threadIdx.x;
    float acc = 0.f;
    for (int m = 0; m < M; ++m) acc += hs[m] * bf2f(w1[m * DFF + j]);
    ws->h1[j] = fmaxf(rbf(acc), 0.f);
}

__global__ void k_out(const u16* w2, WS* ws, u16* out) {
    int m = blockIdx.x * blockDim.x + threadIdx.x;
    if (m >= M) return;
    float acc = 0.f;
    for (int j = 0; j < DFF; ++j) acc += ws->h1[j] * bf2f(w2[j * M + m]);
    float a = rbf(acc);
    float g = ws->mod[3200 + m];
    out[m] = f2bf(ws->x1[m] + rbf(g * a));
}

__global__ void k_dbg(const u16* x, const u16* c, const u16* ckv, WS* ws, u16* out) {
    if (threadIdx.x == 0) {
        printf("DBG x: %g %g %g %g  c: %g %g %g %g ckv: %g %g\n", bf2f(x[0]), bf2f(x[1]), bf2f(x[2]), bf2f(x[3]), bf2f(c[0]), bf2f(c[1]), bf2f(c[2]), bf2f(c[3]), bf2f(ckv[0]), bf2f(ckv[1]));
        printf("DBG out: %g %g %g %g\n", bf2f(out[0]), bf2f(out[1]), bf2f(out[2]), bf2f(out[3]));
    }
}

#define NT 0

typedef unsigned int u32;
typedef unsigned long long u64;
typedef __attribute__((ext_vector_type(4))) unsigned int u32x4;
struct PArgs { const u32x4* p[8]; u32* ws; unsigned n16[8]; };
__device__ __forceinline__ u64 rt() { return __builtin_amdgcn_s_memrealtime(); }
template<int T> __device__ __forceinline__ u32x4 ld(const u32x4* p) {
    return *p;
}
__global__ __launch_bounds__(512, 2) void k_stream(PArgs a) {
    u64 t0 = rt();
    const int tid = blockIdx.x * 512 + threadIdx.x;
    const int nth = 256 * 512;
    u32x4 acc = {0, 0, 0, 0};
    u32x4 v[29];
    int k = 0;
#define LDT(t, cnt) _Pragma("unroll") for (int i = 0; i < cnt; ++i) { unsigned idx = tid + i * nth; if (idx >= a.n16[t]) idx = tid % 1024; v[k++] = ld<0>(a.p[t] + idx); }
    LDT(0, 3) LDT(1, 4) LDT(2, 5) LDT(3, 6) LDT(4, 5) LDT(5, 4) LDT(6, 1) LDT(7, 1)
    u64 ts[8];
    k = 0;
#define USE(t, cnt) _Pragma("unroll") for (int i = 0; i < cnt; ++i) { acc ^= v[k++]; } asm volatile("" :: "v"(acc.x), "v"(acc.y), "v"(acc.z), "v"(acc.w)); ts[t] = rt(); __builtin_amdgcn_sched_barrier(0);
    USE(0, 3) USE(1, 4) USE(2, 5) USE(3, 6) USE(4, 5) USE(5, 4) USE(6, 1) USE(7, 1)
    u32 r = acc.x ^ acc.y ^ acc.z ^ acc.w;
    if (r == 0x12345678u) a.ws[1 << 22] = r;
    if ((threadIdx.x & 63) == 0) {
        u64* o = (u64*)(a.ws + (1 << 23)) + (blockIdx.x * 8 + (threadIdx.x >> 6)) * 16;
        o[0] = t0;
        for (int t = 0; t < 8; ++t) o[1 + t] = ts[t];
    }
}
__global__ void k_report(u32* ws) {
    __shared__ u64 smin[256];
    __shared__ u64 smax[9][256];
    const u64* o = (const u64*)(ws + (1 << 23));
    int t = threadIdx.x;
    u64 mn = ~0ull, mx[9];
    for (int j = 0; j < 9; ++j) mx[j] = 0;
    for (int w = t; w < 2048; w += 256) { mn = min(mn, o[w * 16]); for (int j = 0; j < 9; ++j) mx[j] = max(mx[j], o[w * 16 + j]); }
    smin[t] = mn; for (int j = 0; j < 9; ++j) smax[j][t] = mx[j];
    __syncthreads();
    if (t == 0) {
        for (int i = 1; i < 256; ++i) { mn = min(mn, smin[i]); for (int j = 0; j < 9; ++j) mx[j] = max(mx[j], smax[j][i]); }
        printf("PROBE start_spread %d | %d %d %d %d %d %d %d %d (x10ns)\n", (int)(mx[0]-mn), (int)(mx[1]-mn), (int)(mx[2]-mn), (int)(mx[3]-mn), (int)(mx[4]-mn), (int)(mx[5]-mn), (int)(mx[6]-mn), (int)(mx[7]-mn), (int)(mx[8]-mn));
    }
}
extern "C" void kernel_launch(void* const* d_in, const int* in_sizes, int n_in,
                              void* d_out, int out_size, void* d_ws, size_t ws_size,
                              hipStream_t stream) {
    const u16* x = (const u16*)d_in[0];
    const u16* ckv = (const u16*)d_in[1];
    const u16* w_q = (const u16*)d_in[2];
    const u16* w_uk = (const u16*)d_in[3];
    const u16* w_uv = (const u16*)d_in[4];
    const u16* w_o = (const u16*)d_in[5];
    const u16* w1 = (const u16*)d_in[7];
    const u16* w2 = (const u16*)d_in[8];
    const u16* c = (const u16*)d_in[9];
    const u16* w_mod = (const u16*)d_in[10];
    WS* ws = (WS*)d_ws;
    u16* out = (u16*)d_out;

    {
        PArgs a;
        int idx[8] = {10, 2, 3, 1, 4, 5, 7, 8};
        for (int i = 0; i < 8; ++i) { a.p[i] = (const u32x4*)d_in[idx[i]]; a.n16[i] = (unsigned)in_sizes[idx[i]] / 8; }
        a.ws = (u32*)d_ws;
        k_stream<<<256, 512, 0, stream>>>(a);
        k_report<<<1, 256, 0, stream>>>((u32*)d_ws);
    }
    k_mod<<<15, 256, 0, stream>>>(c, w_mod, ws);
    k_q<<<24, 256, 0, stream>>>(x, w_q, ws);
    k_qabs<<<96, 256, 0, stream>>>(w_uk, ws);
    k_scores<<<1024, 256, 0, stream>>>(ckv, ws);
    k_pv<<<32, 256, 0, stream>>>(ckv, ws);
    k_ctx<<<24, 256, 0, stream>>>(w_uv, ws);
    k_o<<<3, 256, 0, stream>>>(x, w_o, ws);
    k_h1<<<6, 256, 0, stream>>>(w1, ws);
    k_out<<<3, 256, 0, stream>>>(w2, ws, out);
}
```

```cpp
#include <hip/hip_runtime.h>
#include <stdint.h>

typedef unsigned u32;
typedef unsigned long long u64;
typedef unsigned short u16;
typedef u32 u32x2 __attribute__((ext_vector_type(2)));
typedef u32 u32x3 __attribute__((ext_vector_type(3)));
typedef u32 u32x4 __attribute__((ext_vector_type(4)));
typedef float f32x2 __attribute__((ext_vector_type(2)));
typedef float f32x4 __attribute__((ext_vector_type(4)));
typedef short bf16x8 __attribute__((ext_vector_type(8)));
typedef short s16x4 __attribute__((ext_vector_type(4)));
typedef __amdgpu_buffer_rsrc_t rsrc_t;
#define LAS __attribute__((address_space(3)))
#define GAS __attribute__((address_space(1)))

constexpr int NH = 32, SK = 8192, HD = 192, DM = 640, DC = 768, DFF = 1536;
constexpr int NWG = 256, NT = 512;

constexpr u32 OFF_EPOCH = 0;
constexpr u32 OFF_PF = 256;
constexpr u32 OFF_E1 = OFF_PF + 256 * 16;
constexpr u32 OFF_E2 = OFF_E1 + 4 * 320 * 16;
constexpr u32 OFF_E3 = OFF_E2 + 4 * 32 * 96 * 16;
constexpr u32 OFF_ML = OFF_E3 + 32 * 4 * 768;
constexpr u32 OFF_E5 = OFF_ML + 32 * 128 * 8;
constexpr u32 OFF_E6 = OFF_E5 + 4 * 6144 * 4;
constexpr u32 OFF_G = OFF_E6 + 12 * 640 * 4;
constexpr u32 OFF_E7 = OFF_G + 2 * 2560 * 4;
constexpr u32 OFF_O = OFF_E7 + 48 * 640 * 4;
constexpr u32 WS_BYTES = OFF_O + 32 * 4 * 128 * 384;
constexpr u32 OFF_DIAG = WS_BYTES;
static_assert(OFF_O % 128 == 0 && OFF_E3 % 128 == 0 && OFF_E5 % 128 == 0, "alignment");

constexpr int L_KV = 0;
constexpr int L_SP = 98304;
constexpr int L_P = L_SP + 4 * 32 * 68 * 4;
constexpr int L_RED = L_P + 32 * 144;
constexpr int LDS_BYTES = L_RED + 8192;
constexpr int SPIN = 1 << 16;
constexpr u32 POISON = 0xFFFFFFFFu;
constexpr int AUX_SC1 = 16, AUX_NT = 2;
constexpr u64 TAGMUL = 0x9E3779B97F4A7C15ull;

__device__ __forceinline__ float bflo(u32 v) { return __uint_as_float(v << 16); }
__device__ __forceinline__ float bfhi(u32 v) { return __uint_as_float(v & 0xFFFF0000u); }
__device__ __forceinline__ float bf1(u16 v) { return __uint_as_float((u32)v << 16); }
typedef __bf16 bf16x2_t __attribute__((ext_vector_type(2)));
__device__ __forceinline__ u32 cvtpk(float lo, float hi) { f32x2 v = {lo, hi}; return __builtin_bit_cast(u32, __builtin_convertvector(v, bf16x2_t)); }
__device__ __forceinline__ u32 canon(u32 v) { return v < 0xFFFFFFFEu ? v : 0xFFFFFFFEu; }
__device__ __forceinline__ u32 canonf(float v) { return canon(__float_as_uint(v)); }
__device__ __forceinline__ rsrc_t mkrs(const void* p, u32 bytes) { return __builtin_amdgcn_make_buffer_rsrc((void*)p, 0, bytes, 0x00020000); }
__device__ __forceinline__ u32x4 ld4(rsrc_t r, u32 off, int aux) { return aux == AUX_SC1 ? __builtin_amdgcn_raw_buffer_load_b128(r, off, 0, AUX_SC1) : __builtin_amdgcn_raw_buffer_load_b128(r, off, 0, AUX_NT); }
__device__ __forceinline__ u32x3 ld3(rsrc_t r, u32 off, int aux) { return aux == AUX_SC1 ? __builtin_amdgcn_raw_buffer_load_b96(r, off, 0, AUX_SC1) : __builtin_amdgcn_raw_buffer_load_b96(r, off, 0, AUX_NT); }
__device__ __forceinline__ void st4(rsrc_t r, u32 off, u32x4 v) { __builtin_amdgcn_raw_buffer_store_b128(v, r, off, 0, AUX_SC1); }
__device__ __forceinline__ void st2(rsrc_t r, u32 off, u32x2 v) { __builtin_amdgcn_raw_buffer_store_b64(v, r, off, 0, AUX_SC1); }
#define CFENCE() asm volatile("" ::: "memory")
#define SB() __builtin_amdgcn_sched_barrier(0)
#define BAR() asm volatile("s_waitcnt lgkmcnt(0)\n\ts_barrier" ::: "memory")
#define LGKM0() asm volatile("s_waitcnt lgkmcnt(0)" ::: "memory")
#define DRAIN() asm volatile("s_waitcnt vmcnt(0)" ::: "memory")

template <int CTRL> __device__ __forceinline__ float dpp(float x) { return __int_as_float(__builtin_amdgcn_update_dpp(0, __float_as_int(x), CTRL, 0xf, 0xf, true)); }
__device__ __forceinline__ float rs32(float a, float b) { auto r = __builtin_amdgcn_permlane32_swap(__float_as_uint(a), __float_as_uint(b), false, false); return __uint_as_float(r[0]) + __uint_as_float(r[1]); }
__device__ __forceinline__ float rs16(float a, float b) { auto r = __builtin_amdgcn_permlane16_swap(__float_as_uint(a), __float_as_uint(b), false, false); return __uint_as_float(r[0]) + __uint_as_float(r[1]); }
__device__ __forceinline__ float row_sum(float x) { x += dpp<0x128>(x); x += dpp<0x124>(x); x += dpp<0x122>(x); x += dpp<0x121>(x); return x; }
__device__ __forceinline__ float row_max(float x) { x = fmaxf(x, dpp<0x128>(x)); x = fmaxf(x, dpp<0x124>(x)); x = fmaxf(x, dpp<0x122>(x)); x = fmaxf(x, dpp<0x121>(x)); return x; }
__device__ __forceinline__ float wave_sum(float x) { x = row_sum(x); x = rs16(x, x); x = rs32(x, x); return x; }
__device__ __forceinline__ float wave_max(float x) {
    x = row_max(x);
    { auto r = __builtin_amdgcn_permlane16_swap(__float_as_uint(x), __float_as_uint(x), false, false); x = fmaxf(__uint_as_float(r[0]), __uint_as_float(r[1])); }
    { auto r = __builtin_amdgcn_permlane32_swap(__float_as_uint(x), __float_as_uint(x), false, false); x = fmaxf(__uint_as_float(r[0]), __uint_as_float(r[1])); }
    return x;
}
__device__ __forceinline__ float rscat8(const float (&a)[8], int lane) {
    float r0 = rs32(a[0], a[4]), r1 = rs32(a[1], a[5]), r2 = rs32(a[2], a[6]), r3 = rs32(a[3], a[7]);
    float s0 = rs16(r0, r2), s1 = rs16(r1, r3);
    float t0 = s0 + dpp<0x128>(s0), t1 = s1 + dpp<0x128>(s1);
    return (lane & 8) ? t1 : t0;
}
__device__ __forceinline__ bool pois4(u32x4 v) { u32 m = max(max(v.x, v.y), max(v.z, v.w)); return m == POISON; }
__device__ __forceinline__ bool pois3(u32x3 v) { u32 m = max(max(v.x, v.y), v.z); return m == POISON; }
__device__ __forceinline__ bool anyl(bool b) { return __builtin_amdgcn_ballot_w64(b) != 0ull; }
__device__ __forceinline__ f32x4 mfma(u32x4 a, u32x4 b, f32x4 c) { return __builtin_amdgcn_mfma_f32_16x16x32_bf16(__builtin_bit_cast(bf16x8, a), __builtin_bit_cast(bf16x8, b), c, 0, 0, 0); }
__device__ __forceinline__ float silu(float v) { return v / (1.f + __expf(-v)); }
__device__ __forceinline__ void unp8(u32x4 v, float (&f)[8]) { f[0] = bflo(v.x); f[1] = bfhi(v.x); f[2] = bflo(v.y); f[3] = bfhi(v.y); f[4] = bflo(v.z); f[5] = bfhi(v.z); f[6] = bflo(v.w); f[7] = bfhi(v.w); }
__device__ __forceinline__ void unp6(u32x3 v, float (&f)[6]) { f[0] = bflo(v.x); f[1] = bfhi(v.x); f[2] = bflo(v.y); f[3] = bfhi(v.y); f[4] = bflo(v.z); f[5] = bfhi(v.z); }

#define STAMP(i) do { } while (0)

__device__ __forceinline__ void ln_stats(const float (&v)[10], float& mean, float& rstd) {
    float s = 0.f;
#pragma unroll
    for (int i = 0; i < 10; ++i) s += v[i];
    mean = wave_sum(s) * (1.f / 640.f);
    float q = 0.f;
#pragma unroll
    for (int i = 0; i < 10; ++i) { float d = v[i] - mean; q += d * d; }
    float var = wave_sum(q) * (1.f / 640.f);
    rstd = 1.f / sqrtf(var + 1e-5f);
}

__global__ __launch_bounds__(NT, 2) void adaln_mla_mlp_kernel(
    const u16* __restrict__ x, const u16* __restrict__ ckv, const u16* __restrict__ wq, const u16* __restrict__ wuk,
    const u16* __restrict__ wuv, const u16* __restrict__ wo, const u16* __restrict__ w1, const u16* __restrict__ w2,
    const u16* __restrict__ cc, const u16* __restrict__ wmod, u16* __restrict__ out, unsigned char* __restrict__ ws) {
    extern __shared__ __attribute__((aligned(16))) unsigned char smem[];
    LAS unsigned char* L = (LAS unsigned char*)smem;
    const int t = threadIdx.x, lane = t & 63, w = __builtin_amdgcn_readfirstlane(t >> 6);
    const int bid = blockIdx.x;
    const rsrc_t rws = mkrs(ws, WS_BYTES);
    STAMP(0);
    const u64 epoch = __hip_atomic_load((GAS u64*)(ws + OFF_EPOCH), __ATOMIC_RELAXED, __HIP_MEMORY_SCOPE_AGENT);
    const u32x4 PZ = {POISON, POISON, POISON, POISON};
    LAS float* red = (LAS float*)(L + L_RED);

    if (bid < 128) {
        const int xi = bid, h = xi & 31, cq = xi >> 5;
        const rsrc_t rmod = mkrs(wmod, DM * 3840 * 2);
        const bool isM = xi < 80;
        u32x4 mw0, mw1, mw2; u16 c0 = 0, c1 = 0, c2 = 0; u32 xr[5]; u16 xm = 0;
        int m_rg = 0, m_s = 0;
        if (isM) {
            const int x8 = xi & 7, b = (xi >> 3) & 1, slot = xi >> 4, qq = slot * 8 + x8;
            m_rg = qq / 10; m_s = 2 * (qq % 10) + b;
            const int chunk = t & 7, rsub = t >> 3;
            const int col = chunk < 4 ? 32 * m_s + 8 * chunk : 640 + 32 * m_s + 8 * (chunk - 4);
            const int row0 = m_rg * 160 + rsub;
            c0 = cc[row0]; c1 = cc[row0 + 64]; if (w < 4) c2 = cc[row0 + 128];
            if (w == 0) {
#pragma unroll
                for (int i = 0; i < 5; ++i) xr[i] = ((const u32*)x)[lane + 64 * i];
                xm = x[32 * m_s + (lane & 31)];
            }
            const u32 off = (u32)(row0 * 3840 + col) * 2;
            mw0 = ld4(rmod, off, AUX_NT); mw1 = ld4(rmod, off + 64 * 7680, AUX_NT);
            if (w < 4) mw2 = ld4(rmod, off + 128 * 7680, AUX_NT);
            SB();
        }
        if (w == 7) {
            if (lane < 48) { st4(rws, OFF_E3 + (h * 4 + cq) * 768 + lane * 16, PZ); st4(rws, OFF_E5 + (cq * 6144 + h * 192) * 4 + lane * 16, PZ); }
            if (xi < 48) {
                st4(rws, OFF_E7 + xi * 2560 + lane * 16, PZ); st4(rws, OFF_E7 + xi * 2560 + 1024 + lane * 16, PZ);
                if (lane < 32) st4(rws, OFF_E7 + xi * 2560 + 2048 + lane * 16, PZ);
            } else if (lane < 16) {
                const int gi = xi - 48;
                st4(rws, OFF_G + ((gi / 40) * 2560 + (gi % 40) * 64) * 4 + lane * 16, PZ);
            }
            SB();
        }
        const u64 tag = (epoch + 1ull) * TAGMUL;
        const u32 tlo = (u32)tag, thi = (u32)(tag >> 32);
        if (isM) {
            float a[8], f[8];
            const float s0 = silu(bf1(c0)), s1 = silu(bf1(c1));
            unp8(mw0, f);
#pragma unroll
            for (int i = 0; i < 8; ++i) a[i] = s0 * f[i];
            unp8(mw1, f);
#pragma unroll
            for (int i = 0; i < 8; ++i) a[i] += s1 * f[i];
            if (w < 4) {
                const float s2 = silu(bf1(c2));
                unp8(mw2, f);
#pragma unroll
                for (int i = 0; i < 8; ++i) a[i] += s2 * f[i];
            }
            const float v = rscat8(a, lane);
            const int e = ((lane >> 5) & 1) * 4 + ((lane >> 4) & 1) * 2 + ((lane >> 3) & 1);
            red[w * 64 + (lane & 7) * 8 + e] = v;
            float lnx = 0.f;
            if (w == 0) {
                float xv[10];
#pragma unroll
                for (int i = 0; i < 5; ++i) { xv[2 * i] = bflo(xr[i]); xv[2 * i + 1] = bfhi(xr[i]); }
                float mean, rstd; ln_stats(xv, mean, rstd);
                lnx = (bf1(xm) - mean) * rstd;
            }
            BAR();
            if (w == 0) {
                float s = 0.f;
#pragma unroll
                for (int i = 0; i < 8; ++i) s += red[i * 64 + lane];
                auto r = __builtin_amdgcn_permlane32_swap(__float_as_uint(s), __float_as_uint(s), false, false);
                const float sa = __uint_as_float(r[0]), ca = __uint_as_float(r[1]);
                float hv = lnx * ca + sa; if (m_rg == 0) hv += lnx;
                const float hn = dpp<0xB1>(hv);
                if (lane < 32 && !(lane & 1)) st4(rws, OFF_E1 + (m_rg * 320 + m_s * 16 + (lane >> 1)) * 16, u32x4{__float_as_uint(hv), __float_as_uint(hn), tlo, thi});
            }
        }
        STAMP(1);
        if (w == 7) { DRAIN(); if (lane == 0) st4(rws, OFF_PF + bid * 16, u32x4{0u, 0u, tlo, thi}); }
        const rsrc_t ruk = mkrs(wuk, DC * 6144 * 2);
        const int tg = w >> 1, hf = w & 1;
        u32x4 aw[9];
        {
            const u32 base = (u32)(((cq * 192 + tg * 48 + (lane & 15)) * 32 + h) * 192 + hf * 96 + 8 * (lane >> 4)) * 2;
#pragma unroll
            for (int ti = 0; ti < 3; ++ti)
#pragma unroll
                for (int ki = 0; ki < 3; ++ki) aw[ti * 3 + ki] = ld4(ruk, base + ti * 16 * 12288 + ki * 64, AUX_NT);
            SB();
        }
        float qv[24];
        {
            u32x4 g[3];
            const u32 goff = OFF_E2 + (((lane >> 4) * 32 + h) * 96 + hf * 48 + (lane & 15)) * 16;
            for (int it = 0; it < SPIN; ++it) { CFENCE();
#pragma unroll
                for (int i = 0; i < 3; ++i) g[i] = ld4(rws, goff + i * 256, AUX_SC1);
                bool bad = false;
#pragma unroll
                for (int i = 0; i < 3; ++i) bad |= (g[i].z != tlo) | (g[i].w != thi);
                if (!anyl(bad)) break;
            }
            STAMP(2);
            LAS float* qs = (LAS float*)(L + w * 512);
#pragma unroll
            for (int i = 0; i < 3; ++i) {
                float s = rs32(__uint_as_float(g[i].x), __uint_as_float(g[i].y));
                s = rs16(s, s);
                qs[2 * (16 * i + (lane & 15)) + (lane >> 5)] = s;
            }
            LGKM0();
#pragma unroll
            for (int ki = 0; ki < 3; ++ki) {
                const f32x4 u0 = *(LAS f32x4*)(qs + ki * 32 + 8 * (lane >> 4)), u1 = *(LAS f32x4*)(qs + ki * 32 + 8 * (lane >> 4) + 4);
                qv[ki * 8 + 0] = u0.x; qv[ki * 8 + 1] = u0.y; qv[ki * 8 + 2] = u0.z; qv[ki * 8 + 3] = u0.w;
                qv[ki * 8 + 4] = u1.x; qv[ki * 8 + 5] = u1.y; qv[ki * 8 + 6] = u1.z; qv[ki * 8 + 7] = u1.w;
            }
        }
        {
            float acc[3];
#pragma unroll
            for (int ti = 0; ti < 3; ++ti) {
                float s = 0.f;
#pragma unroll
                for (int ki = 0; ki < 3; ++ki) {
                    float f[8]; unp8(aw[ti * 3 + ki], f);
#pragma unroll
                    for (int j = 0; j < 8; ++j) s += f[j] * qv[ki * 8 + j];
                }
                s = rs16(s, s); s = rs32(s, s); acc[ti] = s;
            }
            const int row = lane >> 4;
            const float v = row == 0 ? acc[0] : (row == 1 ? acc[1] : acc[2]);
            if (row < 3) red[hf * 192 + tg * 48 + row * 16 + (lane & 15)] = v;
            BAR();
            if (w == 0 && lane < 48) {
                const int ch = lane < 24 ? lane : lane - 24;
                const float sc = 0.07216878364870322f * 1.4426950408889634f;
                float v8[8];
#pragma unroll
                for (int i = 0; i < 2; ++i) {
                    const f32x4 p0 = *(LAS f32x4*)(red + ch * 8 + i * 4), p1 = *(LAS f32x4*)(red + 192 + ch * 8 + i * 4);
                    v8[i * 4 + 0] = (p0.x + p1.x) * sc; v8[i * 4 + 1] = (p0.y + p1.y) * sc; v8[i * 4 + 2] = (p0.z + p1.z) * sc; v8[i * 4 + 3] = (p0.w + p1.w) * sc;
                }
                u32 o[4];
#pragma unroll
                for (int i = 0; i < 4; ++i) {
                    const u32 hi = cvtpk(v8[2 * i], v8[2 * i + 1]);
                    const u32 lo = cvtpk(v8[2 * i] - bflo(hi), v8[2 * i + 1] - bfhi(hi));
                    o[i] = canon(lane < 24 ? hi : lo);
                }
                st4(rws, OFF_E3 + (h * 4 + cq) * 768 + lane * 16, u32x4{o[0], o[1], o[2], o[3]});
            }
        }
        STAMP(3);
        const rsrc_t ruv = mkrs(wuv, DC * 6144 * 2);
        u32x3 vw[12];
        {
            const u32 base = (u32)(((cq * 192 + w * 24 + (lane >> 5)) * 32 + h) * 192 + 6 * (lane & 31)) * 2;
#pragma unroll
            for (int j = 0; j < 12; ++j) vw[j] = ld3(ruv, base + j * 2 * 12288, AUX_NT);
            SB();
        }
        if (xi >= 48) {
            const int gi = xi - 48, slice = gi % 40, rh = gi / 40;
            const int chunk = t & 7, rsub = t >> 3;
            const int row0 = rh * 320 + rsub;
            u16 cv[5]; u32x4 gw[5];
#pragma unroll
            for (int j = 0; j < 5; ++j) cv[j] = cc[row0 + 64 * j];
            const u32 off = (u32)(row0 * 3840 + 1280 + 64 * slice + 8 * chunk) * 2;
#pragma unroll
            for (int j = 0; j < 5; ++j) gw[j] = ld4(rmod, off + j * 64 * 7680, AUX_NT);
            SB();
            float a[8];
#pragma unroll
            for (int i = 0; i < 8; ++i) a[i] = 0.f;
#pragma unroll
            for (int j = 0; j < 5; ++j) {
                float f[8]; unp8(gw[j], f); const float s = silu(bf1(cv[j]));
#pragma unroll
                for (int i = 0; i < 8; ++i) a[i] += s * f[i];
            }
            const float v = rscat8(a, lane);
            const int e = ((lane >> 5) & 1) * 4 + ((lane >> 4) & 1) * 2 + ((lane >> 3) & 1);
            LAS float* redg = red + 512;
            redg[w * 64 + (lane & 7) * 8 + e] = v;
            BAR();
            if (w == 0 && lane < 16) {
                f32x4 s = {0.f, 0.f, 0.f, 0.f};
#pragma unroll
                for (int i = 0; i < 8; ++i) s += *(LAS f32x4*)(redg + i * 64 + lane * 4);
                st4(rws, OFF_G + (rh * 2560 + slice * 64) * 4 + lane * 16, u32x4{canonf(s.x), canonf(s.y), canonf(s.z), canonf(s.w)});
            }
        }
        {
            if (w < 4) {
                for (int it = 0; it < SPIN; ++it) { CFENCE();
                    const u32x4 g = ld4(rws, OFF_PF + t * 16, AUX_SC1);
                    if (!anyl((g.z != tlo) | (g.w != thi))) break;
                }
            }
            BAR();
        }
        STAMP(4);
        {
            u32x4 ml;
            for (int it = 0; it < SPIN; ++it) { CFENCE();
                ml = ld4(rws, OFF_ML + h * 1024 + lane * 16, AUX_SC1);
                if (!anyl(pois4(ml))) break;
            }
            STAMP(5);
            u32x3 ov[8];
            const u32 obase = OFF_O + (u32)((h * 4 + cq) * 128 + w * 16 + (lane >> 5)) * 384 + (lane & 31) * 12;
#pragma unroll
            for (int j = 0; j < 8; ++j) ov[j] = ld3(rws, obase + j * 768, AUX_SC1);
            SB();
            const float m0 = __uint_as_float(ml.x), l0 = __uint_as_float(ml.y), m1 = __uint_as_float(ml.z), l1 = __uint_as_float(ml.w);
            const float M = wave_max(fmaxf(m0, m1));
            const float w0 = __builtin_amdgcn_exp2f(m0 - M), w1_ = __builtin_amdgcn_exp2f(m1 - M);
            const float Lsum = wave_sum(w0 * l0 + w1_ * l1);
            const float inv = 1.f / Lsum;
            for (int it = 0; it < SPIN; ++it) { CFENCE();
                u32 bad = 0;
#pragma unroll
                for (int j = 0; j < 8; ++j) if (anyl(pois3(ov[j]))) bad |= 1u << j;
                if (!bad) break;
#pragma unroll
                for (int j = 0; j < 8; ++j) if (bad & (1u << j)) ov[j] = ld3(rws, obase + j * 768, AUX_SC1);
            }
            STAMP(6);
            float a[6];
#pragma unroll
            for (int i = 0; i < 6; ++i) a[i] = 0.f;
#pragma unroll
            for (int j = 0; j < 8; ++j) {
                const float wa = __uint_as_float(__builtin_amdgcn_readlane(__float_as_uint(w0), w * 8 + j));
                const float wb = __uint_as_float(__builtin_amdgcn_readlane(__float_as_uint(w1_), w * 8 + j));
                const float ww = (lane >> 5) ? wb : wa;
                float f[6]; unp6(ov[j], f);
#pragma unroll
                for (int i = 0; i < 6; ++i) a[i] += ww * f[i];
            }
#pragma unroll
            for (int i = 0; i < 3; ++i) red[w * 192 + 6 * (lane & 31) + 3 * (lane >> 5) + i] = rs32(a[i], a[i + 3]) * inv;
            BAR();
            LAS float* cl = (LAS float*)(L + w * 128);
            if (lane < 24) {
                float s = 0.f;
#pragma unroll
                for (int i = 0; i < 8; ++i) s += red[i * 192 + w * 24 + lane];
                cl[(lane & 1) * 12 + (lane >> 1)] = s;
            }
            LGKM0();
            float cv[12];
#pragma unroll
            for (int i = 0; i < 3; ++i) { const f32x4 u = *(LAS f32x4*)(cl + (lane >> 5) * 12 + i * 4); cv[i * 4] = u.x; cv[i * 4 + 1] = u.y; cv[i * 4 + 2] = u.z; cv[i * 4 + 3] = u.w; }
#pragma unroll
            for (int i = 0; i < 6; ++i) a[i] = 0.f;
#pragma unroll
            for (int j = 0; j < 12; ++j) {
                float f[6]; unp6(vw[j], f);
#pragma unroll
                for (int i = 0; i < 6; ++i) a[i] += cv[j] * f[i];
            }
            BAR();
#pragma unroll
            for (int i = 0; i < 3; ++i) red[w * 192 + 6 * (lane & 31) + 3 * (lane >> 5) + i] = rs32(a[i], a[i + 3]);
            BAR();
            if (w == 0 && lane < 48) {
                f32x4 s = {0.f, 0.f, 0.f, 0.f};
#pragma unroll
                for (int i = 0; i < 8; ++i) s += *(LAS f32x4*)(red + i * 192 + lane * 4);
                st4(rws, OFF_E5 + (cq * 6144 + h * 192) * 4 + lane * 16, u32x4{canonf(s.x), canonf(s.y), canonf(s.z), canonf(s.w)});
            }
        }
        STAMP(7);
        if (xi < 48) {
            const rsrc_t r1 = mkrs(w1, DM * DFF * 2), r2 = mkrs(w2, DFF * DM * 2);
            u32x4 f1[5], f2a[4]; u32 f2b[4];
            {
                const u32 b1 = (u32)((w * 80 + (lane >> 2)) * DFF + 32 * xi + 8 * (lane & 3)) * 2;
#pragma unroll
                for (int j = 0; j < 5; ++j) f1[j] = ld4(r1, b1 + j * 16 * DFF * 2, AUX_NT);
                const u32 b2 = (u32)((32 * xi + 4 * w) * DM) * 2;
#pragma unroll
                for (int j = 0; j < 4; ++j) { f2a[j] = ld4(r2, b2 + j * 1280 + lane * 16, AUX_NT); f2b[j] = __builtin_amdgcn_raw_buffer_load_b32(r2, b2 + j * 1280 + 1024 + lane * 4, 0, AUX_NT); }
                SB();
            }
            LAS float* gs = (LAS float*)(L + 4096);
            LAS float* xs = (LAS float*)(L + 4096 + 10240);
            LAS float* ap = (LAS float*)(L + 4096 + 10240 + 2560);
            LAS float* hs = (LAS float*)(L + 4096 + 10240 + 2560 + 7680);
            {
                u32x4 g0, g1, g2, g3;
                const int i0 = t, i1 = t + 512;
                for (int it = 0; it < SPIN; ++it) { CFENCE();
                    g0 = ld4(rws, OFF_G + i0 * 16, AUX_SC1); g1 = ld4(rws, OFF_G + 10240 + i0 * 16, AUX_SC1);
                    bool bad = pois4(g0) | pois4(g1);
                    if (t < 128) { g2 = ld4(rws, OFF_G + i1 * 16, AUX_SC1); g3 = ld4(rws, OFF_G + 10240 + i1 * 16, AUX_SC1); bad |= pois4(g2) | pois4(g3); }
                    if (!anyl(bad)) break;
                }
                *(LAS f32x4*)(gs + i0 * 4) = __builtin_bit_cast(f32x4, g0) + __builtin_bit_cast(f32x4, g1);
                if (t < 128) *(LAS f32x4*)(gs + i1 * 4) = __builtin_bit_cast(f32x4, g2) + __builtin_bit_cast(f32x4, g3);
                if (t < 320) { const u32 xx = ((const u32*)x)[t]; xs[2 * t] = bflo(xx); xs[2 * t + 1] = bfhi(xx); }
            }
            STAMP(8);
            {
                const int part = t / 160, i = t % 160;
                if (t < 480) {
                    u32x4 g[4];
                    const u32 goff = OFF_E6 + (part * 4 * 640 + i * 4) * 4;
                    for (int it = 0; it < SPIN; ++it) { CFENCE();
#pragma unroll
                        for (int j = 0; j < 4; ++j) g[j] = ld4(rws, goff + j * 2560, AUX_SC1);
                        if (!anyl(pois4(g[0]) | pois4(g[1]) | pois4(g[2]) | pois4(g[3]))) break;
                    }
                    *(LAS f32x4*)(ap + part * 640 + i * 4) = (__builtin_bit_cast(f32x4, g[0]) + __builtin_bit_cast(f32x4, g[1])) + (__builtin_bit_cast(f32x4, g[2]) + __builtin_bit_cast(f32x4, g[3]));
                }
                BAR();
                STAMP(9);
                float x1[10];
#pragma unroll
                for (int i2 = 0; i2 < 10; ++i2) { const int m = lane + 64 * i2; x1[i2] = xs[m] + gs[m] * ((ap[m] + ap[640 + m]) + ap[1280 + m]); }
                float mean, rstd; ln_stats(x1, mean, rstd);
                BAR();
                if (w == 0) {
#pragma unroll
                    for (int i2 = 0; i2 < 10; ++i2) xs[lane + 64 * i2] = x1[i2];
                }
                if (w == 1) {
#pragma unroll
                    for (int i2 = 0; i2 < 10; ++i2) { const int m = lane + 64 * i2; hs[m] = (x1[i2] - mean) * rstd * (1.f + gs[1280 + m]) + gs[640 + m]; }
                }
                BAR();
                float a[8];
#pragma unroll
                for (int i2 = 0; i2 < 8; ++i2) a[i2] = 0.f;
#pragma unroll
                for (int j = 0; j < 5; ++j) {
                    float f[8]; unp8(f1[j], f); const float hv = hs[w * 80 + j * 16 + (lane >> 2)];
#pragma unroll
                    for (int i2 = 0; i2 < 8; ++i2) a[i2] += hv * f[i2];
                }
                float v = rscat8(a, lane);
                v += dpp<0x104>(v);
                const int e = ((lane >> 5) & 1) * 4 + ((lane >> 4) & 1) * 2 + ((lane >> 3) & 1);
                if (!(lane & 4)) red[w * 32 + (lane & 3) * 8 + e] = v;
                BAR();
                float hid[4];
#pragma unroll
                for (int j = 0; j < 4; ++j) {
                    float s = 0.f;
#pragma unroll
                    for (int i2 = 0; i2 < 8; ++i2) s += red[i2 * 32 + 4 * w + j];
                    hid[j] = fmaxf(s, 0.f);
                }
                float b8[8], b2[2];
#pragma unroll
                for (int i2 = 0; i2 < 8; ++i2) b8[i2] = 0.f;
                b2[0] = b2[1] = 0.f;
#pragma unroll
                for (int j = 0; j < 4; ++j) {
                    float f[8]; unp8(f2a[j], f);
#pragma unroll
                    for (int i2 = 0; i2 < 8; ++i2) b8[i2] += hid[j] * f[i2];
                    b2[0] += hid[j] * bflo(f2b[j]); b2[1] += hid[j] * bfhi(f2b[j]);
                }
                LAS float* fr = (LAS float*)(L + 32768);
                *(LAS f32x4*)(fr + w * 640 + lane * 8) = f32x4{b8[0], b8[1], b8[2], b8[3]};
                *(LAS f32x4*)(fr + w * 640 + lane * 8 + 4) = f32x4{b8[4], b8[5], b8[6], b8[7]};
                *(LAS f32x2*)(fr + w * 640 + 512 + lane * 2) = f32x2{b2[0], b2[1]};
                BAR();
                if (t < 160) {
                    f32x4 s = {0.f, 0.f, 0.f, 0.f};
#pragma unroll
                    for (int i2 = 0; i2 < 8; ++i2) s += *(LAS f32x4*)(fr + i2 * 640 + t * 4);
                    st4(rws, OFF_E7 + xi * 2560 + t * 16, u32x4{canonf(s.x), canonf(s.y), canonf(s.z), canonf(s.w)});
                }
            }
            STAMP(10);
            if (xi < 40 && w < 3) {
                u32x4 g;
                const u32 goff = OFF_E7 + (t >> 2) * 2560 + (16 * xi + 4 * (t & 3)) * 4;
                for (int it = 0; it < SPIN; ++it) { CFENCE();
                    g = ld4(rws, goff, AUX_SC1);
                    if (!anyl(pois4(g))) break;
                }
                f32x4 s = __builtin_bit_cast(f32x4, g);
#pragma unroll
                for (int i = 0; i < 4; ++i) {
                    float v = s[i];
                    v += dpp<0x104>(v);
                    v += dpp<0x128>(v);
                    v = rs16(v, v); v = rs32(v, v);
                    s[i] = v;
                }
                if (lane < 4) *(LAS f32x4*)(red + 512 + w * 16 + lane * 4) = s;
            }
            if (xi < 40) {
                BAR();
                if (t < 8) {
                    const int m = 16 * xi + 2 * t;
                    const float s0 = (red[512 + 2 * t] + red[512 + 16 + 2 * t]) + red[512 + 32 + 2 * t];
                    const float s1 = (red[512 + 2 * t + 1] + red[512 + 16 + 2 * t + 1]) + red[512 + 32 + 2 * t + 1];
                    ((u32*)out)[m >> 1] = cvtpk(xs[m] + gs[1920 + m] * s0, xs[m + 1] + gs[1920 + m + 1] * s1);
                }
                if (xi == 0 && t == 0) __hip_atomic_store((GAS u64*)(ws + OFF_EPOCH), epoch + 1ull, __ATOMIC_RELAXED, __HIP_MEMORY_SCOPE_AGENT);
            }
            STAMP(11);
        }
    } else {
        const int yi = bid - 128, h = yi & 31, mg = yi >> 5;
        const rsrc_t rq = mkrs(wq, DM * 6144 * 2);
        u32x3 qw[10];
        {
            const u32 base = (u32)(((mg * 160 + w * 20 + (lane >> 5)) * 32 + h) * 192 + 6 * (lane & 31)) * 2;
#pragma unroll
            for (int j = 0; j < 10; ++j) qw[j] = ld3(rq, base + j * 2 * 12288, AUX_NT);
            SB();
        }
        {
            const int seg0 = w * 16;
#pragma unroll
            for (int j = 0; j < 6; ++j) {
                const int idx = j * 64 + lane, seg = seg0 + idx / 24, ch = idx % 24;
                st4(rws, OFF_O + (u32)(seg * 128 + yi) * 384 + ch * 16, PZ);
            }
            if (w == 0 && lane < 32) st2(rws, OFF_ML + lane * 1024 + yi * 8, u32x2{POISON, POISON});
            if (w == 1 && lane < 16 && yi < 120) st4(rws, OFF_E6 + ((yi % 12) * 640 + (yi / 12) * 64) * 4 + lane * 16, PZ);
            SB();
        }
        const u64 tag = (epoch + 1ull) * TAGMUL;
        const u32 tlo = (u32)tag, thi = (u32)(tag >> 32);
        {
            const int rg = lane >> 4, g = lane & 15;
            const bool act = g < 14;
            const u32 goff = g < 10 ? OFF_E1 + (rg * 320 + mg * 80 + w * 10 + g) * 16 : OFF_PF + (w * 16 + rg * 4 + (g - 10)) * 16;
            u32x4 gv = {0u, 0u, tlo, thi};
            for (int it = 0; it < SPIN; ++it) { CFENCE();
                if (act) gv = ld4(rws, goff, AUX_SC1);
                if (!anyl((gv.z != tlo) | (gv.w != thi))) break;
            }
            STAMP(1);
            float s = rs32(__uint_as_float(gv.x), __uint_as_float(gv.y));
            s = rs16(s, s);
            float a[6];
#pragma unroll
            for (int i = 0; i < 6; ++i) a[i] = 0.f;
#define QSTEP(j) { const float hv = dpp<0x150 + j>(s); float f[6]; unp6(qw[j], f); _Pragma("unroll") for (int i = 0; i < 6; ++i) a[i] += hv * f[i]; }
            QSTEP(0) QSTEP(1) QSTEP(2) QSTEP(3) QSTEP(4) QSTEP(5) QSTEP(6) QSTEP(7) QSTEP(8) QSTEP(9)
#undef QSTEP
#pragma unroll
            for (int i = 0; i < 3; ++i) red[w * 192 + 6 * (lane & 31) + 3 * (lane >> 5) + i] = rs32(a[i], a[i + 3]);
            DRAIN();
            BAR();
            if (t < 96) {
                f32x2 s2 = {0.f, 0.f};
#pragma unroll
                for (int i = 0; i < 8; ++i) s2 += *(LAS f32x2*)(red + i * 192 + t * 2);
                st4(rws, OFF_E2 + ((mg * 32 + h) * 96 + t) * 16, u32x4{__float_as_uint(s2.x), __float_as_uint(s2.y), tlo, thi});
            }
            if (t == 128) st4(rws, OFF_PF + bid * 16, u32x4{0u, 0u, tlo, thi});
        }
        STAMP(2);
        {
            const unsigned char* src = (const unsigned char*)ckv + (size_t)yi * 64 * 1536;
#pragma unroll
            for (int j = 0; j < 12; ++j) {
                const int idx = (w * 12 + j) * 64 + lane, row = idx / 96, p = idx % 96;
                const int ch = (p & ~15) | ((p ^ row) & 15);
                __builtin_amdgcn_global_load_lds((const GAS u32*)(src + row * 1536 + ch * 16), (LAS u32*)(L + L_KV + (w * 12 + j) * 1024), 16, 0, AUX_NT);
            }
            SB();
        }
        const int kq = w >> 1, ht = w & 1;
        u32x4 qf[12];
        {
            const u32 qoff = OFF_E3 + (u32)((ht * 16 + (lane & 15)) * 4 + kq) * 768 + (lane >> 4) * 16;
            for (int it = 0; it < SPIN; ++it) { CFENCE();
                qf[0] = ld4(rws, qoff, AUX_SC1);
                if (!anyl(pois4(qf[0]))) break;
            }
            STAMP(3);
#pragma unroll
            for (int j = 1; j < 12; ++j) qf[j] = ld4(rws, qoff + (j % 6) * 64 + (j / 6) * 384, AUX_SC1);
            SB();
            for (int it = 0; it < SPIN; ++it) { CFENCE();
                u32 bad = 0;
#pragma unroll
                for (int j = 1; j < 12; ++j) if (anyl(pois4(qf[j]))) bad |= 1u << j;
                if (!bad) break;
#pragma unroll
                for (int j = 1; j < 12; ++j) if (bad & (1u << j)) qf[j] = ld4(rws, qoff + (j % 6) * 64 + (j / 6) * 384, AUX_SC1);
            }
        }
        DRAIN();
        BAR();
        STAMP(4);
        {
            const int r = lane & 15;
            const int keyr = 8 * (r >> 3) + 2 * (r & 3) + ((r >> 2) & 1);
            f32x4 acc[4];
#pragma unroll
            for (int kt = 0; kt < 4; ++kt) acc[kt] = f32x4{0.f, 0.f, 0.f, 0.f};
#pragma unroll
            for (int ks = 0; ks < 6; ++ks) {
                const int ch = kq * 24 + ks * 4 + (lane >> 4);
                const int p = (ch & ~15) | ((ch ^ keyr) & 15);
#pragma unroll
                for (int kt = 0; kt < 4; ++kt) {
                    const u32x4 a = *(LAS u32x4*)(L + L_KV + (kt * 16 + keyr) * 1536 + p * 16);
                    acc[kt] = mfma(a, qf[ks], acc[kt]);
                    acc[kt] = mfma(a, qf[6 + ks], acc[kt]);
                }
            }
            LAS float* sp = (LAS float*)(L + L_SP);
#pragma unroll
            for (int kt = 0; kt < 4; ++kt) *(LAS f32x4*)(sp + (kq * 32 + ht * 16 + (lane & 15)) * 68 + kt * 16 + 4 * (lane >> 4)) = acc[kt];
        }
        BAR();
        {
            LAS float* sp = (LAS float*)(L + L_SP);
            const int hd = t >> 4, qd = t & 15;
            f32x4 s = *(LAS f32x4*)(sp + hd * 68 + qd * 4);
#pragma unroll
            for (int k = 1; k < 4; ++k) s += *(LAS f32x4*)(sp + (k * 32 + hd) * 68 + qd * 4);
            const float m = row_max(fmaxf(fmaxf(s.x, s.y), fmaxf(s.z, s.w)));
            const float p0 = __builtin_amdgcn_exp2f(s.x - m), p1 = __builtin_amdgcn_exp2f(s.y - m), p2 = __builtin_amdgcn_exp2f(s.z - m), p3 = __builtin_amdgcn_exp2f(s.w - m);
            const u32 b01 = cvtpk(p0, p1), b23 = cvtpk(p2, p3);
            const float l = row_sum((bflo(b01) + bfhi(b01)) + (bflo(b23) + bfhi(b23)));
            *(LAS u32x2*)(L + L_P + hd * 144 + qd * 8) = u32x2{b01, b23};
            if (qd == 0) st2(rws, OFF_ML + hd * 1024 + yi * 8, u32x2{canonf(m), canonf(l)});
        }
        BAR();
        STAMP(5);
        {
            u32x4 pb[2][2];
#pragma unroll
            for (int hh = 0; hh < 2; ++hh)
#pragma unroll
                for (int k2 = 0; k2 < 2; ++k2) pb[hh][k2] = *(LAS u32x4*)(L + L_P + (hh * 16 + (lane & 15)) * 144 + k2 * 64 + (lane >> 4) * 16);
            f32x4 acc[6][2];
#pragma unroll
            for (int ct = 0; ct < 6; ++ct) { acc[ct][0] = f32x4{0.f, 0.f, 0.f, 0.f}; acc[ct][1] = f32x4{0.f, 0.f, 0.f, 0.f}; }
            const int g = lane >> 4, e = (lane >> 2) & 3, pp = lane & 3;
#pragma unroll
            for (int ct = 0; ct < 6; ++ct) {
                const int ch = w * 12 + ct * 2 + (pp >> 1);
#pragma unroll
                for (int k2 = 0; k2 < 2; ++k2) {
                    const int ka = k2 * 32 + 8 * g + 2 * e, kb = ka + 1;
                    const int pa = (ch & ~15) | ((ch ^ ka) & 15), pbx = (ch & ~15) | ((ch ^ kb) & 15);
                    const s16x4 t0 = __builtin_amdgcn_ds_read_tr16_b64_v4i16((LAS s16x4*)(L + L_KV + ka * 1536 + pa * 16 + (pp & 1) * 8));
                    const s16x4 t1 = __builtin_amdgcn_ds_read_tr16_b64_v4i16((LAS s16x4*)(L + L_KV + kb * 1536 + pbx * 16 + (pp & 1) * 8));
                    const u32x2 a0 = __builtin_bit_cast(u32x2, t0), a1 = __builtin_bit_cast(u32x2, t1);
                    const u32x4 a = {a0.x, a0.y, a1.x, a1.y};
                    acc[ct][0] = mfma(a, pb[0][k2], acc[ct][0]);
                    acc[ct][1] = mfma(a, pb[1][k2], acc[ct][1]);
                }
            }
            const int ocq = w >> 1;
#pragma unroll
            for (int hh = 0; hh < 2; ++hh)
#pragma unroll
                for (int cp = 0; cp < 3; ++cp) {
                    const f32x4 A = acc[2 * cp][hh], B = acc[2 * cp + 1][hh];
                    const u32 ax = cvtpk(A.x, A.y), ay = cvtpk(A.z, A.w), bx = cvtpk(B.x, B.y), by = cvtpk(B.z, B.w);
                    auto r0 = __builtin_amdgcn_permlane16_swap(ax, bx, false, false);
                    auto r1 = __builtin_amdgcn_permlane16_swap(ay, by, false, false);
                    const u32x4 v = {canon(r0[0]), canon(r1[0]), canon(r0[1]), canon(r1[1])};
                    const int c = (w & 1) * 96 + (2 * cp + (g & 1)) * 16 + (g >> 1) * 8;
                    st4(rws, OFF_O + (u32)(((hh * 16 + (lane & 15)) * 4 + ocq) * 128 + yi) * 384 + c * 2, v);
                }
        }
        STAMP(6);
        if (yi < 120) {
            const int rg = yi % 12, cs = yi / 12;
            const rsrc_t ro = mkrs(wo, 6144 * DM * 2);
            u32x4 ow[8];
            const u32 base = (u32)((rg * 512 + w * 64 + (lane >> 3) * 8) * DM + cs * 64 + (lane & 7) * 8) * 2;
#pragma unroll
            for (int j = 0; j < 8; ++j) ow[j] = ld4(ro, base + j * 1280, AUX_NT);
            SB();
            u32x4 g;
            const u32 goff = OFF_E5 + ((lane >> 4) * 6144 + rg * 512 + w * 64 + (lane & 15) * 4) * 4;
            for (int it = 0; it < SPIN; ++it) { CFENCE();
                g = ld4(rws, goff, AUX_SC1);
                if (!anyl(pois4(g))) break;
            }
            STAMP(7);
            f32x4 s = __builtin_bit_cast(f32x4, g);
#pragma unroll
            for (int i = 0; i < 4; ++i) { float v = s[i]; v = rs16(v, v); v = rs32(v, v); s[i] = v; }
            LAS float* cs_ = (LAS float*)(L + L_SP + w * 256);
            if (lane < 16) *(LAS f32x4*)(cs_ + lane * 4) = s;
            LGKM0();
            const f32x4 c0 = *(LAS f32x4*)(cs_ + (lane >> 3) * 8), c1 = *(LAS f32x4*)(cs_ + (lane >> 3) * 8 + 4);
            const float cv[8] = {c0.x, c0.y, c0.z, c0.w, c1.x, c1.y, c1.z, c1.w};
            float a[8];
#pragma unroll
            for (int i = 0; i < 8; ++i) a[i] = 0.f;
#pragma unroll
            for (int j = 0; j < 8; ++j) {
                float f[8]; unp8(ow[j], f);
#pragma unroll
                for (int i = 0; i < 8; ++i) a[i] += cv[j] * f[i];
            }
            const float v = rscat8(a, lane);
            const int e = ((lane >> 5) & 1) * 4 + ((lane >> 4) & 1) * 2 + ((lane >> 3) & 1);
            red[w * 64 + (lane & 7) * 8 + e] = v;
            BAR();
            if (w == 0 && lane < 16) {
                f32x4 s4 = {0.f, 0.f, 0.f, 0.f};
#pragma unroll
                for (int i = 0; i < 8; ++i) s4 += *(LAS f32x4*)(red + i * 64 + lane * 4);
                st4(rws, OFF_E6 + (rg * 640 + cs * 64) * 4 + lane * 16, u32x4{canonf(s4.x), canonf(s4.y), canonf(s4.z), canonf(s4.w)});
            }
            STAMP(8);
        }
    }
}

extern "C" void kernel_launch(void* const* d_in, const int* in_sizes, int n_in, void* d_out, int out_size, void* d_ws, size_t ws_size, hipStream_t stream) {
    (void)in_sizes; (void)n_in; (void)out_size; (void)ws_size;
    hipFuncSetAttribute((const void*)adaln_mla_mlp_kernel, hipFuncAttributeMaxDynamicSharedMemorySize, LDS_BYTES);
    adaln_mla_mlp_kernel<<<NWG, NT, LDS_BYTES, stream>>>(
        (const u16*)d_in[0], (const u16*)d_in[1], (const u16*)d_in[2], (const u16*)d_in[3], (const u16*)d_in[4], (const u16*)d_in[5],
        (const u16*)d_in[7], (const u16*)d_in[8], (const u16*)d_in[9], (const u16*)d_in[10], (u16*)d_out, (unsigned char*)d_ws);
}
```

```cpp
#define AMP_WG 0
#define AMP_FROM 9
#define AMP_TO 10
#define AMP_K 4
#include <hip/hip_runtime.h>
#include <stdint.h>

typedef unsigned u32;
typedef unsigned long long u64;
typedef unsigned short u16;
typedef u32 u32x2 __attribute__((ext_vector_type(2)));
typedef u32 u32x3 __attribute__((ext_vector_type(3)));
typedef u32 u32x4 __attribute__((ext_vector_type(4)));
typedef float f32x2 __attribute__((ext_vector_type(2)));
typedef float f32x4 __attribute__((ext_vector_type(4)));
typedef short bf16x8 __attribute__((ext_vector_type(8)));
typedef short s16x4 __attribute__((ext_vector_type(4)));
typedef __amdgpu_buffer_rsrc_t rsrc_t;
#define LAS __attribute__((address_space(3)))
#define GAS __attribute__((address_space(1)))

constexpr int NH = 32, SK = 8192, HD = 192, DM = 640, DC = 768, DFF = 1536;
constexpr int NWG = 256, NT = 512;

constexpr u32 OFF_EPOCH = 0;
constexpr u32 OFF_PF = 256;
constexpr u32 OFF_E1 = OFF_PF + 256 * 16;
constexpr u32 OFF_E2 = OFF_E1 + 4 * 320 * 16;
constexpr u32 OFF_E3 = OFF_E2 + 4 * 32 * 96 * 16;
constexpr u32 OFF_ML = OFF_E3 + 32 * 4 * 768;
constexpr u32 OFF_E5 = OFF_ML + 32 * 128 * 8;
constexpr u32 OFF_E6 = OFF_E5 + 4 * 6144 * 4;
constexpr u32 OFF_G = OFF_E6 + 12 * 640 * 4;
constexpr u32 OFF_E7 = OFF_G + 2 * 2560 * 4;
constexpr u32 OFF_O = OFF_E7 + 48 * 640 * 4;
constexpr u32 WS_BYTES = OFF_O + 32 * 4 * 128 * 384;
constexpr u32 OFF_DIAG = WS_BYTES;
static_assert(OFF_O % 128 == 0 && OFF_E3 % 128 == 0 && OFF_E5 % 128 == 0, "alignment");

constexpr int L_KV = 0;
constexpr int L_SP = 98304;
constexpr int L_P = L_SP + 4 * 32 * 68 * 4;
constexpr int L_RED = L_P + 32 * 144;
constexpr int LDS_BYTES = L_RED + 8192;
constexpr int SPIN = 1 << 16;
constexpr u32 POISON = 0xFFFFFFFFu;
constexpr int AUX_SC1 = 16, AUX_NT = 2;
constexpr u64 TAGMUL = 0x9E3779B97F4A7C15ull;

__device__ __forceinline__ float bflo(u32 v) { return __uint_as_float(v << 16); }
__device__ __forceinline__ float bfhi(u32 v) { return __uint_as_float(v & 0xFFFF0000u); }
__device__ __forceinline__ float bf1(u16 v) { return __uint_as_float((u32)v << 16); }
typedef __bf16 bf16x2_t __attribute__((ext_vector_type(2)));
__device__ __forceinline__ u32 cvtpk(float lo, float hi) { f32x2 v = {lo, hi}; return __builtin_bit_cast(u32, __builtin_convertvector(v, bf16x2_t)); }
__device__ __forceinline__ u32 canon(u32 v) { return v < 0xFFFFFFFEu ? v : 0xFFFFFFFEu; }
__device__ __forceinline__ u32 canonf(float v) { return canon(__float_as_uint(v)); }
__device__ __forceinline__ rsrc_t mkrs(const void* p, u32 bytes) { return __builtin_amdgcn_make_buffer_rsrc((void*)p, 0, bytes, 0x00020000); }
__device__ __forceinline__ u32x4 ld4(rsrc_t r, u32 off, int aux) { return aux == AUX_SC1 ? __builtin_amdgcn_raw_buffer_load_b128(r, off, 0, AUX_SC1) : __builtin_amdgcn_raw_buffer_load_b128(r, off, 0, AUX_NT); }
__device__ __forceinline__ u32x3 ld3(rsrc_t r, u32 off, int aux) { return aux == AUX_SC1 ? __builtin_amdgcn_raw_buffer_load_b96(r, off, 0, AUX_SC1) : __builtin_amdgcn_raw_buffer_load_b96(r, off, 0, AUX_NT); }
__device__ __forceinline__ void st4(rsrc_t r, u32 off, u32x4 v) { __builtin_amdgcn_raw_buffer_store_b128(v, r, off, 0, AUX_SC1); }
__device__ __forceinline__ void st2(rsrc_t r, u32 off, u32x2 v) { __builtin_amdgcn_raw_buffer_store_b64(v, r, off, 0, AUX_SC1); }
#define CFENCE() asm volatile("" ::: "memory")
#define SB() __builtin_amdgcn_sched_barrier(0)
#define BAR() asm volatile("s_waitcnt lgkmcnt(0)\n\ts_barrier" ::: "memory")
#define LGKM0() asm volatile("s_waitcnt lgkmcnt(0)" ::: "memory")
#define DRAIN() asm volatile("s_waitcnt vmcnt(0)" ::: "memory")

template <int CTRL> __device__ __forceinline__ float dpp(float x) { return __int_as_float(__builtin_amdgcn_update_dpp(0, __float_as_int(x), CTRL, 0xf, 0xf, true)); }
__device__ __forceinline__ float rs32(float a, float b) { auto r = __builtin_amdgcn_permlane32_swap(__float_as_uint(a), __float_as_uint(b), false, false); return __uint_as_float(r[0]) + __uint_as_float(r[1]); }
__device__ __forceinline__ float rs16(float a, float b) { auto r = __builtin_amdgcn_permlane16_swap(__float_as_uint(a), __float_as_uint(b), false, false); return __uint_as_float(r[0]) + __uint_as_float(r[1]); }
__device__ __forceinline__ float row_sum(float x) { x += dpp<0x128>(x); x += dpp<0x124>(x); x += dpp<0x122>(x); x += dpp<0x121>(x); return x; }
__device__ __forceinline__ float row_max(float x) { x = fmaxf(x, dpp<0x128>(x)); x = fmaxf(x, dpp<0x124>(x)); x = fmaxf(x, dpp<0x122>(x)); x = fmaxf(x, dpp<0x121>(x)); return x; }
__device__ __forceinline__ float wave_sum(float x) { x = row_sum(x); x = rs16(x, x); x = rs32(x, x); return x; }
__device__ __forceinline__ float wave_max(float x) {
    x = row_max(x);
    { auto r = __builtin_amdgcn_permlane16_swap(__float_as_uint(x), __float_as_uint(x), false, false); x = fmaxf(__uint_as_float(r[0]), __uint_as_float(r[1])); }
    { auto r = __builtin_amdgcn_permlane32_swap(__float_as_uint(x), __float_as_uint(x), false, false); x = fmaxf(__uint_as_float(r[0]), __uint_as_float(r[1])); }
    return x;
}
__device__ __forceinline__ float rscat8(const float (&a)[8], int lane) {
    float r0 = rs32(a[0], a[4]), r1 = rs32(a[1], a[5]), r2 = rs32(a[2], a[6]), r3 = rs32(a[3], a[7]);
    float s0 = rs16(r0, r2), s1 = rs16(r1, r3);
    float t0 = s0 + dpp<0x128>(s0), t1 = s1 + dpp<0x128>(s1);
    return (lane & 8) ? t1 : t0;
}
__device__ __forceinline__ bool pois4(u32x4 v) { u32 m = max(max(v.x, v.y), max(v.z, v.w)); return m == POISON; }
__device__ __forceinline__ bool pois3(u32x3 v) { u32 m = max(max(v.x, v.y), v.z); return m == POISON; }
__device__ __forceinline__ bool anyl(bool b) { return __builtin_amdgcn_ballot_w64(b) != 0ull; }
__device__ __forceinline__ f32x4 mfma(u32x4 a, u32x4 b, f32x4 c) { return __builtin_amdgcn_mfma_f32_16x16x32_bf16(__builtin_bit_cast(bf16x8, a), __builtin_bit_cast(bf16x8, b), c, 0, 0, 0); }
__device__ __forceinline__ float silu(float v) { return v / (1.f + __expf(-v)); }
__device__ __forceinline__ void unp8(u32x4 v, float (&f)[8]) { f[0] = bflo(v.x); f[1] = bfhi(v.x); f[2] = bflo(v.y); f[3] = bfhi(v.y); f[4] = bflo(v.z); f[5] = bfhi(v.z); f[6] = bflo(v.w); f[7] = bfhi(v.w); }
__device__ __forceinline__ void unp6(u32x3 v, float (&f)[6]) { f[0] = bflo(v.x); f[1] = bfhi(v.x); f[2] = bflo(v.y); f[3] = bfhi(v.y); f[4] = bflo(v.z); f[5] = bfhi(v.z); }

#define STAMP(i) do { st[i] = __builtin_amdgcn_s_memrealtime(); } while (0)

__device__ __forceinline__ void ln_stats(const float (&v)[10], float& mean, float& rstd) {
    float s = 0.f;
#pragma unroll
    for (int i = 0; i < 10; ++i) s += v[i];
    mean = wave_sum(s) * (1.f / 640.f);
    float q = 0.f;
#pragma unroll
    for (int i = 0; i < 10; ++i) { float d = v[i] - mean; q += d * d; }
    float var = wave_sum(q) * (1.f / 640.f);
    rstd = 1.f / sqrtf(var + 1e-5f);
}

__global__ __launch_bounds__(NT, 2) void adaln_mla_mlp_kernel(
    const u16* __restrict__ x, const u16* __restrict__ ckv, const u16* __restrict__ wq, const u16* __restrict__ wuk,
    const u16* __restrict__ wuv, const u16* __restrict__ wo, const u16* __restrict__ w1, const u16* __restrict__ w2,
    const u16* __restrict__ cc, const u16* __restrict__ wmod, u16* __restrict__ out, unsigned char* __restrict__ ws) {
    extern __shared__ __attribute__((aligned(16))) unsigned char smem[];
    LAS unsigned char* L = (LAS unsigned char*)smem;
    const int t = threadIdx.x, lane = t & 63, w = __builtin_amdgcn_readfirstlane(t >> 6);
    const int bid = blockIdx.x;
    const rsrc_t rws = mkrs(ws, WS_BYTES);
    u64 st[16];
#pragma unroll
    for (int i = 0; i < 16; ++i) st[i] = 0;
    STAMP(0);
    const u64 epoch = __hip_atomic_load((GAS u64*)(ws + OFF_EPOCH), __ATOMIC_RELAXED, __HIP_MEMORY_SCOPE_AGENT);
    const u32x4 PZ = {POISON, POISON, POISON, POISON};
    LAS float* red = (LAS float*)(L + L_RED);

    if (bid < 128) {
        const int xi = bid, h = xi & 31, cq = xi >> 5;
        const rsrc_t rmod = mkrs(wmod, DM * 3840 * 2);
        const bool isM = xi < 80;
        u32x4 mw0, mw1, mw2; u16 c0 = 0, c1 = 0, c2 = 0; u32 xr[5]; u16 xm = 0;
        int m_rg = 0, m_s = 0;
        if (isM) {
            const int x8 = xi & 7, b = (xi >> 3) & 1, slot = xi >> 4, qq = slot * 8 + x8;
            m_rg = qq / 10; m_s = 2 * (qq % 10) + b;
            const int chunk = t & 7, rsub = t >> 3;
            const int col = chunk < 4 ? 32 * m_s + 8 * chunk : 640 + 32 * m_s + 8 * (chunk - 4);
            const int row0 = m_rg * 160 + rsub;
            c0 = cc[row0]; c1 = cc[row0 + 64]; if (w < 4) c2 = cc[row0 + 128];
            if (w == 0) {
#pragma unroll
                for (int i = 0; i < 5; ++i) xr[i] = ((const u32*)x)[lane + 64 * i];
                xm = x[32 * m_s + (lane & 31)];
            }
            const u32 off = (u32)(row0 * 3840 + col) * 2;
            mw0 = ld4(rmod, off, AUX_NT); mw1 = ld4(rmod, off + 64 * 7680, AUX_NT);
            if (w < 4) mw2 = ld4(rmod, off + 128 * 7680, AUX_NT);
            SB();
        }
        if (w == 7) {
            if (lane < 48) { st4(rws, OFF_E3 + (h * 4 + cq) * 768 + lane * 16, PZ); st4(rws, OFF_E5 + (cq * 6144 + h * 192) * 4 + lane * 16, PZ); }
            if (xi < 48) {
                st4(rws, OFF_E7 + xi * 2560 + lane * 16, PZ); st4(rws, OFF_E7 + xi * 2560 + 1024 + lane * 16, PZ);
                if (lane < 32) st4(rws, OFF_E7 + xi * 2560 + 2048 + lane * 16, PZ);
            } else if (lane < 16) {
                const int gi = xi - 48;
                st4(rws, OFF_G + ((gi / 40) * 2560 + (gi % 40) * 64) * 4 + lane * 16, PZ);
            }
            SB();
        }
        const u64 tag = (epoch + 1ull) * TAGMUL;
        const u32 tlo = (u32)tag, thi = (u32)(tag >> 32);
        if (isM) {
            float a[8], f[8];
            const float s0 = silu(bf1(c0)), s1 = silu(bf1(c1));
            unp8(mw0, f);
#pragma unroll
            for (int i = 0; i < 8; ++i) a[i] = s0 * f[i];
            unp8(mw1, f);
#pragma unroll
            for (int i = 0; i < 8; ++i) a[i] += s1 * f[i];
            if (w < 4) {
                const float s2 = silu(bf1(c2));
                unp8(mw2, f);
#pragma unroll
                for (int i = 0; i < 8; ++i) a[i] += s2 * f[i];
            }
            const float v = rscat8(a, lane);
            const int e = ((lane >> 5) & 1) * 4 + ((lane >> 4) & 1) * 2 + ((lane >> 3) & 1);
            red[w * 64 + (lane & 7) * 8 + e] = v;
            float lnx = 0.f;
            if (w == 0) {
                float xv[10];
#pragma unroll
                for (int i = 0; i < 5; ++i) { xv[2 * i] = bflo(xr[i]); xv[2 * i + 1] = bfhi(xr[i]); }
                float mean, rstd; ln_stats(xv, mean, rstd);
                lnx = (bf1(xm) - mean) * rstd;
            }
            BAR();
            if (w == 0) {
                float s = 0.f;
#pragma unroll
                for (int i = 0; i < 8; ++i) s += red[i * 64 + lane];
                auto r = __builtin_amdgcn_permlane32_swap(__float_as_uint(s), __float_as_uint(s), false, false);
                const float sa = __uint_as_float(r[0]), ca = __uint_as_float(r[1]);
                float hv = lnx * ca + sa; if (m_rg == 0) hv += lnx;
                const float hn = dpp<0xB1>(hv);
                if (lane < 32 && !(lane & 1)) st4(rws, OFF_E1 + (m_rg * 320 + m_s * 16 + (lane >> 1)) * 16, u32x4{__float_as_uint(hv), __float_as_uint(hn), tlo, thi});
            }
        }
        STAMP(1);
        if (w == 7) { DRAIN(); if (lane == 0) st4(rws, OFF_PF + bid * 16, u32x4{0u, 0u, tlo, thi}); }
        const rsrc_t ruk = mkrs(wuk, DC * 6144 * 2);
        const int tg = w >> 1, hf = w & 1;
        u32x4 aw[9];
        {
            const u32 base = (u32)(((cq * 192 + tg * 48 + (lane & 15)) * 32 + h) * 192 + hf * 96 + 8 * (lane >> 4)) * 2;
#pragma unroll
            for (int ti = 0; ti < 3; ++ti)
#pragma unroll
                for (int ki = 0; ki < 3; ++ki) aw[ti * 3 + ki] = ld4(ruk, base + ti * 16 * 12288 + ki * 64, AUX_NT);
            SB();
        }
        float qv[24];
        {
            u32x4 g[3];
            const u32 goff = OFF_E2 + (((lane >> 4) * 32 + h) * 96 + hf * 48 + (lane & 15)) * 16;
            for (int it = 0; it < SPIN; ++it) { CFENCE();
#pragma unroll
                for (int i = 0; i < 3; ++i) g[i] = ld4(rws, goff + i * 256, AUX_SC1);
                bool bad = false;
#pragma unroll
                for (int i = 0; i < 3; ++i) bad |= (g[i].z != tlo) | (g[i].w != thi);
                if (!anyl(bad)) break;
            }
            STAMP(2);
            LAS float* qs = (LAS float*)(L + w * 512);
#pragma unroll
            for (int i = 0; i < 3; ++i) {
                float s = rs32(__uint_as_float(g[i].x), __uint_as_float(g[i].y));
                s = rs16(s, s);
                qs[2 * (16 * i + (lane & 15)) + (lane >> 5)] = s;
            }
            LGKM0();
#pragma unroll
            for (int ki = 0; ki < 3; ++ki) {
                const f32x4 u0 = *(LAS f32x4*)(qs + ki * 32 + 8 * (lane >> 4)), u1 = *(LAS f32x4*)(qs + ki * 32 + 8 * (lane >> 4) + 4);
                qv[ki * 8 + 0] = u0.x; qv[ki * 8 + 1] = u0.y; qv[ki * 8 + 2] = u0.z; qv[ki * 8 + 3] = u0.w;
                qv[ki * 8 + 4] = u1.x; qv[ki * 8 + 5] = u1.y; qv[ki * 8 + 6] = u1.z; qv[ki * 8 + 7] = u1.w;
            }
        }
        {
            float acc[3];
#pragma unroll
            for (int ti = 0; ti < 3; ++ti) {
                float s = 0.f;
#pragma unroll
                for (int ki = 0; ki < 3; ++ki) {
                    float f[8]; unp8(aw[ti * 3 + ki], f);
#pragma unroll
                    for (int j = 0; j < 8; ++j) s += f[j] * qv[ki * 8 + j];
                }
                s = rs16(s, s); s = rs32(s, s); acc[ti] = s;
            }
            const int row = lane >> 4;
            const float v = row == 0 ? acc[0] : (row == 1 ? acc[1] : acc[2]);
            if (row < 3) red[hf * 192 + tg * 48 + row * 16 + (lane & 15)] = v;
            BAR();
            if (w == 0 && lane < 48) {
                const int ch = lane < 24 ? lane : lane - 24;
                const float sc = 0.07216878364870322f * 1.4426950408889634f;
                float v8[8];
#pragma unroll
                for (int i = 0; i < 2; ++i) {
                    const f32x4 p0 = *(LAS f32x4*)(red + ch * 8 + i * 4), p1 = *(LAS f32x4*)(red + 192 + ch * 8 + i * 4);
                    v8[i * 4 + 0] = (p0.x + p1.x) * sc; v8[i * 4 + 1] = (p0.y + p1.y) * sc; v8[i * 4 + 2] = (p0.z + p1.z) * sc; v8[i * 4 + 3] = (p0.w + p1.w) * sc;
                }
                u32 o[4];
#pragma unroll
                for (int i = 0; i < 4; ++i) {
                    const u32 hi = cvtpk(v8[2 * i], v8[2 * i + 1]);
                    const u32 lo = cvtpk(v8[2 * i] - bflo(hi), v8[2 * i + 1] - bfhi(hi));
                    o[i] = canon(lane < 24 ? hi : lo);
                }
                st4(rws, OFF_E3 + (h * 4 + cq) * 768 + lane * 16, u32x4{o[0], o[1], o[2], o[3]});
            }
        }
        STAMP(3);
        const rsrc_t ruv = mkrs(wuv, DC * 6144 * 2);
        u32x3 vw[12];
        {
            const u32 base = (u32)(((cq * 192 + w * 24 + (lane >> 5)) * 32 + h) * 192 + 6 * (lane & 31)) * 2;
#pragma unroll
            for (int j = 0; j < 12; ++j) vw[j] = ld3(ruv, base + j * 2 * 12288, AUX_NT);
            SB();
        }
        if (xi >= 48) {
            const int gi = xi - 48, slice = gi % 40, rh = gi / 40;
            const int chunk = t & 7, rsub = t >> 3;
            const int row0 = rh * 320 + rsub;
            u16 cv[5]; u32x4 gw[5];
#pragma unroll
            for (int j = 0; j < 5; ++j) cv[j] = cc[row0 + 64 * j];
            const u32 off = (u32)(row0 * 3840 + 1280 + 64 * slice + 8 * chunk) * 2;
#pragma unroll
            for (int j = 0; j < 5; ++j) gw[j] = ld4(rmod, off + j * 64 * 7680, AUX_NT);
            SB();
            float a[8];
#pragma unroll
            for (int i = 0; i < 8; ++i) a[i] = 0.f;
#pragma unroll
            for (int j = 0; j < 5; ++j) {
                float f[8]; unp8(gw[j], f); const float s = silu(bf1(cv[j]));
#pragma unroll
                for (int i = 0; i < 8; ++i) a[i] += s * f[i];
            }
            const float v = rscat8(a, lane);
            const int e = ((lane >> 5) & 1) * 4 + ((lane >> 4) & 1) * 2 + ((lane >> 3) & 1);
            LAS float* redg = red + 512;
            redg[w * 64 + (lane & 7) * 8 + e] = v;
            BAR();
            if (w == 0 && lane < 16) {
                f32x4 s = {0.f, 0.f, 0.f, 0.f};
#pragma unroll
                for (int i = 0; i < 8; ++i) s += *(LAS f32x4*)(redg + i * 64 + lane * 4);
                st4(rws, OFF_G + (rh * 2560 + slice * 64) * 4 + lane * 16, u32x4{canonf(s.x), canonf(s.y), canonf(s.z), canonf(s.w)});
            }
        }
        {
            if (w < 4) {
                for (int it = 0; it < SPIN; ++it) { CFENCE();
                    const u32x4 g = ld4(rws, OFF_PF + t * 16, AUX_SC1);
                    if (!anyl((g.z != tlo) | (g.w != thi))) break;
                }
            }
            BAR();
        }
        STAMP(4);
        {
            u32x4 ml;
            for (int it = 0; it < SPIN; ++it) { CFENCE();
                ml = ld4(rws, OFF_ML + h * 1024 + lane * 16, AUX_SC1);
                if (!anyl(pois4(ml))) break;
            }
            STAMP(5);
            u32x3 ov[8];
            const u32 obase = OFF_O + (u32)((h * 4 + cq) * 128 + w * 16 + (lane >> 5)) * 384 + (lane & 31) * 12;
#pragma unroll
            for (int j = 0; j < 8; ++j) ov[j] = ld3(rws, obase + j * 768, AUX_SC1);
            SB();
            const float m0 = __uint_as_float(ml.x), l0 = __uint_as_float(ml.y), m1 = __uint_as_float(ml.z), l1 = __uint_as_float(ml.w);
            const float M = wave_max(fmaxf(m0, m1));
            const float w0 = __builtin_amdgcn_exp2f(m0 - M), w1_ = __builtin_amdgcn_exp2f(m1 - M);
            const float Lsum = wave_sum(w0 * l0 + w1_ * l1);
            const float inv = 1.f / Lsum;
            for (int it = 0; it < SPIN; ++it) { CFENCE();
                u32 bad = 0;
#pragma unroll
                for (int j = 0; j < 8; ++j) if (anyl(pois3(ov[j]))) bad |= 1u << j;
                if (!bad) break;
#pragma unroll
                for (int j = 0; j < 8; ++j) if (bad & (1u << j)) ov[j] = ld3(rws, obase + j * 768, AUX_SC1);
            }
            STAMP(6);
            float a[6];
#pragma unroll
            for (int i = 0; i < 6; ++i) a[i] = 0.f;
#pragma unroll
            for (int j = 0; j < 8; ++j) {
                const float wa = __uint_as_float(__builtin_amdgcn_readlane(__float_as_uint(w0), w * 8 + j));
                const float wb = __uint_as_float(__builtin_amdgcn_readlane(__float_as_uint(w1_), w * 8 + j));
                const float ww = (lane >> 5) ? wb : wa;
                float f[6]; unp6(ov[j], f);
#pragma unroll
                for (int i = 0; i < 6; ++i) a[i] += ww * f[i];
            }
#pragma unroll
            for (int i = 0; i < 3; ++i) red[w * 192 + 6 * (lane & 31) + 3 * (lane >> 5) + i] = rs32(a[i], a[i + 3]) * inv;
            BAR();
            LAS float* cl = (LAS float*)(L + w * 128);
            if (lane < 24) {
                float s = 0.f;
#pragma unroll
                for (int i = 0; i < 8; ++i) s += red[i * 192 + w * 24 + lane];
                cl[(lane & 1) * 12 + (lane >> 1)] = s;
            }
            LGKM0();
            float cv[12];
#pragma unroll
            for (int i = 0; i < 3; ++i) { const f32x4 u = *(LAS f32x4*)(cl + (lane >> 5) * 12 + i * 4); cv[i * 4] = u.x; cv[i * 4 + 1] = u.y; cv[i * 4 + 2] = u.z; cv[i * 4 + 3] = u.w; }
#pragma unroll
            for (int i = 0; i < 6; ++i) a[i] = 0.f;
#pragma unroll
            for (int j = 0; j < 12; ++j) {
                float f[6]; unp6(vw[j], f);
#pragma unroll
                for (int i = 0; i < 6; ++i) a[i] += cv[j] * f[i];
            }
            BAR();
#pragma unroll
            for (int i = 0; i < 3; ++i) red[w * 192 + 6 * (lane & 31) + 3 * (lane >> 5) + i] = rs32(a[i], a[i + 3]);
            BAR();
            if (w == 0 && lane < 48) {
                f32x4 s = {0.f, 0.f, 0.f, 0.f};
#pragma unroll
                for (int i = 0; i < 8; ++i) s += *(LAS f32x4*)(red + i * 192 + lane * 4);
                st4(rws, OFF_E5 + (cq * 6144 + h * 192) * 4 + lane * 16, u32x4{canonf(s.x), canonf(s.y), canonf(s.z), canonf(s.w)});
            }
        }
        STAMP(7);
        if (xi < 48) {
            const rsrc_t r1 = mkrs(w1, DM * DFF * 2), r2 = mkrs(w2, DFF * DM * 2);
            u32x4 f1[5], f2a[4]; u32 f2b[4];
            {
                const u32 b1 = (u32)((w * 80 + (lane >> 2)) * DFF + 32 * xi + 8 * (lane & 3)) * 2;
#pragma unroll
                for (int j = 0; j < 5; ++j) f1[j] = ld4(r1, b1 + j * 16 * DFF * 2, AUX_NT);
                const u32 b2 = (u32)((32 * xi + 4 * w) * DM) * 2;
#pragma unroll
                for (int j = 0; j < 4; ++j) { f2a[j] = ld4(r2, b2 + j * 1280 + lane * 16, AUX_NT); f2b[j] = __builtin_amdgcn_raw_buffer_load_b32(r2, b2 + j * 1280 + 1024 + lane * 4, 0, AUX_NT); }
                SB();
            }
            LAS float* gs = (LAS float*)(L + 4096);
            LAS float* xs = (LAS float*)(L + 4096 + 10240);
            LAS float* ap = (LAS float*)(L + 4096 + 10240 + 2560);
            LAS float* hs = (LAS float*)(L + 4096 + 10240 + 2560 + 7680);
            {
                u32x4 g0, g1, g2, g3;
                const int i0 = t, i1 = t + 512;
                for (int it = 0; it < SPIN; ++it) { CFENCE();
                    g0 = ld4(rws, OFF_G + i0 * 16, AUX_SC1); g1 = ld4(rws, OFF_G + 10240 + i0 * 16, AUX_SC1);
                    bool bad = pois4(g0) | pois4(g1);
                    if (t < 128) { g2 = ld4(rws, OFF_G + i1 * 16, AUX_SC1); g3 = ld4(rws, OFF_G + 10240 + i1 * 16, AUX_SC1); bad |= pois4(g2) | pois4(g3); }
                    if (!anyl(bad)) break;
                }
                *(LAS f32x4*)(gs + i0 * 4) = __builtin_bit_cast(f32x4, g0) + __builtin_bit_cast(f32x4, g1);
                if (t < 128) *(LAS f32x4*)(gs + i1 * 4) = __builtin_bit_cast(f32x4, g2) + __builtin_bit_cast(f32x4, g3);
                if (t < 320) { const u32 xx = ((const u32*)x)[t]; xs[2 * t] = bflo(xx); xs[2 * t + 1] = bfhi(xx); }
            }
            STAMP(8);
            {
                const int part = t / 160, i = t % 160;
                if (t < 480) {
                    u32x4 g[4];
                    const u32 goff = OFF_E6 + (part * 4 * 640 + i * 4) * 4;
                    for (int it = 0; it < SPIN; ++it) { CFENCE();
#pragma unroll
                        for (int j = 0; j < 4; ++j) g[j] = ld4(rws, goff + j * 2560, AUX_SC1);
                        if (!anyl(pois4(g[0]) | pois4(g[1]) | pois4(g[2]) | pois4(g[3]))) break;
                    }
                    *(LAS f32x4*)(ap + part * 640 + i * 4) = (__builtin_bit_cast(f32x4, g[0]) + __builtin_bit_cast(f32x4, g[1])) + (__builtin_bit_cast(f32x4, g[2]) + __builtin_bit_cast(f32x4, g[3]));
                }
                BAR();
                STAMP(9);
                float x1[10];
#pragma unroll
                for (int i2 = 0; i2 < 10; ++i2) { const int m = lane + 64 * i2; x1[i2] = xs[m] + gs[m] * ((ap[m] + ap[640 + m]) + ap[1280 + m]); }
                float mean, rstd; ln_stats(x1, mean, rstd);
                BAR();
                if (w == 0) {
#pragma unroll
                    for (int i2 = 0; i2 < 10; ++i2) xs[lane + 64 * i2] = x1[i2];
                }
                if (w == 1) {
#pragma unroll
                    for (int i2 = 0; i2 < 10; ++i2) { const int m = lane + 64 * i2; hs[m] = (x1[i2] - mean) * rstd * (1.f + gs[1280 + m]) + gs[640 + m]; }
                }
                BAR();
                float a[8];
#pragma unroll
                for (int i2 = 0; i2 < 8; ++i2) a[i2] = 0.f;
#pragma unroll
                for (int j = 0; j < 5; ++j) {
                    float f[8]; unp8(f1[j], f); const float hv = hs[w * 80 + j * 16 + (lane >> 2)];
#pragma unroll
                    for (int i2 = 0; i2 < 8; ++i2) a[i2] += hv * f[i2];
                }
                float v = rscat8(a, lane);
                v += dpp<0x104>(v);
                const int e = ((lane >> 5) & 1) * 4 + ((lane >> 4) & 1) * 2 + ((lane >> 3) & 1);
                if (!(lane & 4)) red[w * 32 + (lane & 3) * 8 + e] = v;
                BAR();
                float hid[4];
#pragma unroll
                for (int j = 0; j < 4; ++j) {
                    float s = 0.f;
#pragma unroll
                    for (int i2 = 0; i2 < 8; ++i2) s += red[i2 * 32 + 4 * w + j];
                    hid[j] = fmaxf(s, 0.f);
                }
                float b8[8], b2[2];
#pragma unroll
                for (int i2 = 0; i2 < 8; ++i2) b8[i2] = 0.f;
                b2[0] = b2[1] = 0.f;
#pragma unroll
                for (int j = 0; j < 4; ++j) {
                    float f[8]; unp8(f2a[j], f);
#pragma unroll
                    for (int i2 = 0; i2 < 8; ++i2) b8[i2] += hid[j] * f[i2];
                    b2[0] += hid[j] * bflo(f2b[j]); b2[1] += hid[j] * bfhi(f2b[j]);
                }
                LAS float* fr = (LAS float*)(L + 32768);
                *(LAS f32x4*)(fr + w * 640 + lane * 8) = f32x4{b8[0], b8[1], b8[2], b8[3]};
                *(LAS f32x4*)(fr + w * 640 + lane * 8 + 4) = f32x4{b8[4], b8[5], b8[6], b8[7]};
                *(LAS f32x2*)(fr + w * 640 + 512 + lane * 2) = f32x2{b2[0], b2[1]};
                BAR();
                if (t < 160) {
                    f32x4 s = {0.f, 0.f, 0.f, 0.f};
#pragma unroll
                    for (int i2 = 0; i2 < 8; ++i2) s += *(LAS f32x4*)(fr + i2 * 640 + t * 4);
                    st4(rws, OFF_E7 + xi * 2560 + t * 16, u32x4{canonf(s.x), canonf(s.y), canonf(s.z), canonf(s.w)});
                }
            }
            STAMP(10);
            if (xi < 40 && w < 3) {
                u32x4 g;
                const u32 goff = OFF_E7 + (t >> 2) * 2560 + (16 * xi + 4 * (t & 3)) * 4;
                for (int it = 0; it < SPIN; ++it) { CFENCE();
                    g = ld4(rws, goff, AUX_SC1);
                    if (!anyl(pois4(g))) break;
                }
                f32x4 s = __builtin_bit_cast(f32x4, g);
#pragma unroll
                for (int i = 0; i < 4; ++i) {
                    float v = s[i];
                    v += dpp<0x104>(v);
                    v += dpp<0x128>(v);
                    v = rs16(v, v); v = rs32(v, v);
                    s[i] = v;
                }
                if (lane < 4) *(LAS f32x4*)(red + 512 + w * 16 + lane * 4) = s;
            }
            if (xi < 40) {
                BAR();
                if (t < 8) {
                    const int m = 16 * xi + 2 * t;
                    const float s0 = (red[512 + 2 * t] + red[512 + 16 + 2 * t]) + red[512 + 32 + 2 * t];
                    const float s1 = (red[512 + 2 * t + 1] + red[512 + 16 + 2 * t + 1]) + red[512 + 32 + 2 * t + 1];
                    ((u32*)out)[m >> 1] = cvtpk(xs[m] + gs[1920 + m] * s0, xs[m + 1] + gs[1920 + m + 1] * s1);
                }
                if (xi == 0 && t == 0) __hip_atomic_store((GAS u64*)(ws + OFF_EPOCH), epoch + 1ull, __ATOMIC_RELAXED, __HIP_MEMORY_SCOPE_AGENT);
            }
            STAMP(11);
        }
    } else {
        const int yi = bid - 128, h = yi & 31, mg = yi >> 5;
        const rsrc_t rq = mkrs(wq, DM * 6144 * 2);
        u32x3 qw[10];
        {
            const u32 base = (u32)(((mg * 160 + w * 20 + (lane >> 5)) * 32 + h) * 192 + 6 * (lane & 31)) * 2;
#pragma unroll
            for (int j = 0; j < 10; ++j) qw[j] = ld3(rq, base + j * 2 * 12288, AUX_NT);
            SB();
        }
        {
            const int seg0 = w * 16;
#pragma unroll
            for (int j = 0; j < 6; ++j) {
                const int idx = j * 64 + lane, seg = seg0 + idx / 24, ch = idx % 24;
                st4(rws, OFF_O + (u32)(seg * 128 + yi) * 384 + ch * 16, PZ);
            }
            if (w == 0 && lane < 32) st2(rws, OFF_ML + lane * 1024 + yi * 8, u32x2{POISON, POISON});
            if (w == 1 && lane < 16 && yi < 120) st4(rws, OFF_E6 + ((yi % 12) * 640 + (yi / 12) * 64) * 4 + lane * 16, PZ);
            SB();
        }
        const u64 tag = (epoch + 1ull) * TAGMUL;
        const u32 tlo = (u32)tag, thi = (u32)(tag >> 32);
        {
            const int rg = lane >> 4, g = lane & 15;
            const bool act = g < 14;
            const u32 goff = g < 10 ? OFF_E1 + (rg * 320 + mg * 80 + w * 10 + g) * 16 : OFF_PF + (w * 16 + rg * 4 + (g - 10)) * 16;
            u32x4 gv = {0u, 0u, tlo, thi};
            for (int it = 0; it < SPIN; ++it) { CFENCE();
                if (act) gv = ld4(rws, goff, AUX_SC1);
                if (!anyl((gv.z != tlo) | (gv.w != thi))) break;
            }
            STAMP(1);
            float s = rs32(__uint_as_float(gv.x), __uint_as_float(gv.y));
            s = rs16(s, s);
            float a[6];
#pragma unroll
            for (int i = 0; i < 6; ++i) a[i] = 0.f;
#define QSTEP(j) { const float hv = dpp<0x150 + j>(s); float f[6]; unp6(qw[j], f); _Pragma("unroll") for (int i = 0; i < 6; ++i) a[i] += hv * f[i]; }
            QSTEP(0) QSTEP(1) QSTEP(2) QSTEP(3) QSTEP(4) QSTEP(5) QSTEP(6) QSTEP(7) QSTEP(8) QSTEP(9)
#undef QSTEP
#pragma unroll
            for (int i = 0; i < 3; ++i) red[w * 192 + 6 * (lane & 31) + 3 * (lane >> 5) + i] = rs32(a[i], a[i + 3]);
            DRAIN();
            BAR();
            if (t < 96) {
                f32x2 s2 = {0.f, 0.f};
#pragma unroll
                for (int i = 0; i < 8; ++i) s2 += *(LAS f32x2*)(red + i * 192 + t * 2);
                st4(rws, OFF_E2 + ((mg * 32 + h) * 96 + t) * 16, u32x4{__float_as_uint(s2.x), __float_as_uint(s2.y), tlo, thi});
            }
            if (t == 128) st4(rws, OFF_PF + bid * 16, u32x4{0u, 0u, tlo, thi});
        }
        STAMP(2);
        {
            const unsigned char* src = (const unsigned char*)ckv + (size_t)yi * 64 * 1536;
#pragma unroll
            for (int j = 0; j < 12; ++j) {
                const int idx = (w * 12 + j) * 64 + lane, row = idx / 96, p = idx % 96;
                const int ch = (p & ~15) | ((p ^ row) & 15);
                __builtin_amdgcn_global_load_lds((const GAS u32*)(src + row * 1536 + ch * 16), (LAS u32*)(L + L_KV + (w * 12 + j) * 1024), 16, 0, AUX_NT);
            }
            SB();
        }
        const int kq = w >> 1, ht = w & 1;
        u32x4 qf[12];
        {
            const u32 qoff = OFF_E3 + (u32)((ht * 16 + (lane & 15)) * 4 + kq) * 768 + (lane >> 4) * 16;
            for (int it = 0; it < SPIN; ++it) { CFENCE();
                qf[0] = ld4(rws, qoff, AUX_SC1);
                if (!anyl(pois4(qf[0]))) break;
            }
            STAMP(3);
#pragma unroll
            for (int j = 1; j < 12; ++j) qf[j] = ld4(rws, qoff + (j % 6) * 64 + (j / 6) * 384, AUX_SC1);
            SB();
            for (int it = 0; it < SPIN; ++it) { CFENCE();
                u32 bad = 0;
#pragma unroll
                for (int j = 1; j < 12; ++j) if (anyl(pois4(qf[j]))) bad |= 1u << j;
                if (!bad) break;
#pragma unroll
                for (int j = 1; j < 12; ++j) if (bad & (1u << j)) qf[j] = ld4(rws, qoff + (j % 6) * 64 + (j / 6) * 384, AUX_SC1);
            }
        }
        DRAIN();
        BAR();
        STAMP(4);
        {
            const int r = lane & 15;
            const int keyr = 8 * (r >> 3) + 2 * (r & 3) + ((r >> 2) & 1);
            f32x4 acc[4];
#pragma unroll
            for (int kt = 0; kt < 4; ++kt) acc[kt] = f32x4{0.f, 0.f, 0.f, 0.f};
#pragma unroll
            for (int ks = 0; ks < 6; ++ks) {
                const int ch = kq * 24 + ks * 4 + (lane >> 4);
                const int p = (ch & ~15) | ((ch ^ keyr) & 15);
#pragma unroll
                for (int kt = 0; kt < 4; ++kt) {
                    const u32x4 a = *(LAS u32x4*)(L + L_KV + (kt * 16 + keyr) * 1536 + p * 16);
                    acc[kt] = mfma(a, qf[ks], acc[kt]);
                    acc[kt] = mfma(a, qf[6 + ks], acc[kt]);
                }
            }
            LAS float* sp = (LAS float*)(L + L_SP);
#pragma unroll
            for (int kt = 0; kt < 4; ++kt) *(LAS f32x4*)(sp + (kq * 32 + ht * 16 + (lane & 15)) * 68 + kt * 16 + 4 * (lane >> 4)) = acc[kt];
        }
        BAR();
        {
            LAS float* sp = (LAS float*)(L + L_SP);
            const int hd = t >> 4, qd = t & 15;
            f32x4 s = *(LAS f32x4*)(sp + hd * 68 + qd * 4);
#pragma unroll
            for (int k = 1; k < 4; ++k) s += *(LAS f32x4*)(sp + (k * 32 + hd) * 68 + qd * 4);
            const float m = row_max(fmaxf(fmaxf(s.x, s.y), fmaxf(s.z, s.w)));
            const float p0 = __builtin_amdgcn_exp2f(s.x - m), p1 = __builtin_amdgcn_exp2f(s.y - m), p2 = __builtin_amdgcn_exp2f(s.z - m), p3 = __builtin_amdgcn_exp2f(s.w - m);
            const u32 b01 = cvtpk(p0, p1), b23 = cvtpk(p2, p3);
            const float l = row_sum((bflo(b01) + bfhi(b01)) + (bflo(b23) + bfhi(b23)));
            *(LAS u32x2*)(L + L_P + hd * 144 + qd * 8) = u32x2{b01, b23};
            if (qd == 0) st2(rws, OFF_ML + hd * 1024 + yi * 8, u32x2{canonf(m), canonf(l)});
        }
        BAR();
        STAMP(5);
        {
            u32x4 pb[2][2];
#pragma unroll
            for (int hh = 0; hh < 2; ++hh)
#pragma unroll
                for (int k2 = 0; k2 < 2; ++k2) pb[hh][k2] = *(LAS u32x4*)(L + L_P + (hh * 16 + (lane & 15)) * 144 + k2 * 64 + (lane >> 4) * 16);
            f32x4 acc[6][2];
#pragma unroll
            for (int ct = 0; ct < 6; ++ct) { acc[ct][0] = f32x4{0.f, 0.f, 0.f, 0.f}; acc[ct][1] = f32x4{0.f, 0.f, 0.f, 0.f}; }
            const int g = lane >> 4, e = (lane >> 2) & 3, pp = lane & 3;
#pragma unroll
            for (int ct = 0; ct < 6; ++ct) {
                const int ch = w * 12 + ct * 2 + (pp >> 1);
#pragma unroll
                for (int k2 = 0; k2 < 2; ++k2) {
                    const int ka = k2 * 32 + 8 * g + 2 * e, kb = ka + 1;
                    const int pa = (ch & ~15) | ((ch ^ ka) & 15), pbx = (ch & ~15) | ((ch ^ kb) & 15);
                    const s16x4 t0 = __builtin_amdgcn_ds_read_tr16_b64_v4i16((LAS s16x4*)(L + L_KV + ka * 1536 + pa * 16 + (pp & 1) * 8));
                    const s16x4 t1 = __builtin_amdgcn_ds_read_tr16_b64_v4i16((LAS s16x4*)(L + L_KV + kb * 1536 + pbx * 16 + (pp & 1) * 8));
                    const u32x2 a0 = __builtin_bit_cast(u32x2, t0), a1 = __builtin_bit_cast(u32x2, t1);
                    const u32x4 a = {a0.x, a0.y, a1.x, a1.y};
                    acc[ct][0] = mfma(a, pb[0][k2], acc[ct][0]);
                    acc[ct][1] = mfma(a, pb[1][k2], acc[ct][1]);
                }
            }
            const int ocq = w >> 1;
#pragma unroll
            for (int hh = 0; hh < 2; ++hh)
#pragma unroll
                for (int cp = 0; cp < 3; ++cp) {
                    const f32x4 A = acc[2 * cp][hh], B = acc[2 * cp + 1][hh];
                    const u32 ax = cvtpk(A.x, A.y), ay = cvtpk(A.z, A.w), bx = cvtpk(B.x, B.y), by = cvtpk(B.z, B.w);
                    auto r0 = __builtin_amdgcn_permlane16_swap(ax, bx, false, false);
                    auto r1 = __builtin_amdgcn_permlane16_swap(ay, by, false, false);
                    const u32x4 v = {canon(r0[0]), canon(r1[0]), canon(r0[1]), canon(r1[1])};
                    const int c = (w & 1) * 96 + (2 * cp + (g & 1)) * 16 + (g >> 1) * 8;
                    st4(rws, OFF_O + (u32)(((hh * 16 + (lane & 15)) * 4 + ocq) * 128 + yi) * 384 + c * 2, v);
                }
        }
        STAMP(6);
        if (yi < 120) {
            const int rg = yi % 12, cs = yi / 12;
            const rsrc_t ro = mkrs(wo, 6144 * DM * 2);
            u32x4 ow[8];
            const u32 base = (u32)((rg * 512 + w * 64 + (lane >> 3) * 8) * DM + cs * 64 + (lane & 7) * 8) * 2;
#pragma unroll
            for (int j = 0; j < 8; ++j) ow[j] = ld4(ro, base + j * 1280, AUX_NT);
            SB();
            u32x4 g;
            const u32 goff = OFF_E5 + ((lane >> 4) * 6144 + rg * 512 + w * 64 + (lane & 15) * 4) * 4;
            for (int it = 0; it < SPIN; ++it) { CFENCE();
                g = ld4(rws, goff, AUX_SC1);
                if (!anyl(pois4(g))) break;
            }
            STAMP(7);
            f32x4 s = __builtin_bit_cast(f32x4, g);
#pragma unroll
            for (int i = 0; i < 4; ++i) { float v = s[i]; v = rs16(v, v); v = rs32(v, v); s[i] = v; }
            LAS float* cs_ = (LAS float*)(L + L_SP + w * 256);
            if (lane < 16) *(LAS f32x4*)(cs_ + lane * 4) = s;
            LGKM0();
            const f32x4 c0 = *(LAS f32x4*)(cs_ + (lane >> 3) * 8), c1 = *(LAS f32x4*)(cs_ + (lane >> 3) * 8 + 4);
            const float cv[8] = {c0.x, c0.y, c0.z, c0.w, c1.x, c1.y, c1.z, c1.w};
            float a[8];
#pragma unroll
            for (int i = 0; i < 8; ++i) a[i] = 0.f;
#pragma unroll
            for (int j = 0; j < 8; ++j) {
                float f[8]; unp8(ow[j], f);
#pragma unroll
                for (int i = 0; i < 8; ++i) a[i] += cv[j] * f[i];
            }
            const float v = rscat8(a, lane);
            const int e = ((lane >> 5) & 1) * 4 + ((lane >> 4) & 1) * 2 + ((lane >> 3) & 1);
            red[w * 64 + (lane & 7) * 8 + e] = v;
            BAR();
            if (w == 0 && lane < 16) {
                f32x4 s4 = {0.f, 0.f, 0.f, 0.f};
#pragma unroll
                for (int i = 0; i < 8; ++i) s4 += *(LAS f32x4*)(red + i * 64 + lane * 4);
                st4(rws, OFF_E6 + (rg * 640 + cs * 64) * 4 + lane * 16, u32x4{canonf(s4.x), canonf(s4.y), canonf(s4.z), canonf(s4.w)});
            }
            STAMP(8);
        }
    }
    if (bid == AMP_WG && t == 0) {
        const u64 now = __builtin_amdgcn_s_memrealtime();
        const u64 tgt = now + (u64)AMP_K * (st[AMP_TO] - st[AMP_FROM]);
        for (int it = 0; it < 2000000; ++it) { if (__builtin_amdgcn_s_memrealtime() >= tgt) break; __builtin_amdgcn_s_sleep(1); }
    }
}

extern "C" void kernel_launch(void* const* d_in, const int* in_sizes, int n_in, void* d_out, int out_size, void* d_ws, size_t ws_size, hipStream_t stream) {
    (void)in_sizes; (void)n_in; (void)out_size; (void)ws_size;
    hipFuncSetAttribute((const void*)adaln_mla_mlp_kernel, hipFuncAttributeMaxDynamicSharedMemorySize, LDS_BYTES);
    adaln_mla_mlp_kernel<<<NWG, NT, LDS_BYTES, stream>>>(
        (const u16*)d_in[0], (const u16*)d_in[1], (const u16*)d_in[2], (const u16*)d_in[3], (const u16*)d_in[4], (const u16*)d_in[5],
        (const u16*)d_in[7], (const u16*)d_in[8], (const u16*)d_in[9], (const u16*)d_in[10], (u16*)d_out, (unsigned char*)d_ws);
}
```

```cpp
#include <hip/hip_runtime.h>
#include <stdint.h>

typedef unsigned u32;
typedef unsigned long long u64;
typedef unsigned short u16;
typedef u32 u32x2 __attribute__((ext_vector_type(2)));
typedef u32 u32x3 __attribute__((ext_vector_type(3)));
typedef u32 u32x4 __attribute__((ext_vector_type(4)));
typedef float f32x2 __attribute__((ext_vector_type(2)));
typedef float f32x4 __attribute__((ext_vector_type(4)));
typedef short bf16x8 __attribute__((ext_vector_type(8)));
typedef short s16x4 __attribute__((ext_vector_type(4)));
typedef __amdgpu_buffer_rsrc_t rsrc_t;
#define LAS __attribute__((address_space(3)))
#define GAS __attribute__((address_space(1)))

constexpr int NH = 32, SK = 8192, HD = 192, DM = 640, DC = 768, DFF = 1536;
constexpr int NWG = 256, NT = 512;

constexpr u32 OFF_EPOCH = 0;
constexpr u32 OFF_PF = 256;
constexpr u32 OFF_E1 = OFF_PF + 256 * 16;
constexpr u32 OFF_E2 = OFF_E1 + 4 * 320 * 16;
constexpr u32 OFF_E3 = OFF_E2 + 4 * 32 * 96 * 16;
constexpr u32 OFF_ML = OFF_E3 + 32 * 4 * 768;
constexpr u32 OFF_E5 = OFF_ML + 32 * 128 * 8;
constexpr u32 OFF_E6 = OFF_E5 + 4 * 6144 * 4;
constexpr u32 OFF_G = OFF_E6 + 12 * 640 * 4;
constexpr u32 OFF_E7 = OFF_G + 2 * 2560 * 4;
constexpr u32 OFF_O = OFF_E7 + 48 * 640 * 4;
constexpr u32 WS_BYTES = OFF_O + 32 * 4 * 128 * 384;
constexpr u32 OFF_DIAG = WS_BYTES;
static_assert(OFF_O % 128 == 0 && OFF_E3 % 128 == 0 && OFF_E5 % 128 == 0, "alignment");

constexpr int L_KV = 0;
constexpr int L_SP = 98304;
constexpr int L_P = L_SP + 4 * 32 * 68 * 4;
constexpr int L_RED = L_P + 32 * 144;
constexpr int LDS_BYTES = L_RED + 8192;
constexpr int SPIN = 1 << 16;
constexpr u32 POISON = 0xFFFFFFFFu;
constexpr int AUX_SC1 = 16, AUX_NT = 2;
constexpr u64 TAGMUL = 0x9E3779B97F4A7C15ull;

__device__ __forceinline__ float bflo(u32 v) { return __uint_as_float(v << 16); }
__device__ __forceinline__ float bfhi(u32 v) { return __uint_as_float(v & 0xFFFF0000u); }
__device__ __forceinline__ float bf1(u16 v) { return __uint_as_float((u32)v << 16); }
typedef __bf16 bf16x2_t __attribute__((ext_vector_type(2)));
__device__ __forceinline__ u32 cvtpk(float lo, float hi) { f32x2 v = {lo, hi}; return __builtin_bit_cast(u32, __builtin_convertvector(v, bf16x2_t)); }
__device__ __forceinline__ u32 canon(u32 v) { return v < 0xFFFFFFFEu ? v : 0xFFFFFFFEu; }
__device__ __forceinline__ u32 canonf(float v) { return canon(__float_as_uint(v)); }
__device__ __forceinline__ rsrc_t mkrs(const void* p, u32 bytes) { return __builtin_amdgcn_make_buffer_rsrc((void*)p, 0, bytes, 0x00020000); }
__device__ __forceinline__ u32x4 ld4(rsrc_t r, u32 off, int aux) { return aux == AUX_SC1 ? __builtin_amdgcn_raw_buffer_load_b128(r, off, 0, AUX_SC1) : __builtin_amdgcn_raw_buffer_load_b128(r, off, 0, AUX_NT); }
__device__ __forceinline__ u32x3 ld3(rsrc_t r, u32 off, int aux) { return aux == AUX_SC1 ? __builtin_amdgcn_raw_buffer_load_b96(r, off, 0, AUX_SC1) : __builtin_amdgcn_raw_buffer_load_b96(r, off, 0, AUX_NT); }
__device__ __forceinline__ void st4(rsrc_t r, u32 off, u32x4 v) { __builtin_amdgcn_raw_buffer_store_b128(v, r, off, 0, AUX_SC1); }
__device__ __forceinline__ void st2(rsrc_t r, u32 off, u32x2 v) { __builtin_amdgcn_raw_buffer_store_b64(v, r, off, 0, AUX_SC1); }
#define CFENCE() asm volatile("" ::: "memory")
#define SB() __builtin_amdgcn_sched_barrier(0)
#define BAR() asm volatile("s_waitcnt lgkmcnt(0)\n\ts_barrier" ::: "memory")
#define LGKM0() asm volatile("s_waitcnt lgkmcnt(0)" ::: "memory")
#define DRAIN() asm volatile("s_waitcnt vmcnt(0)" ::: "memory")

template <int CTRL> __device__ __forceinline__ float dpp(float x) { return __int_as_float(__builtin_amdgcn_update_dpp(0, __float_as_int(x), CTRL, 0xf, 0xf, true)); }
__device__ __forceinline__ float rs32(float a, float b) { auto r = __builtin_amdgcn_permlane32_swap(__float_as_uint(a), __float_as_uint(b), false, false); return __uint_as_float(r[0]) + __uint_as_float(r[1]); }
__device__ __forceinline__ float rs16(float a, float b) { auto r = __builtin_amdgcn_permlane16_swap(__float_as_uint(a), __float_as_uint(b), false, false); return __uint_as_float(r[0]) + __uint_as_float(r[1]); }
__device__ __forceinline__ float row_sum(float x) { x += dpp<0x128>(x); x += dpp<0x124>(x); x += dpp<0x122>(x); x += dpp<0x121>(x); return x; }
__device__ __forceinline__ float row_max(float x) { x = fmaxf(x, dpp<0x128>(x)); x = fmaxf(x, dpp<0x124>(x)); x = fmaxf(x, dpp<0x122>(x)); x = fmaxf(x, dpp<0x121>(x)); return x; }
__device__ __forceinline__ float wave_sum(float x) { x = row_sum(x); x = rs16(x, x); x = rs32(x, x); return x; }
__device__ __forceinline__ float wave_max(float x) {
    x = row_max(x);
    { auto r = __builtin_amdgcn_permlane16_swap(__float_as_uint(x), __float_as_uint(x), false, false); x = fmaxf(__uint_as_float(r[0]), __uint_as_float(r[1])); }
    { auto r = __builtin_amdgcn_permlane32_swap(__float_as_uint(x), __float_as_uint(x), false, false); x = fmaxf(__uint_as_float(r[0]), __uint_as_float(r[1])); }
    return x;
}
__device__ __forceinline__ float rscat8(const float (&a)[8], int lane) {
    float r0 = rs32(a[0], a[4]), r1 = rs32(a[1], a[5]), r2 = rs32(a[2], a[6]), r3 = rs32(a[3], a[7]);
    float s0 = rs16(r0, r2), s1 = rs16(r1, r3);
    float t0 = s0 + dpp<0x128>(s0), t1 = s1 + dpp<0x128>(s1);
    return (lane & 8) ? t1 : t0;
}
__device__ __forceinline__ bool pois4(u32x4 v) { u32 m = max(max(v.x, v.y), max(v.z, v.w)); return m == POISON; }
__device__ __forceinline__ bool pois3(u32x3 v) { u32 m = max(max(v.x, v.y), v.z); return m == POISON; }
__device__ __forceinline__ bool anyl(bool b) { return __builtin_amdgcn_ballot_w64(b) != 0ull; }
__device__ __forceinline__ f32x4 mfma(u32x4 a, u32x4 b, f32x4 c) { return __builtin_amdgcn_mfma_f32_16x16x32_bf16(__builtin_bit_cast(bf16x8, a), __builtin_bit_cast(bf16x8, b), c, 0, 0, 0); }
__device__ __forceinline__ float silu(float v) { return v / (1.f + __expf(-v)); }
__device__ __forceinline__ void unp8(u32x4 v, float (&f)[8]) { f[0] = bflo(v.x); f[1] = bfhi(v.x); f[2] = bflo(v.y); f[3] = bfhi(v.y); f[4] = bflo(v.z); f[5] = bfhi(v.z); f[6] = bflo(v.w); f[7] = bfhi(v.w); }
__device__ __forceinline__ void unp6(u32x3 v, float (&f)[6]) { f[0] = bflo(v.x); f[1] = bfhi(v.x); f[2] = bflo(v.y); f[3] = bfhi(v.y); f[4] = bflo(v.z); f[5] = bfhi(v.z); }

#define STAMP(i) do { } while (0)

__device__ __forceinline__ void ln_stats(const float (&v)[10], float& mean, float& rstd) {
    float s = 0.f;
#pragma unroll
    for (int i = 0; i < 10; ++i) s += v[i];
    mean = wave_sum(s) * (1.f / 640.f);
    float q = 0.f;
#pragma unroll
    for (int i = 0; i < 10; ++i) { float d = v[i] - mean; q += d * d; }
    float var = wave_sum(q) * (1.f / 640.f);
    rstd = 1.f / sqrtf(var + 1e-5f);
}

__global__ __launch_bounds__(NT, 2) void adaln_mla_mlp_kernel(
    const u16* __restrict__ x, const u16* __restrict__ ckv, const u16* __restrict__ wq, const u16* __restrict__ wuk,
    const u16* __restrict__ wuv, const u16* __restrict__ wo, const u16* __restrict__ w1, const u16* __restrict__ w2,
    const u16* __restrict__ cc, const u16* __restrict__ wmod, u16* __restrict__ out, unsigned char* __restrict__ ws) {
    extern __shared__ __attribute__((aligned(16))) unsigned char smem[];
    LAS unsigned char* L = (LAS unsigned char*)smem;
    const int t = threadIdx.x, lane = t & 63, w = __builtin_amdgcn_readfirstlane(t >> 6);
    const int bid = blockIdx.x;
    const rsrc_t rws = mkrs(ws, WS_BYTES);
    STAMP(0);
    const u64 epoch = __hip_atomic_load((GAS u64*)(ws + OFF_EPOCH), __ATOMIC_RELAXED, __HIP_MEMORY_SCOPE_AGENT);
    if (bid >= 120 && bid < 128 && w == 6) asm volatile("buffer_wbl2 sc1" ::: "memory");
    const u32x4 PZ = {POISON, POISON, POISON, POISON};
    LAS float* red = (LAS float*)(L + L_RED);

    if (bid < 128) {
        const int xi = bid, h = xi & 31, cq = xi >> 5;
        const rsrc_t rmod = mkrs(wmod, DM * 3840 * 2);
        const bool isM = xi < 80;
        u32x4 mw0, mw1, mw2; u16 c0 = 0, c1 = 0, c2 = 0; u32 xr[5]; u16 xm = 0;
        int m_rg = 0, m_s = 0;
        if (isM) {
            const int x8 = xi & 7, b = (xi >> 3) & 1, slot = xi >> 4, qq = slot * 8 + x8;
            m_rg = qq / 10; m_s = 2 * (qq % 10) + b;
            const int chunk = t & 7, rsub = t >> 3;
            const int col = chunk < 4 ? 32 * m_s + 8 * chunk : 640 + 32 * m_s + 8 * (chunk - 4);
            const int row0 = m_rg * 160 + rsub;
            c0 = cc[row0]; c1 = cc[row0 + 64]; if (w < 4) c2 = cc[row0 + 128];
            if (w == 0) {
#pragma unroll
                for (int i = 0; i < 5; ++i) xr[i] = ((const u32*)x)[lane + 64 * i];
                xm = x[32 * m_s + (lane & 31)];
            }
            const u32 off = (u32)(row0 * 3840 + col) * 2;
            mw0 = ld4(rmod, off, AUX_NT); mw1 = ld4(rmod, off + 64 * 7680, AUX_NT);
            if (w < 4) mw2 = ld4(rmod, off + 128 * 7680, AUX_NT);
            SB();
        }
        if (w == 7) {
            if (lane < 48) { st4(rws, OFF_E3 + (h * 4 + cq) * 768 + lane * 16, PZ); st4(rws, OFF_E5 + (cq * 6144 + h * 192) * 4 + lane * 16, PZ); }
            if (xi < 48) {
                st4(rws, OFF_E7 + xi * 2560 + lane * 16, PZ); st4(rws, OFF_E7 + xi * 2560 + 1024 + lane * 16, PZ);
                if (lane < 32) st4(rws, OFF_E7 + xi * 2560 + 2048 + lane * 16, PZ);
            } else if (lane < 16) {
                const int gi = xi - 48;
                st4(rws, OFF_G + ((gi / 40) * 2560 + (gi % 40) * 64) * 4 + lane * 16, PZ);
            }
            SB();
        }
        const u64 tag = (epoch + 1ull) * TAGMUL;
        const u32 tlo = (u32)tag, thi = (u32)(tag >> 32);
        if (isM) {
            float a[8], f[8];
            const float s0 = silu(bf1(c0)), s1 = silu(bf1(c1));
            unp8(mw0, f);
#pragma unroll
            for (int i = 0; i < 8; ++i) a[i] = s0 * f[i];
            unp8(mw1, f);
#pragma unroll
            for (int i = 0; i < 8; ++i) a[i] += s1 * f[i];
            if (w < 4) {
                const float s2 = silu(bf1(c2));
                unp8(mw2, f);
#pragma unroll
                for (int i = 0; i < 8; ++i) a[i] += s2 * f[i];
            }
            const float v = rscat8(a, lane);
            const int e = ((lane >> 5) & 1) * 4 + ((lane >> 4) & 1) * 2 + ((lane >> 3) & 1);
            red[w * 64 + (lane & 7) * 8 + e] = v;
            float lnx = 0.f;
            if (w == 0) {
                float xv[10];
#pragma unroll
                for (int i = 0; i < 5; ++i) { xv[2 * i] = bflo(xr[i]); xv[2 * i + 1] = bfhi(xr[i]); }
                float mean, rstd; ln_stats(xv, mean, rstd);
                lnx = (bf1(xm) - mean) * rstd;
            }
            BAR();
            if (w == 0) {
                float s = 0.f;
#pragma unroll
                for (int i = 0; i < 8; ++i) s += red[i * 64 + lane];
                auto r = __builtin_amdgcn_permlane32_swap(__float_as_uint(s), __float_as_uint(s), false, false);
                const float sa = __uint_as_float(r[0]), ca = __uint_as_float(r[1]);
                float hv = lnx * ca + sa; if (m_rg == 0) hv += lnx;
                const float hn = dpp<0xB1>(hv);
                if (lane < 32 && !(lane & 1)) st4(rws, OFF_E1 + (m_rg * 320 + m_s * 16 + (lane >> 1)) * 16, u32x4{__float_as_uint(hv), __float_as_uint(hn), tlo, thi});
            }
        }
        STAMP(1);
        if (w == 7) { DRAIN(); if (lane == 0) st4(rws, OFF_PF + bid * 16, u32x4{0u, 0u, tlo, thi}); }
        const rsrc_t ruk = mkrs(wuk, DC * 6144 * 2);
        const int tg = w >> 1, hf = w & 1;
        u32x4 aw[9];
        {
            const u32 base = (u32)(((cq * 192 + tg * 48 + (lane & 15)) * 32 + h) * 192 + hf * 96 + 8 * (lane >> 4)) * 2;
#pragma unroll
            for (int ti = 0; ti < 3; ++ti)
#pragma unroll
                for (int ki = 0; ki < 3; ++ki) aw[ti * 3 + ki] = ld4(ruk, base + ti * 16 * 12288 + ki * 64, AUX_NT);
            SB();
        }
        float qv[24];
        {
            u32x4 g[3];
            const u32 goff = OFF_E2 + (((lane >> 4) * 32 + h) * 96 + hf * 48 + (lane & 15)) * 16;
            for (int it = 0; it < SPIN; ++it) { CFENCE();
#pragma unroll
                for (int i = 0; i < 3; ++i) g[i] = ld4(rws, goff + i * 256, AUX_SC1);
                bool bad = false;
#pragma unroll
                for (int i = 0; i < 3; ++i) bad |= (g[i].z != tlo) | (g[i].w != thi);
                if (!anyl(bad)) break;
            }
            STAMP(2);
            LAS float* qs = (LAS float*)(L + w * 512);
#pragma unroll
            for (int i = 0; i < 3; ++i) {
                float s = rs32(__uint_as_float(g[i].x), __uint_as_float(g[i].y));
                s = rs16(s, s);
                qs[2 * (16 * i + (lane & 15)) + (lane >> 5)] = s;
            }
            LGKM0();
#pragma unroll
            for (int ki = 0; ki < 3; ++ki) {
                const f32x4 u0 = *(LAS f32x4*)(qs + ki * 32 + 8 * (lane >> 4)), u1 = *(LAS f32x4*)(qs + ki * 32 + 8 * (lane >> 4) + 4);
                qv[ki * 8 + 0] = u0.x; qv[ki * 8 + 1] = u0.y; qv[ki * 8 + 2] = u0.z; qv[ki * 8 + 3] = u0.w;
                qv[ki * 8 + 4] = u1.x; qv[ki * 8 + 5] = u1.y; qv[ki * 8 + 6] = u1.z; qv[ki * 8 + 7] = u1.w;
            }
        }
        {
            float acc[3];
#pragma unroll
            for (int ti = 0; ti < 3; ++ti) {
                float s = 0.f;
#pragma unroll
                for (int ki = 0; ki < 3; ++ki) {
                    float f[8]; unp8(aw[ti * 3 + ki], f);
#pragma unroll
                    for (int j = 0; j < 8; ++j) s += f[j] * qv[ki * 8 + j];
                }
                s = rs16(s, s); s = rs32(s, s); acc[ti] = s;
            }
            const int row = lane >> 4;
            const float v = row == 0 ? acc[0] : (row == 1 ? acc[1] : acc[2]);
            if (row < 3) red[hf * 192 + tg * 48 + row * 16 + (lane & 15)] = v;
            BAR();
            if (w == 0 && lane < 48) {
                const int ch = lane < 24 ? lane : lane - 24;
                const float sc = 0.07216878364870322f * 1.4426950408889634f;
                float v8[8];
#pragma unroll
                for (int i = 0; i < 2; ++i) {
                    const f32x4 p0 = *(LAS f32x4*)(red + ch * 8 + i * 4), p1 = *(LAS f32x4*)(red + 192 + ch * 8 + i * 4);
                    v8[i * 4 + 0] = (p0.x + p1.x) * sc; v8[i * 4 + 1] = (p0.y + p1.y) * sc; v8[i * 4 + 2] = (p0.z + p1.z) * sc; v8[i * 4 + 3] = (p0.w + p1.w) * sc;
                }
                u32 o[4];
#pragma unroll
                for (int i = 0; i < 4; ++i) {
                    const u32 hi = cvtpk(v8[2 * i], v8[2 * i + 1]);
                    const u32 lo = cvtpk(v8[2 * i] - bflo(hi), v8[2 * i + 1] - bfhi(hi));
                    o[i] = canon(lane < 24 ? hi : lo);
                }
                st4(rws, OFF_E3 + (h * 4 + cq) * 768 + lane * 16, u32x4{o[0], o[1], o[2], o[3]});
            }
        }
        STAMP(3);
        const rsrc_t ruv = mkrs(wuv, DC * 6144 * 2);
        u32x3 vw[12];
        {
            const u32 base = (u32)(((cq * 192 + w * 24 + (lane >> 5)) * 32 + h) * 192 + 6 * (lane & 31)) * 2;
#pragma unroll
            for (int j = 0; j < 12; ++j) vw[j] = ld3(ruv, base + j * 2 * 12288, AUX_NT);
            SB();
        }
        if (xi >= 48) {
            const int gi = xi - 48, slice = gi % 40, rh = gi / 40;
            const int chunk = t & 7, rsub = t >> 3;
            const int row0 = rh * 320 + rsub;
            u16 cv[5]; u32x4 gw[5];
#pragma unroll
            for (int j = 0; j < 5; ++j) cv[j] = cc[row0 + 64 * j];
            const u32 off = (u32)(row0 * 3840 + 1280 + 64 * slice + 8 * chunk) * 2;
#pragma unroll
            for (int j = 0; j < 5; ++j) gw[j] = ld4(rmod, off + j * 64 * 7680, AUX_NT);
            SB();
            float a[8];
#pragma unroll
            for (int i = 0; i < 8; ++i) a[i] = 0.f;
#pragma unroll
            for (int j = 0; j < 5; ++j) {
                float f[8]; unp8(gw[j], f); const float s = silu(bf1(cv[j]));
#pragma unroll
                for (int i = 0; i < 8; ++i) a[i] += s * f[i];
            }
            const float v = rscat8(a, lane);
            const int e = ((lane >> 5) & 1) * 4 + ((lane >> 4) & 1) * 2 + ((lane >> 3) & 1);
            LAS float* redg = red + 512;
            redg[w * 64 + (lane & 7) * 8 + e] = v;
            BAR();
            if (w == 0 && lane < 16) {
                f32x4 s = {0.f, 0.f, 0.f, 0.f};
#pragma unroll
                for (int i = 0; i < 8; ++i) s += *(LAS f32x4*)(redg + i * 64 + lane * 4);
                st4(rws, OFF_G + (rh * 2560 + slice * 64) * 4 + lane * 16, u32x4{canonf(s.x), canonf(s.y), canonf(s.z), canonf(s.w)});
            }
        }
        {
            if (w < 4) {
                for (int it = 0; it < SPIN; ++it) { CFENCE();
                    const u32x4 g = ld4(rws, OFF_PF + t * 16, AUX_SC1);
                    if (!anyl((g.z != tlo) | (g.w != thi))) break;
                }
            }
            BAR();
        }
        STAMP(4);
        {
            u32x4 ml;
            for (int it = 0; it < SPIN; ++it) { CFENCE();
                ml = ld4(rws, OFF_ML + h * 1024 + lane * 16, AUX_SC1);
                if (!anyl(pois4(ml))) break;
            }
            STAMP(5);
            u32x3 ov[8];
            const u32 obase = OFF_O + (u32)((h * 4 + cq) * 128 + w * 16 + (lane >> 5)) * 384 + (lane & 31) * 12;
#pragma unroll
            for (int j = 0; j < 8; ++j) ov[j] = ld3(rws, obase + j * 768, AUX_SC1);
            SB();
            const float m0 = __uint_as_float(ml.x), l0 = __uint_as_float(ml.y), m1 = __uint_as_float(ml.z), l1 = __uint_as_float(ml.w);
            const float M = wave_max(fmaxf(m0, m1));
            const float w0 = __builtin_amdgcn_exp2f(m0 - M), w1_ = __builtin_amdgcn_exp2f(m1 - M);
            const float Lsum = wave_sum(w0 * l0 + w1_ * l1);
            const float inv = 1.f / Lsum;
            for (int it = 0; it < SPIN; ++it) { CFENCE();
                u32 bad = 0;
#pragma unroll
                for (int j = 0; j < 8; ++j) if (anyl(pois3(ov[j]))) bad |= 1u << j;
                if (!bad) break;
#pragma unroll
                for (int j = 0; j < 8; ++j) if (bad & (1u << j)) ov[j] = ld3(rws, obase + j * 768, AUX_SC1);
            }
            STAMP(6);
            float a[6];
#pragma unroll
            for (int i = 0; i < 6; ++i) a[i] = 0.f;
#pragma unroll
            for (int j = 0; j < 8; ++j) {
                const float wa = __uint_as_float(__builtin_amdgcn_readlane(__float_as_uint(w0), w * 8 + j));
                const float wb = __uint_as_float(__builtin_amdgcn_readlane(__float_as_uint(w1_), w * 8 + j));
                const float ww = (lane >> 5) ? wb : wa;
                float f[6]; unp6(ov[j], f);
#pragma unroll
                for (int i = 0; i < 6; ++i) a[i] += ww * f[i];
            }
#pragma unroll
            for (int i = 0; i < 3; ++i) red[w * 192 + 6 * (lane & 31) + 3 * (lane >> 5) + i] = rs32(a[i], a[i + 3]) * inv;
            BAR();
            LAS float* cl = (LAS float*)(L + w * 128);
            if (lane < 24) {
                float s = 0.f;
#pragma unroll
                for (int i = 0; i < 8; ++i) s += red[i * 192 + w * 24 + lane];
                cl[(lane & 1) * 12 + (lane >> 1)] = s;
            }
            LGKM0();
            float cv[12];
#pragma unroll
            for (int i = 0; i < 3; ++i) { const f32x4 u = *(LAS f32x4*)(cl + (lane >> 5) * 12 + i * 4); cv[i * 4] = u.x; cv[i * 4 + 1] = u.y; cv[i * 4 + 2] = u.z; cv[i * 4 + 3] = u.w; }
#pragma unroll
            for (int i = 0; i < 6; ++i) a[i] = 0.f;
#pragma unroll
            for (int j = 0; j < 12; ++j) {
                float f[6]; unp6(vw[j], f);
#pragma unroll
                for (int i = 0; i < 6; ++i) a[i] += cv[j] * f[i];
            }
            BAR();
#pragma unroll
            for (int i = 0; i < 3; ++i) red[w * 192 + 6 * (lane & 31) + 3 * (lane >> 5) + i] = rs32(a[i], a[i + 3]);
            BAR();
            if (w == 0 && lane < 48) {
                f32x4 s = {0.f, 0.f, 0.f, 0.f};
#pragma unroll
                for (int i = 0; i < 8; ++i) s += *(LAS f32x4*)(red + i * 192 + lane * 4);
                st4(rws, OFF_E5 + (cq * 6144 + h * 192) * 4 + lane * 16, u32x4{canonf(s.x), canonf(s.y), canonf(s.z), canonf(s.w)});
            }
        }
        STAMP(7);
        if (xi < 48) {
            const rsrc_t r1 = mkrs(w1, DM * DFF * 2), r2 = mkrs(w2, DFF * DM * 2);
            u32x4 f1[5], f2a[4]; u32 f2b[4];
            {
                const u32 b1 = (u32)((w * 80 + (lane >> 2)) * DFF + 32 * xi + 8 * (lane & 3)) * 2;
#pragma unroll
                for (int j = 0; j < 5; ++j) f1[j] = ld4(r1, b1 + j * 16 * DFF * 2, AUX_NT);
                const u32 b2 = (u32)((32 * xi + 4 * w) * DM) * 2;
#pragma unroll
                for (int j = 0; j < 4; ++j) { f2a[j] = ld4(r2, b2 + j * 1280 + lane * 16, AUX_NT); f2b[j] = __builtin_amdgcn_raw_buffer_load_b32(r2, b2 + j * 1280 + 1024 + lane * 4, 0, AUX_NT); }
                SB();
            }
            LAS float* gs = (LAS float*)(L + 4096);
            LAS float* xs = (LAS float*)(L + 4096 + 10240);
            LAS float* ap = (LAS float*)(L + 4096 + 10240 + 2560);
            LAS float* hs = (LAS float*)(L + 4096 + 10240 + 2560 + 7680);
            {
                u32x4 g0, g1, g2, g3;
                const int i0 = t, i1 = t + 512;
                for (int it = 0; it < SPIN; ++it) { CFENCE();
                    g0 = ld4(rws, OFF_G + i0 * 16, AUX_SC1); g1 = ld4(rws, OFF_G + 10240 + i0 * 16, AUX_SC1);
                    bool bad = pois4(g0) | pois4(g1);
                    if (t < 128) { g2 = ld4(rws, OFF_G + i1 * 16, AUX_SC1); g3 = ld4(rws, OFF_G + 10240 + i1 * 16, AUX_SC1); bad |= pois4(g2) | pois4(g3); }
                    if (!anyl(bad)) break;
                }
                *(LAS f32x4*)(gs + i0 * 4) = __builtin_bit_cast(f32x4, g0) + __builtin_bit_cast(f32x4, g1);
                if (t < 128) *(LAS f32x4*)(gs + i1 * 4) = __builtin_bit_cast(f32x4, g2) + __builtin_bit_cast(f32x4, g3);
                if (t < 320) { const u32 xx = ((const u32*)x)[t]; xs[2 * t] = bflo(xx); xs[2 * t + 1] = bfhi(xx); }
            }
            STAMP(8);
            {
                const int part = t / 160, i = t % 160;
                if (t < 480) {
                    u32x4 g[4];
                    const u32 goff = OFF_E6 + (part * 4 * 640 + i * 4) * 4;
                    for (int it = 0; it < SPIN; ++it) { CFENCE();
#pragma unroll
                        for (int j = 0; j < 4; ++j) g[j] = ld4(rws, goff + j * 2560, AUX_SC1);
                        if (!anyl(pois4(g[0]) | pois4(g[1]) | pois4(g[2]) | pois4(g[3]))) break;
                    }
                    *(LAS f32x4*)(ap + part * 640 + i * 4) = (__builtin_bit_cast(f32x4, g[0]) + __builtin_bit_cast(f32x4, g[1])) + (__builtin_bit_cast(f32x4, g[2]) + __builtin_bit_cast(f32x4, g[3]));
                }
                BAR();
                STAMP(9);
                float x1[10];
#pragma unroll
                for (int i2 = 0; i2 < 10; ++i2) { const int m = lane + 64 * i2; x1[i2] = xs[m] + gs[m] * ((ap[m] + ap[640 + m]) + ap[1280 + m]); }
                float mean, rstd; ln_stats(x1, mean, rstd);
                BAR();
                if (w == 0) {
#pragma unroll
                    for (int i2 = 0; i2 < 10; ++i2) xs[lane + 64 * i2] = x1[i2];
                }
                if (w == 1) {
#pragma unroll
                    for (int i2 = 0; i2 < 10; ++i2) { const int m = lane + 64 * i2; hs[m] = (x1[i2] - mean) * rstd * (1.f + gs[1280 + m]) + gs[640 + m]; }
                }
                BAR();
                float a[8];
#pragma unroll
                for (int i2 = 0; i2 < 8; ++i2) a[i2] = 0.f;
#pragma unroll
                for (int j = 0; j < 5; ++j) {
                    float f[8]; unp8(f1[j], f); const float hv = hs[w * 80 + j * 16 + (lane >> 2)];
#pragma unroll
                    for (int i2 = 0; i2 < 8; ++i2) a[i2] += hv * f[i2];
                }
                float v = rscat8(a, lane);
                v += dpp<0x104>(v);
                const int e = ((lane >> 5) & 1) * 4 + ((lane >> 4) & 1) * 2 + ((lane >> 3) & 1);
                if (!(lane & 4)) red[w * 32 + (lane & 3) * 8 + e] = v;
                BAR();
                float hid[4];
#pragma unroll
                for (int j = 0; j < 4; ++j) {
                    float s = 0.f;
#pragma unroll
                    for (int i2 = 0; i2 < 8; ++i2) s += red[i2 * 32 + 4 * w + j];
                    hid[j] = fmaxf(s, 0.f);
                }
                float b8[8], b2[2];
#pragma unroll
                for (int i2 = 0; i2 < 8; ++i2) b8[i2] = 0.f;
                b2[0] = b2[1] = 0.f;
#pragma unroll
                for (int j = 0; j < 4; ++j) {
                    float f[8]; unp8(f2a[j], f);
#pragma unroll
                    for (int i2 = 0; i2 < 8; ++i2) b8[i2] += hid[j] * f[i2];
                    b2[0] += hid[j] * bflo(f2b[j]); b2[1] += hid[j] * bfhi(f2b[j]);
                }
                LAS float* fr = (LAS float*)(L + 32768);
                *(LAS f32x4*)(fr + w * 640 + lane * 8) = f32x4{b8[0], b8[1], b8[2], b8[3]};
                *(LAS f32x4*)(fr + w * 640 + lane * 8 + 4) = f32x4{b8[4], b8[5], b8[6], b8[7]};
                *(LAS f32x2*)(fr + w * 640 + 512 + lane * 2) = f32x2{b2[0], b2[1]};
                BAR();
                if (t < 160) {
                    f32x4 s = {0.f, 0.f, 0.f, 0.f};
#pragma unroll
                    for (int i2 = 0; i2 < 8; ++i2) s += *(LAS f32x4*)(fr + i2 * 640 + t * 4);
                    st4(rws, OFF_E7 + xi * 2560 + t * 16, u32x4{canonf(s.x), canonf(s.y), canonf(s.z), canonf(s.w)});
                }
            }
            STAMP(10);
            if (xi < 40 && w < 3) {
                u32x4 g;
                const u32 goff = OFF_E7 + (t >> 2) * 2560 + (16 * xi + 4 * (t & 3)) * 4;
                for (int it = 0; it < SPIN; ++it) { CFENCE();
                    g = ld4(rws, goff, AUX_SC1);
                    if (!anyl(pois4(g))) break;
                }
                f32x4 s = __builtin_bit_cast(f32x4, g);
#pragma unroll
                for (int i = 0; i < 4; ++i) {
                    float v = s[i];
                    v += dpp<0x104>(v);
                    v += dpp<0x128>(v);
                    v = rs16(v, v); v = rs32(v, v);
                    s[i] = v;
                }
                if (lane < 4) *(LAS f32x4*)(red + 512 + w * 16 + lane * 4) = s;
            }
            if (xi < 40) {
                BAR();
                if (t < 8) {
                    const int m = 16 * xi + 2 * t;
                    const float s0 = (red[512 + 2 * t] + red[512 + 16 + 2 * t]) + red[512 + 32 + 2 * t];
                    const float s1 = (red[512 + 2 * t + 1] + red[512 + 16 + 2 * t + 1]) + red[512 + 32 + 2 * t + 1];
                    ((u32*)out)[m >> 1] = cvtpk(xs[m] + gs[1920 + m] * s0, xs[m + 1] + gs[1920 + m + 1] * s1);
                }
                if (xi == 0 && t == 0) __hip_atomic_store((GAS u64*)(ws + OFF_EPOCH), epoch + 1ull, __ATOMIC_RELAXED, __HIP_MEMORY_SCOPE_AGENT);
            }
            STAMP(11);
        }
    } else {
        const int yi = bid - 128, h = yi & 31, mg = yi >> 5;
        const rsrc_t rq = mkrs(wq, DM * 6144 * 2);
        u32x3 qw[10];
        {
            const u32 base = (u32)(((mg * 160 + w * 20 + (lane >> 5)) * 32 + h) * 192 + 6 * (lane & 31)) * 2;
#pragma unroll
            for (int j = 0; j < 10; ++j) qw[j] = ld3(rq, base + j * 2 * 12288, AUX_NT);
            SB();
        }
        {
            const int seg0 = w * 16;
#pragma unroll
            for (int j = 0; j < 6; ++j) {
                const int idx = j * 64 + lane, seg = seg0 + idx / 24, ch = idx % 24;
                st4(rws, OFF_O + (u32)(seg * 128 + yi) * 384 + ch * 16, PZ);
            }
            if (w == 0 && lane < 32) st2(rws, OFF_ML + lane * 1024 + yi * 8, u32x2{POISON, POISON});
            if (w == 1 && lane < 16 && yi < 120) st4(rws, OFF_E6 + ((yi % 12) * 640 + (yi / 12) * 64) * 4 + lane * 16, PZ);
            SB();
        }
        const u64 tag = (epoch + 1ull) * TAGMUL;
        const u32 tlo = (u32)tag, thi = (u32)(tag >> 32);
        {
            const int rg = lane >> 4, g = lane & 15;
            const bool act = g < 14;
            const u32 goff = g < 10 ? OFF_E1 + (rg * 320 + mg * 80 + w * 10 + g) * 16 : OFF_PF + (w * 16 + rg * 4 + (g - 10)) * 16;
            u32x4 gv = {0u, 0u, tlo, thi};
            for (int it = 0; it < SPIN; ++it) { CFENCE();
                if (act) gv = ld4(rws, goff, AUX_SC1);
                if (!anyl((gv.z != tlo) | (gv.w != thi))) break;
            }
            STAMP(1);
            float s = rs32(__uint_as_float(gv.x), __uint_as_float(gv.y));
            s = rs16(s, s);
            float a[6];
#pragma unroll
            for (int i = 0; i < 6; ++i) a[i] = 0.f;
#define QSTEP(j) { const float hv = dpp<0x150 + j>(s); float f[6]; unp6(qw[j], f); _Pragma("unroll") for (int i = 0; i < 6; ++i) a[i] += hv * f[i]; }
            QSTEP(0) QSTEP(1) QSTEP(2) QSTEP(3) QSTEP(4) QSTEP(5) QSTEP(6) QSTEP(7) QSTEP(8) QSTEP(9)
#undef QSTEP
#pragma unroll
            for (int i = 0; i < 3; ++i) red[w * 192 + 6 * (lane & 31) + 3 * (lane >> 5) + i] = rs32(a[i], a[i + 3]);
            DRAIN();
            BAR();
            if (t < 96) {
                f32x2 s2 = {0.f, 0.f};
#pragma unroll
                for (int i = 0; i < 8; ++i) s2 += *(LAS f32x2*)(red + i * 192 + t * 2);
                st4(rws, OFF_E2 + ((mg * 32 + h) * 96 + t) * 16, u32x4{__float_as_uint(s2.x), __float_as_uint(s2.y), tlo, thi});
            }
            if (t == 128) st4(rws, OFF_PF + bid * 16, u32x4{0u, 0u, tlo, thi});
        }
        STAMP(2);
        {
            const unsigned char* src = (const unsigned char*)ckv + (size_t)yi * 64 * 1536;
#pragma unroll
            for (int j = 0; j < 12; ++j) {
                const int idx = (w * 12 + j) * 64 + lane, row = idx / 96, p = idx % 96;
                const int ch = (p & ~15) | ((p ^ row) & 15);
                __builtin_amdgcn_global_load_lds((const GAS u32*)(src + row * 1536 + ch * 16), (LAS u32*)(L + L_KV + (w * 12 + j) * 1024), 16, 0, AUX_NT);
            }
            SB();
        }
        const int kq = w >> 1, ht = w & 1;
        u32x4 qf[12];
        {
            const u32 qoff = OFF_E3 + (u32)((ht * 16 + (lane & 15)) * 4 + kq) * 768 + (lane >> 4) * 16;
            for (int it = 0; it < SPIN; ++it) { CFENCE();
                qf[0] = ld4(rws, qoff, AUX_SC1);
                if (!anyl(pois4(qf[0]))) break;
            }
            STAMP(3);
#pragma unroll
            for (int j = 1; j < 12; ++j) qf[j] = ld4(rws, qoff + (j % 6) * 64 + (j / 6) * 384, AUX_SC1);
            SB();
            for (int it = 0; it < SPIN; ++it) { CFENCE();
                u32 bad = 0;
#pragma unroll
                for (int j = 1; j < 12; ++j) if (anyl(pois4(qf[j]))) bad |= 1u << j;
                if (!bad) break;
#pragma unroll
                for (int j = 1; j < 12; ++j) if (bad & (1u << j)) qf[j] = ld4(rws, qoff + (j % 6) * 64 + (j / 6) * 384, AUX_SC1);
            }
        }
        DRAIN();
        BAR();
        STAMP(4);
        {
            const int r = lane & 15;
            const int keyr = 8 * (r >> 3) + 2 * (r & 3) + ((r >> 2) & 1);
            f32x4 acc[4];
#pragma unroll
            for (int kt = 0; kt < 4; ++kt) acc[kt] = f32x4{0.f, 0.f, 0.f, 0.f};
#pragma unroll
            for (int ks = 0; ks < 6; ++ks) {
                const int ch = kq * 24 + ks * 4 + (lane >> 4);
                const int p = (ch & ~15) | ((ch ^ keyr) & 15);
#pragma unroll
                for (int kt = 0; kt < 4; ++kt) {
                    const u32x4 a = *(LAS u32x4*)(L + L_KV + (kt * 16 + keyr) * 1536 + p * 16);
                    acc[kt] = mfma(a, qf[ks], acc[kt]);
                    acc[kt] = mfma(a, qf[6 + ks], acc[kt]);
                }
            }
            LAS float* sp = (LAS float*)(L + L_SP);
#pragma unroll
            for (int kt = 0; kt < 4; ++kt) *(LAS f32x4*)(sp + (kq * 32 + ht * 16 + (lane & 15)) * 68 + kt * 16 + 4 * (lane >> 4)) = acc[kt];
        }
        BAR();
        {
            LAS float* sp = (LAS float*)(L + L_SP);
            const int hd = t >> 4, qd = t & 15;
            f32x4 s = *(LAS f32x4*)(sp + hd * 68 + qd * 4);
#pragma unroll
            for (int k = 1; k < 4; ++k) s += *(LAS f32x4*)(sp + (k * 32 + hd) * 68 + qd * 4);
            const float m = row_max(fmaxf(fmaxf(s.x, s.y), fmaxf(s.z, s.w)));
            const float p0 = __builtin_amdgcn_exp2f(s.x - m), p1 = __builtin_amdgcn_exp2f(s.y - m), p2 = __builtin_amdgcn_exp2f(s.z - m), p3 = __builtin_amdgcn_exp2f(s.w - m);
            const u32 b01 = cvtpk(p0, p1), b23 = cvtpk(p2, p3);
            const float l = row_sum((bflo(b01) + bfhi(b01)) + (bflo(b23) + bfhi(b23)));
            *(LAS u32x2*)(L + L_P + hd * 144 + qd * 8) = u32x2{b01, b23};
            if (qd == 0) st2(rws, OFF_ML + hd * 1024 + yi * 8, u32x2{canonf(m), canonf(l)});
        }
        BAR();
        STAMP(5);
        {
            u32x4 pb[2][2];
#pragma unroll
            for (int hh = 0; hh < 2; ++hh)
#pragma unroll
                for (int k2 = 0; k2 < 2; ++k2) pb[hh][k2] = *(LAS u32x4*)(L + L_P + (hh * 16 + (lane & 15)) * 144 + k2 * 64 + (lane >> 4) * 16);
            f32x4 acc[6][2];
#pragma unroll
            for (int ct = 0; ct < 6; ++ct) { acc[ct][0] = f32x4{0.f, 0.f, 0.f, 0.f}; acc[ct][1] = f32x4{0.f, 0.f, 0.f, 0.f}; }
            const int g = lane >> 4, e = (lane >> 2) & 3, pp = lane & 3;
#pragma unroll
            for (int ct = 0; ct < 6; ++ct) {
                const int ch = w * 12 + ct * 2 + (pp >> 1);
#pragma unroll
                for (int k2 = 0; k2 < 2; ++k2) {
                    const int ka = k2 * 32 + 8 * g + 2 * e, kb = ka + 1;
                    const int pa = (ch & ~15) | ((ch ^ ka) & 15), pbx = (ch & ~15) | ((ch ^ kb) & 15);
                    const s16x4 t0 = __builtin_amdgcn_ds_read_tr16_b64_v4i16((LAS s16x4*)(L + L_KV + ka * 1536 + pa * 16 + (pp & 1) * 8));
                    const s16x4 t1 = __builtin_amdgcn_ds_read_tr16_b64_v4i16((LAS s16x4*)(L + L_KV + kb * 1536 + pbx * 16 + (pp & 1) * 8));
                    const u32x2 a0 = __builtin_bit_cast(u32x2, t0), a1 = __builtin_bit_cast(u32x2, t1);
                    const u32x4 a = {a0.x, a0.y, a1.x, a1.y};
                    acc[ct][0] = mfma(a, pb[0][k2], acc[ct][0]);
                    acc[ct][1] = mfma(a, pb[1][k2], acc[ct][1]);
                }
            }
            const int ocq = w >> 1;
#pragma unroll
            for (int hh = 0; hh < 2; ++hh)
#pragma unroll
                for (int cp = 0; cp < 3; ++cp) {
                    const f32x4 A = acc[2 * cp][hh], B = acc[2 * cp + 1][hh];
                    const u32 ax = cvtpk(A.x, A.y), ay = cvtpk(A.z, A.w), bx = cvtpk(B.x, B.y), by = cvtpk(B.z, B.w);
                    auto r0 = __builtin_amdgcn_permlane16_swap(ax, bx, false, false);
                    auto r1 = __builtin_amdgcn_permlane16_swap(ay, by, false, false);
                    const u32x4 v = {canon(r0[0]), canon(r1[0]), canon(r0[1]), canon(r1[1])};
                    const int c = (w & 1) * 96 + (2 * cp + (g & 1)) * 16 + (g >> 1) * 8;
                    st4(rws, OFF_O + (u32)(((hh * 16 + (lane & 15)) * 4 + ocq) * 128 + yi) * 384 + c * 2, v);
                }
        }
        STAMP(6);
        if (yi < 120) {
            const int rg = yi % 12, cs = yi / 12;
            const rsrc_t ro = mkrs(wo, 6144 * DM * 2);
            u32x4 ow[8];
            const u32 base = (u32)((rg * 512 + w * 64 + (lane >> 3) * 8) * DM + cs * 64 + (lane & 7) * 8) * 2;
#pragma unroll
            for (int j = 0; j < 8; ++j) ow[j] = ld4(ro, base + j * 1280, AUX_NT);
            SB();
            u32x4 g;
            const u32 goff = OFF_E5 + ((lane >> 4) * 6144 + rg * 512 + w * 64 + (lane & 15) * 4) * 4;
            for (int it = 0; it < SPIN; ++it) { CFENCE();
                g = ld4(rws, goff, AUX_SC1);
                if (!anyl(pois4(g))) break;
            }
            STAMP(7);
            f32x4 s = __builtin_bit_cast(f32x4, g);
#pragma unroll
            for (int i = 0; i < 4; ++i) { float v = s[i]; v = rs16(v, v); v = rs32(v, v); s[i] = v; }
            LAS float* cs_ = (LAS float*)(L + L_SP + w * 256);
            if (lane < 16) *(LAS f32x4*)(cs_ + lane * 4) = s;
            LGKM0();
            const f32x4 c0 = *(LAS f32x4*)(cs_ + (lane >> 3) * 8), c1 = *(LAS f32x4*)(cs_ + (lane >> 3) * 8 + 4);
            const float cv[8] = {c0.x, c0.y, c0.z, c0.w, c1.x, c1.y, c1.z, c1.w};
            float a[8];
#pragma unroll
            for (int i = 0; i < 8; ++i) a[i] = 0.f;
#pragma unroll
            for (int j = 0; j < 8; ++j) {
                float f[8]; unp8(ow[j], f);
#pragma unroll
                for (int i = 0; i < 8; ++i) a[i] += cv[j] * f[i];
            }
            const float v = rscat8(a, lane);
            const int e = ((lane >> 5) & 1) * 4 + ((lane >> 4) & 1) * 2 + ((lane >> 3) & 1);
            red[w * 64 + (lane & 7) * 8 + e] = v;
            BAR();
            if (w == 0 && lane < 16) {
                f32x4 s4 = {0.f, 0.f, 0.f, 0.f};
#pragma unroll
                for (int i = 0; i < 8; ++i) s4 += *(LAS f32x4*)(red + i * 64 + lane * 4);
                st4(rws, OFF_E6 + (rg * 640 + cs * 64) * 4 + lane * 16, u32x4{canonf(s4.x), canonf(s4.y), canonf(s4.z), canonf(s4.w)});
            }
            STAMP(8);
        }
    }
}

extern "C" void kernel_launch(void* const* d_in, const int* in_sizes, int n_in, void* d_out, int out_size, void* d_ws, size_t ws_size, hipStream_t stream) {
    (void)in_sizes; (void)n_in; (void)out_size; (void)ws_size;
    hipFuncSetAttribute((const void*)adaln_mla_mlp_kernel, hipFuncAttributeMaxDynamicSharedMemorySize, LDS_BYTES);
    adaln_mla_mlp_kernel<<<NWG, NT, LDS_BYTES, stream>>>(
        (const u16*)d_in[0], (const u16*)d_in[1], (const u16*)d_in[2], (const u16*)d_in[3], (const u16*)d_in[4], (const u16*)d_in[5],
        (const u16*)d_in[7], (const u16*)d_in[8], (const u16*)d_in[9], (const u16*)d_in[10], (u16*)d_out, (unsigned char*)d_ws);
}
```

```cpp
#define AMP_WG 0
#define AMP_FROM 0
#define AMP_TO 11
#define AMP_K 1
#define KAMP 8
#include <hip/hip_runtime.h>
#include <stdint.h>

typedef unsigned u32;
typedef unsigned long long u64;
typedef unsigned short u16;
typedef u32 u32x2 __attribute__((ext_vector_type(2)));
typedef u32 u32x3 __attribute__((ext_vector_type(3)));
typedef u32 u32x4 __attribute__((ext_vector_type(4)));
typedef float f32x2 __attribute__((ext_vector_type(2)));
typedef float f32x4 __attribute__((ext_vector_type(4)));
typedef short bf16x8 __attribute__((ext_vector_type(8)));
typedef short s16x4 __attribute__((ext_vector_type(4)));
typedef __amdgpu_buffer_rsrc_t rsrc_t;
#define LAS __attribute__((address_space(3)))
#define GAS __attribute__((address_space(1)))

constexpr int NH = 32, SK = 8192, HD = 192, DM = 640, DC = 768, DFF = 1536;
constexpr int NWG = 256, NT = 512;

constexpr u32 OFF_EPOCH = 0;
constexpr u32 OFF_PF = 256;
constexpr u32 OFF_E1 = OFF_PF + 256 * 16;
constexpr u32 OFF_E2 = OFF_E1 + 4 * 320 * 16;
constexpr u32 OFF_E3 = OFF_E2 + 4 * 32 * 96 * 16;
constexpr u32 OFF_ML = OFF_E3 + 32 * 4 * 768;
constexpr u32 OFF_E5 = OFF_ML + 32 * 128 * 8;
constexpr u32 OFF_E6 = OFF_E5 + 4 * 6144 * 4;
constexpr u32 OFF_G = OFF_E6 + 12 * 640 * 4;
constexpr u32 OFF_E7 = OFF_G + 2 * 2560 * 4;
constexpr u32 OFF_O = OFF_E7 + 48 * 640 * 4;
constexpr u32 WS_BYTES = OFF_O + 32 * 4 * 128 * 384;
constexpr u32 OFF_DIAG = WS_BYTES;
static_assert(OFF_O % 128 == 0 && OFF_E3 % 128 == 0 && OFF_E5 % 128 == 0, "alignment");

constexpr int L_KV = 0;
constexpr int L_SP = 98304;
constexpr int L_P = L_SP + 4 * 32 * 68 * 4;
constexpr int L_RED = L_P + 32 * 144;
constexpr int LDS_BYTES = L_RED + 8192;
constexpr int SPIN = 1 << 16;
constexpr u32 POISON = 0xFFFFFFFFu;
constexpr int AUX_SC1 = 16, AUX_NT = 2;
constexpr u64 TAGMUL = 0x9E3779B97F4A7C15ull;

__device__ __forceinline__ float bflo(u32 v) { return __uint_as_float(v << 16); }
__device__ __forceinline__ float bfhi(u32 v) { return __uint_as_float(v & 0xFFFF0000u); }
__device__ __forceinline__ float bf1(u16 v) { return __uint_as_float((u32)v << 16); }
typedef __bf16 bf16x2_t __attribute__((ext_vector_type(2)));
__device__ __forceinline__ u32 cvtpk(float lo, float hi) { f32x2 v = {lo, hi}; return __builtin_bit_cast(u32, __builtin_convertvector(v, bf16x2_t)); }
__device__ __forceinline__ u32 canon(u32 v) { return v < 0xFFFFFFFEu ? v : 0xFFFFFFFEu; }
__device__ __forceinline__ u32 canonf(float v) { return canon(__float_as_uint(v)); }
__device__ __forceinline__ rsrc_t mkrs(const void* p, u32 bytes) { return __builtin_amdgcn_make_buffer_rsrc((void*)p, 0, bytes, 0x00020000); }
__device__ __forceinline__ u32x4 ld4(rsrc_t r, u32 off, int aux) { return aux == AUX_SC1 ? __builtin_amdgcn_raw_buffer_load_b128(r, off, 0, AUX_SC1) : __builtin_amdgcn_raw_buffer_load_b128(r, off, 0, AUX_NT); }
__device__ __forceinline__ u32x3 ld3(rsrc_t r, u32 off, int aux) { return aux == AUX_SC1 ? __builtin_amdgcn_raw_buffer_load_b96(r, off, 0, AUX_SC1) : __builtin_amdgcn_raw_buffer_load_b96(r, off, 0, AUX_NT); }
__device__ __forceinline__ void st4(rsrc_t r, u32 off, u32x4 v) { __builtin_amdgcn_raw_buffer_store_b128(v, r, off, 0, AUX_SC1); }
__device__ __forceinline__ void st2(rsrc_t r, u32 off, u32x2 v) { __builtin_amdgcn_raw_buffer_store_b64(v, r, off, 0, AUX_SC1); }
#define CFENCE() asm volatile("" ::: "memory")
#define SB() __builtin_amdgcn_sched_barrier(0)
#define BAR() asm volatile("s_waitcnt lgkmcnt(0)\n\ts_barrier" ::: "memory")
#define LGKM0() asm volatile("s_waitcnt lgkmcnt(0)" ::: "memory")
#define DRAIN() asm volatile("s_waitcnt vmcnt(0)" ::: "memory")

template <int CTRL> __device__ __forceinline__ float dpp(float x) { return __int_as_float(__builtin_amdgcn_update_dpp(0, __float_as_int(x), CTRL, 0xf, 0xf, true)); }
__device__ __forceinline__ float rs32(float a, float b) { auto r = __builtin_amdgcn_permlane32_swap(__float_as_uint(a), __float_as_uint(b), false, false); return __uint_as_float(r[0]) + __uint_as_float(r[1]); }
__device__ __forceinline__ float rs16(float a, float b) { auto r = __builtin_amdgcn_permlane16_swap(__float_as_uint(a), __float_as_uint(b), false, false); return __uint_as_float(r[0]) + __uint_as_float(r[1]); }
__device__ __forceinline__ float row_sum(float x) { x += dpp<0x128>(x); x += dpp<0x124>(x); x += dpp<0x122>(x); x += dpp<0x121>(x); return x; }
__device__ __forceinline__ float row_max(float x) { x = fmaxf(x, dpp<0x128>(x)); x = fmaxf(x, dpp<0x124>(x)); x = fmaxf(x, dpp<0x122>(x)); x = fmaxf(x, dpp<0x121>(x)); return x; }
__device__ __forceinline__ float wave_sum(float x) { x = row_sum(x); x = rs16(x, x); x = rs32(x, x); return x; }
__device__ __forceinline__ float wave_max(float x) {
    x = row_max(x);
    { auto r = __builtin_amdgcn_permlane16_swap(__float_as_uint(x), __float_as_uint(x), false, false); x = fmaxf(__uint_as_float(r[0]), __uint_as_float(r[1])); }
    { auto r = __builtin_amdgcn_permlane32_swap(__float_as_uint(x), __float_as_uint(x), false, false); x = fmaxf(__uint_as_float(r[0]), __uint_as_float(r[1])); }
    return x;
}
__device__ __forceinline__ float rscat8(const float (&a)[8], int lane) {
    float r0 = rs32(a[0], a[4]), r1 = rs32(a[1], a[5]), r2 = rs32(a[2], a[6]), r3 = rs32(a[3], a[7]);
    float s0 = rs16(r0, r2), s1 = rs16(r1, r3);
    float t0 = s0 + dpp<0x128>(s0), t1 = s1 + dpp<0x128>(s1);
    return (lane & 8) ? t1 : t0;
}
__device__ __forceinline__ bool pois4(u32x4 v) { u32 m = max(max(v.x, v.y), max(v.z, v.w)); return m == POISON; }
__device__ __forceinline__ bool pois3(u32x3 v) { u32 m = max(max(v.x, v.y), v.z); return m == POISON; }
__device__ __forceinline__ bool anyl(bool b) { return __builtin_amdgcn_ballot_w64(b) != 0ull; }
__device__ __forceinline__ f32x4 mfma(u32x4 a, u32x4 b, f32x4 c) { return __builtin_amdgcn_mfma_f32_16x16x32_bf16(__builtin_bit_cast(bf16x8, a), __builtin_bit_cast(bf16x8, b), c, 0, 0, 0); }
__device__ __forceinline__ float silu(float v) { return v / (1.f + __expf(-v)); }
__device__ __forceinline__ void unp8(u32x4 v, float (&f)[8]) { f[0] = bflo(v.x); f[1] = bfhi(v.x); f[2] = bflo(v.y); f[3] = bfhi(v.y); f[4] = bflo(v.z); f[5] = bfhi(v.z); f[6] = bflo(v.w); f[7] = bfhi(v.w); }
__device__ __forceinline__ void unp6(u32x3 v, float (&f)[6]) { f[0] = bflo(v.x); f[1] = bfhi(v.x); f[2] = bflo(v.y); f[3] = bfhi(v.y); f[4] = bflo(v.z); f[5] = bfhi(v.z); }

#define STAMP(i) do { st[i] = __builtin_amdgcn_s_memrealtime(); } while (0)

__device__ __forceinline__ void ln_stats(const float (&v)[10], float& mean, float& rstd) {
    float s = 0.f;
#pragma unroll
    for (int i = 0; i < 10; ++i) s += v[i];
    mean = wave_sum(s) * (1.f / 640.f);
    float q = 0.f;
#pragma unroll
    for (int i = 0; i < 10; ++i) { float d = v[i] - mean; q += d * d; }
    float var = wave_sum(q) * (1.f / 640.f);
    rstd = 1.f / sqrtf(var + 1e-5f);
}

__global__ __launch_bounds__(NT, 2) void adaln_mla_mlp_kernel(
    const u16* __restrict__ x_, const u16* __restrict__ ckv_, const u16* __restrict__ wq_, const u16* __restrict__ wuk_,
    const u16* __restrict__ wuv_, const u16* __restrict__ wo_, const u16* __restrict__ w1_, const u16* __restrict__ w2_,
    const u16* __restrict__ cc_, const u16* __restrict__ wmod_, u16* __restrict__ out_, unsigned char* __restrict__ ws_) {
    typedef const __attribute__((address_space(4))) u64* kap;
    kap ka = (kap)__builtin_amdgcn_kernarg_segment_ptr();
    u64 tA, tB, tC, d0, d1;
    asm volatile("s_memrealtime %0\n s_waitcnt lgkmcnt(0)" : "=s"(tA));
    asm volatile("s_load_dwordx2 %0, %1, 0x58\n s_waitcnt lgkmcnt(0)" : "=s"(d0) : "s"(ka));
    asm volatile("s_memrealtime %0\n s_waitcnt lgkmcnt(0)" : "=s"(tB));
    asm volatile("s_load_dwordx2 %0, %1, 0x10\n s_waitcnt lgkmcnt(0)" : "=s"(d1) : "s"(ka));
    asm volatile("s_memrealtime %0\n s_waitcnt lgkmcnt(0)" : "=s"(tC));
    asm volatile("" : "+s"(ka) : "s"(d0), "s"(d1));
    const u16* x = (const u16*)ka[0]; const u16* ckv = (const u16*)ka[1]; const u16* wq = (const u16*)ka[2]; const u16* wuk = (const u16*)ka[3];
    const u16* wuv = (const u16*)ka[4]; const u16* wo = (const u16*)ka[5]; const u16* w1 = (const u16*)ka[6]; const u16* w2 = (const u16*)ka[7];
    const u16* cc = (const u16*)ka[8]; const u16* wmod = (const u16*)ka[9]; u16* out = (u16*)ka[10]; unsigned char* ws = (unsigned char*)ka[11];
    extern __shared__ __attribute__((aligned(16))) unsigned char smem[];
    LAS unsigned char* L = (LAS unsigned char*)smem;
    const int t = threadIdx.x, lane = t & 63, w = __builtin_amdgcn_readfirstlane(t >> 6);
    const int bid = blockIdx.x;
    const rsrc_t rws = mkrs(ws, WS_BYTES);
    u64 st[16];
#pragma unroll
    for (int i = 0; i < 16; ++i) st[i] = 0;
    STAMP(0);
    const u64 epoch = __hip_atomic_load((GAS u64*)(ws + OFF_EPOCH), __ATOMIC_RELAXED, __HIP_MEMORY_SCOPE_AGENT);
    const u32x4 PZ = {POISON, POISON, POISON, POISON};
    LAS float* red = (LAS float*)(L + L_RED);

    if (bid < 128) {
        const int xi = bid, h = xi & 31, cq = xi >> 5;
        const rsrc_t rmod = mkrs(wmod, DM * 3840 * 2);
        const bool isM = xi < 80;
        u32x4 mw0, mw1, mw2; u16 c0 = 0, c1 = 0, c2 = 0; u32 xr[5]; u16 xm = 0;
        int m_rg = 0, m_s = 0;
        if (isM) {
            const int x8 = xi & 7, b = (xi >> 3) & 1, slot = xi >> 4, qq = slot * 8 + x8;
            m_rg = qq / 10; m_s = 2 * (qq % 10) + b;
            const int chunk = t & 7, rsub = t >> 3;
            const int col = chunk < 4 ? 32 * m_s + 8 * chunk : 640 + 32 * m_s + 8 * (chunk - 4);
            const int row0 = m_rg * 160 + rsub;
            c0 = cc[row0]; c1 = cc[row0 + 64]; if (w < 4) c2 = cc[row0 + 128];
            if (w == 0) {
#pragma unroll
                for (int i = 0; i < 5; ++i) xr[i] = ((const u32*)x)[lane + 64 * i];
                xm = x[32 * m_s + (lane & 31)];
            }
            const u32 off = (u32)(row0 * 3840 + col) * 2;
            mw0 = ld4(rmod, off, AUX_NT); mw1 = ld4(rmod, off + 64 * 7680, AUX_NT);
            if (w < 4) mw2 = ld4(rmod, off + 128 * 7680, AUX_NT);
            SB();
        }
        if (w == 7) {
            if (lane < 48) { st4(rws, OFF_E3 + (h * 4 + cq) * 768 + lane * 16, PZ); st4(rws, OFF_E5 + (cq * 6144 + h * 192) * 4 + lane * 16, PZ); }
            if (xi < 48) {
                st4(rws, OFF_E7 + xi * 2560 + lane * 16, PZ); st4(rws, OFF_E7 + xi * 2560 + 1024 + lane * 16, PZ);
                if (lane < 32) st4(rws, OFF_E7 + xi * 2560 + 2048 + lane * 16, PZ);
            } else if (lane < 16) {
                const int gi = xi - 48;
                st4(rws, OFF_G + ((gi / 40) * 2560 + (gi % 40) * 64) * 4 + lane * 16, PZ);
            }
            SB();
        }
        const u64 tag = (epoch + 1ull) * TAGMUL;
        const u32 tlo = (u32)tag, thi = (u32)(tag >> 32);
        if (isM) {
            float a[8], f[8];
            const float s0 = silu(bf1(c0)), s1 = silu(bf1(c1));
            unp8(mw0, f);
#pragma unroll
            for (int i = 0; i < 8; ++i) a[i] = s0 * f[i];
            unp8(mw1, f);
#pragma unroll
            for (int i = 0; i < 8; ++i) a[i] += s1 * f[i];
            if (w < 4) {
                const float s2 = silu(bf1(c2));
                unp8(mw2, f);
#pragma unroll
                for (int i = 0; i < 8; ++i) a[i] += s2 * f[i];
            }
            const float v = rscat8(a, lane);
            const int e = ((lane >> 5) & 1) * 4 + ((lane >> 4) & 1) * 2 + ((lane >> 3) & 1);
            red[w * 64 + (lane & 7) * 8 + e] = v;
            float lnx = 0.f;
            if (w == 0) {
                float xv[10];
#pragma unroll
                for (int i = 0; i < 5; ++i) { xv[2 * i] = bflo(xr[i]); xv[2 * i + 1] = bfhi(xr[i]); }
                float mean, rstd; ln_stats(xv, mean, rstd);
                lnx = (bf1(xm) - mean) * rstd;
            }
            BAR();
            if (w == 0) {
                float s = 0.f;
#pragma unroll
                for (int i = 0; i < 8; ++i) s += red[i * 64 + lane];
                auto r = __builtin_amdgcn_permlane32_swap(__float_as_uint(s), __float_as_uint(s), false, false);
                const float sa = __uint_as_float(r[0]), ca = __uint_as_float(r[1]);
                float hv = lnx * ca + sa; if (m_rg == 0) hv += lnx;
                const float hn = dpp<0xB1>(hv);
                if (lane < 32 && !(lane & 1)) st4(rws, OFF_E1 + (m_rg * 320 + m_s * 16 + (lane >> 1)) * 16, u32x4{__float_as_uint(hv), __float_as_uint(hn), tlo, thi});
            }
        }
        STAMP(1);
        if (w == 7) { DRAIN(); if (lane == 0) st4(rws, OFF_PF + bid * 16, u32x4{0u, 0u, tlo, thi}); }
        const rsrc_t ruk = mkrs(wuk, DC * 6144 * 2);
        const int tg = w >> 1, hf = w & 1;
        u32x4 aw[9];
        {
            const u32 base = (u32)(((cq * 192 + tg * 48 + (lane & 15)) * 32 + h) * 192 + hf * 96 + 8 * (lane >> 4)) * 2;
#pragma unroll
            for (int ti = 0; ti < 3; ++ti)
#pragma unroll
                for (int ki = 0; ki < 3; ++ki) aw[ti * 3 + ki] = ld4(ruk, base + ti * 16 * 12288 + ki * 64, AUX_NT);
            SB();
        }
        float qv[24];
        {
            u32x4 g[3];
            const u32 goff = OFF_E2 + (((lane >> 4) * 32 + h) * 96 + hf * 48 + (lane & 15)) * 16;
            for (int it = 0; it < SPIN; ++it) { CFENCE();
#pragma unroll
                for (int i = 0; i < 3; ++i) g[i] = ld4(rws, goff + i * 256, AUX_SC1);
                bool bad = false;
#pragma unroll
                for (int i = 0; i < 3; ++i) bad |= (g[i].z != tlo) | (g[i].w != thi);
                if (!anyl(bad)) break;
            }
            STAMP(2);
            LAS float* qs = (LAS float*)(L + w * 512);
#pragma unroll
            for (int i = 0; i < 3; ++i) {
                float s = rs32(__uint_as_float(g[i].x), __uint_as_float(g[i].y));
                s = rs16(s, s);
                qs[2 * (16 * i + (lane & 15)) + (lane >> 5)] = s;
            }
            LGKM0();
#pragma unroll
            for (int ki = 0; ki < 3; ++ki) {
                const f32x4 u0 = *(LAS f32x4*)(qs + ki * 32 + 8 * (lane >> 4)), u1 = *(LAS f32x4*)(qs + ki * 32 + 8 * (lane >> 4) + 4);
                qv[ki * 8 + 0] = u0.x; qv[ki * 8 + 1] = u0.y; qv[ki * 8 + 2] = u0.z; qv[ki * 8 + 3] = u0.w;
                qv[ki * 8 + 4] = u1.x; qv[ki * 8 + 5] = u1.y; qv[ki * 8 + 6] = u1.z; qv[ki * 8 + 7] = u1.w;
            }
        }
        {
            float acc[3];
#pragma unroll
            for (int ti = 0; ti < 3; ++ti) {
                float s = 0.f;
#pragma unroll
                for (int ki = 0; ki < 3; ++ki) {
                    float f[8]; unp8(aw[ti * 3 + ki], f);
#pragma unroll
                    for (int j = 0; j < 8; ++j) s += f[j] * qv[ki * 8 + j];
                }
                s = rs16(s, s); s = rs32(s, s); acc[ti] = s;
            }
            const int row = lane >> 4;
            const float v = row == 0 ? acc[0] : (row == 1 ? acc[1] : acc[2]);
            if (row < 3) red[hf * 192 + tg * 48 + row * 16 + (lane & 15)] = v;
            BAR();
            if (w == 0 && lane < 48) {
                const int ch = lane < 24 ? lane : lane - 24;
                const float sc = 0.07216878364870322f * 1.4426950408889634f;
                float v8[8];
#pragma unroll
                for (int i = 0; i < 2; ++i) {
                    const f32x4 p0 = *(LAS f32x4*)(red + ch * 8 + i * 4), p1 = *(LAS f32x4*)(red + 192 + ch * 8 + i * 4);
                    v8[i * 4 + 0] = (p0.x + p1.x) * sc; v8[i * 4 + 1] = (p0.y + p1.y) * sc; v8[i * 4 + 2] = (p0.z + p1.z) * sc; v8[i * 4 + 3] = (p0.w + p1.w) * sc;
                }
                u32 o[4];
#pragma unroll
                for (int i = 0; i < 4; ++i) {
                    const u32 hi = cvtpk(v8[2 * i], v8[2 * i + 1]);
                    const u32 lo = cvtpk(v8[2 * i] - bflo(hi), v8[2 * i + 1] - bfhi(hi));
                    o[i] = canon(lane < 24 ? hi : lo);
                }
                st4(rws, OFF_E3 + (h * 4 + cq) * 768 + lane * 16, u32x4{o[0], o[1], o[2], o[3]});
            }
        }
        STAMP(3);
        const rsrc_t ruv = mkrs(wuv, DC * 6144 * 2);
        u32x3 vw[12];
        {
            const u32 base = (u32)(((cq * 192 + w * 24 + (lane >> 5)) * 32 + h) * 192 + 6 * (lane & 31)) * 2;
#pragma unroll
            for (int j = 0; j < 12; ++j) vw[j] = ld3(ruv, base + j * 2 * 12288, AUX_NT);
            SB();
        }
        if (xi >= 48) {
            const int gi = xi - 48, slice = gi % 40, rh = gi / 40;
            const int chunk = t & 7, rsub = t >> 3;
            const int row0 = rh * 320 + rsub;
            u16 cv[5]; u32x4 gw[5];
#pragma unroll
            for (int j = 0; j < 5; ++j) cv[j] = cc[row0 + 64 * j];
            const u32 off = (u32)(row0 * 3840 + 1280 + 64 * slice + 8 * chunk) * 2;
#pragma unroll
            for (int j = 0; j < 5; ++j) gw[j] = ld4(rmod, off + j * 64 * 7680, AUX_NT);
            SB();
            float a[8];
#pragma unroll
            for (int i = 0; i < 8; ++i) a[i] = 0.f;
#pragma unroll
            for (int j = 0; j < 5; ++j) {
                float f[8]; unp8(gw[j], f); const float s = silu(bf1(cv[j]));
#pragma unroll
                for (int i = 0; i < 8; ++i) a[i] += s * f[i];
            }
            const float v = rscat8(a, lane);
            const int e = ((lane >> 5) & 1) * 4 + ((lane >> 4) & 1) * 2 + ((lane >> 3) & 1);
            LAS float* redg = red + 512;
            redg[w * 64 + (lane & 7) * 8 + e] = v;
            BAR();
            if (w == 0 && lane < 16) {
                f32x4 s = {0.f, 0.f, 0.f, 0.f};
#pragma unroll
                for (int i = 0; i < 8; ++i) s += *(LAS f32x4*)(redg + i * 64 + lane * 4);
                st4(rws, OFF_G + (rh * 2560 + slice * 64) * 4 + lane * 16, u32x4{canonf(s.x), canonf(s.y), canonf(s.z), canonf(s.w)});
            }
        }
        {
            if (w < 4) {
                for (int it = 0; it < SPIN; ++it) { CFENCE();
                    const u32x4 g = ld4(rws, OFF_PF + t * 16, AUX_SC1);
                    if (!anyl((g.z != tlo) | (g.w != thi))) break;
                }
            }
            BAR();
        }
        STAMP(4);
        {
            u32x4 ml;
            for (int it = 0; it < SPIN; ++it) { CFENCE();
                ml = ld4(rws, OFF_ML + h * 1024 + lane * 16, AUX_SC1);
                if (!anyl(pois4(ml))) break;
            }
            STAMP(5);
            u32x3 ov[8];
            const u32 obase = OFF_O + (u32)((h * 4 + cq) * 128 + w * 16 + (lane >> 5)) * 384 + (lane & 31) * 12;
#pragma unroll
            for (int j = 0; j < 8; ++j) ov[j] = ld3(rws, obase + j * 768, AUX_SC1);
            SB();
            const float m0 = __uint_as_float(ml.x), l0 = __uint_as_float(ml.y), m1 = __uint_as_float(ml.z), l1 = __uint_as_float(ml.w);
            const float M = wave_max(fmaxf(m0, m1));
            const float w0 = __builtin_amdgcn_exp2f(m0 - M), w1_ = __builtin_amdgcn_exp2f(m1 - M);
            const float Lsum = wave_sum(w0 * l0 + w1_ * l1);
            const float inv = 1.f / Lsum;
            for (int it = 0; it < SPIN; ++it) { CFENCE();
                u32 bad = 0;
#pragma unroll
                for (int j = 0; j < 8; ++j) if (anyl(pois3(ov[j]))) bad |= 1u << j;
                if (!bad) break;
#pragma unroll
                for (int j = 0; j < 8; ++j) if (bad & (1u << j)) ov[j] = ld3(rws, obase + j * 768, AUX_SC1);
            }
            STAMP(6);
            float a[6];
#pragma unroll
            for (int i = 0; i < 6; ++i) a[i] = 0.f;
#pragma unroll
            for (int j = 0; j < 8; ++j) {
                const float wa = __uint_as_float(__builtin_amdgcn_readlane(__float_as_uint(w0), w * 8 + j));
                const float wb = __uint_as_float(__builtin_amdgcn_readlane(__float_as_uint(w1_), w * 8 + j));
                const float ww = (lane >> 5) ? wb : wa;
                float f[6]; unp6(ov[j], f);
#pragma unroll
                for (int i = 0; i < 6; ++i) a[i] += ww * f[i];
            }
#pragma unroll
            for (int i = 0; i < 3; ++i) red[w * 192 + 6 * (lane & 31) + 3 * (lane >> 5) + i] = rs32(a[i], a[i + 3]) * inv;
            BAR();
            LAS float* cl = (LAS float*)(L + w * 128);
            if (lane < 24) {
                float s = 0.f;
#pragma unroll
                for (int i = 0; i < 8; ++i) s += red[i * 192 + w * 24 + lane];
                cl[(lane & 1) * 12 + (lane >> 1)] = s;
            }
            LGKM0();
            float cv[12];
#pragma unroll
            for (int i = 0; i < 3; ++i) { const f32x4 u = *(LAS f32x4*)(cl + (lane >> 5) * 12 + i * 4); cv[i * 4] = u.x; cv[i * 4 + 1] = u.y; cv[i * 4 + 2] = u.z; cv[i * 4 + 3] = u.w; }
#pragma unroll
            for (int i = 0; i < 6; ++i) a[i] = 0.f;
#pragma unroll
            for (int j = 0; j < 12; ++j) {
                float f[6]; unp6(vw[j], f);
#pragma unroll
                for (int i = 0; i < 6; ++i) a[i] += cv[j] * f[i];
            }
            BAR();
#pragma unroll
            for (int i = 0; i < 3; ++i) red[w * 192 + 6 * (lane & 31) + 3 * (lane >> 5) + i] = rs32(a[i], a[i + 3]);
            BAR();
            if (w == 0 && lane < 48) {
                f32x4 s = {0.f, 0.f, 0.f, 0.f};
#pragma unroll
                for (int i = 0; i < 8; ++i) s += *(LAS f32x4*)(red + i * 192 + lane * 4);
                st4(rws, OFF_E5 + (cq * 6144 + h * 192) * 4 + lane * 16, u32x4{canonf(s.x), canonf(s.y), canonf(s.z), canonf(s.w)});
            }
        }
        STAMP(7);
        if (xi < 48) {
            const rsrc_t r1 = mkrs(w1, DM * DFF * 2), r2 = mkrs(w2, DFF * DM * 2);
            u32x4 f1[5], f2a[4]; u32 f2b[4];
            {
                const u32 b1 = (u32)((w * 80 + (lane >> 2)) * DFF + 32 * xi + 8 * (lane & 3)) * 2;
#pragma unroll
                for (int j = 0; j < 5; ++j) f1[j] = ld4(r1, b1 + j * 16 * DFF * 2, AUX_NT);
                const u32 b2 = (u32)((32 * xi + 4 * w) * DM) * 2;
#pragma unroll
                for (int j = 0; j < 4; ++j) { f2a[j] = ld4(r2, b2 + j * 1280 + lane * 16, AUX_NT); f2b[j] = __builtin_amdgcn_raw_buffer_load_b32(r2, b2 + j * 1280 + 1024 + lane * 4, 0, AUX_NT); }
                SB();
            }
            LAS float* gs = (LAS float*)(L + 4096);
            LAS float* xs = (LAS float*)(L + 4096 + 10240);
            LAS float* ap = (LAS float*)(L + 4096 + 10240 + 2560);
            LAS float* hs = (LAS float*)(L + 4096 + 10240 + 2560 + 7680);
            {
                u32x4 g0, g1, g2, g3;
                const int i0 = t, i1 = t + 512;
                for (int it = 0; it < SPIN; ++it) { CFENCE();
                    g0 = ld4(rws, OFF_G + i0 * 16, AUX_SC1); g1 = ld4(rws, OFF_G + 10240 + i0 * 16, AUX_SC1);
                    bool bad = pois4(g0) | pois4(g1);
                    if (t < 128) { g2 = ld4(rws, OFF_G + i1 * 16, AUX_SC1); g3 = ld4(rws, OFF_G + 10240 + i1 * 16, AUX_SC1); bad |= pois4(g2) | pois4(g3); }
                    if (!anyl(bad)) break;
                }
                *(LAS f32x4*)(gs + i0 * 4) = __builtin_bit_cast(f32x4, g0) + __builtin_bit_cast(f32x4, g1);
                if (t < 128) *(LAS f32x4*)(gs + i1 * 4) = __builtin_bit_cast(f32x4, g2) + __builtin_bit_cast(f32x4, g3);
                if (t < 320) { const u32 xx = ((const u32*)x)[t]; xs[2 * t] = bflo(xx); xs[2 * t + 1] = bfhi(xx); }
            }
            STAMP(8);
            {
                const int part = t / 160, i = t % 160;
                if (t < 480) {
                    u32x4 g[4];
                    const u32 goff = OFF_E6 + (part * 4 * 640 + i * 4) * 4;
                    for (int it = 0; it < SPIN; ++it) { CFENCE();
#pragma unroll
                        for (int j = 0; j < 4; ++j) g[j] = ld4(rws, goff + j * 2560, AUX_SC1);
                        if (!anyl(pois4(g[0]) | pois4(g[1]) | pois4(g[2]) | pois4(g[3]))) break;
                    }
                    *(LAS f32x4*)(ap + part * 640 + i * 4) = (__builtin_bit_cast(f32x4, g[0]) + __builtin_bit_cast(f32x4, g[1])) + (__builtin_bit_cast(f32x4, g[2]) + __builtin_bit_cast(f32x4, g[3]));
                }
                BAR();
                STAMP(9);
                float x1[10];
#pragma unroll
                for (int i2 = 0; i2 < 10; ++i2) { const int m = lane + 64 * i2; x1[i2] = xs[m] + gs[m] * ((ap[m] + ap[640 + m]) + ap[1280 + m]); }
                float mean, rstd; ln_stats(x1, mean, rstd);
                BAR();
                if (w == 0) {
#pragma unroll
                    for (int i2 = 0; i2 < 10; ++i2) xs[lane + 64 * i2] = x1[i2];
                }
                if (w == 1) {
#pragma unroll
                    for (int i2 = 0; i2 < 10; ++i2) { const int m = lane + 64 * i2; hs[m] = (x1[i2] - mean) * rstd * (1.f + gs[1280 + m]) + gs[640 + m]; }
                }
                BAR();
                float a[8];
#pragma unroll
                for (int i2 = 0; i2 < 8; ++i2) a[i2] = 0.f;
#pragma unroll
                for (int j = 0; j < 5; ++j) {
                    float f[8]; unp8(f1[j], f); const float hv = hs[w * 80 + j * 16 + (lane >> 2)];
#pragma unroll
                    for (int i2 = 0; i2 < 8; ++i2) a[i2] += hv * f[i2];
                }
                float v = rscat8(a, lane);
                v += dpp<0x104>(v);
                const int e = ((lane >> 5) & 1) * 4 + ((lane >> 4) & 1) * 2 + ((lane >> 3) & 1);
                if (!(lane & 4)) red[w * 32 + (lane & 3) * 8 + e] = v;
                BAR();
                float hid[4];
#pragma unroll
                for (int j = 0; j < 4; ++j) {
                    float s = 0.f;
#pragma unroll
                    for (int i2 = 0; i2 < 8; ++i2) s += red[i2 * 32 + 4 * w + j];
                    hid[j] = fmaxf(s, 0.f);
                }
                float b8[8], b2[2];
#pragma unroll
                for (int i2 = 0; i2 < 8; ++i2) b8[i2] = 0.f;
                b2[0] = b2[1] = 0.f;
#pragma unroll
                for (int j = 0; j < 4; ++j) {
                    float f[8]; unp8(f2a[j], f);
#pragma unroll
                    for (int i2 = 0; i2 < 8; ++i2) b8[i2] += hid[j] * f[i2];
                    b2[0] += hid[j] * bflo(f2b[j]); b2[1] += hid[j] * bfhi(f2b[j]);
                }
                LAS float* fr = (LAS float*)(L + 32768);
                *(LAS f32x4*)(fr + w * 640 + lane * 8) = f32x4{b8[0], b8[1], b8[2], b8[3]};
                *(LAS f32x4*)(fr + w * 640 + lane * 8 + 4) = f32x4{b8[4], b8[5], b8[6], b8[7]};
                *(LAS f32x2*)(fr + w * 640 + 512 + lane * 2) = f32x2{b2[0], b2[1]};
                BAR();
                if (t < 160) {
                    f32x4 s = {0.f, 0.f, 0.f, 0.f};
#pragma unroll
                    for (int i2 = 0; i2 < 8; ++i2) s += *(LAS f32x4*)(fr + i2 * 640 + t * 4);
                    st4(rws, OFF_E7 + xi * 2560 + t * 16, u32x4{canonf(s.x), canonf(s.y), canonf(s.z), canonf(s.w)});
                }
            }
            STAMP(10);
            if (xi < 40 && w < 3) {
                u32x4 g;
                const u32 goff = OFF_E7 + (t >> 2) * 2560 + (16 * xi + 4 * (t & 3)) * 4;
                for (int it = 0; it < SPIN; ++it) { CFENCE();
                    g = ld4(rws, goff, AUX_SC1);
                    if (!anyl(pois4(g))) break;
                }
                f32x4 s = __builtin_bit_cast(f32x4, g);
#pragma unroll
                for (int i = 0; i < 4; ++i) {
                    float v = s[i];
                    v += dpp<0x104>(v);
                    v += dpp<0x128>(v);
                    v = rs16(v, v); v = rs32(v, v);
                    s[i] = v;
                }
                if (lane < 4) *(LAS f32x4*)(red + 512 + w * 16 + lane * 4) = s;
            }
            if (xi < 40) {
                BAR();
                if (t < 8) {
                    const int m = 16 * xi + 2 * t;
                    const float s0 = (red[512 + 2 * t] + red[512 + 16 + 2 * t]) + red[512 + 32 + 2 * t];
                    const float s1 = (red[512 + 2 * t + 1] + red[512 + 16 + 2 * t + 1]) + red[512 + 32 + 2 * t + 1];
                    ((u32*)out)[m >> 1] = cvtpk(xs[m] + gs[1920 + m] * s0, xs[m + 1] + gs[1920 + m + 1] * s1);
                }
                if (xi == 0 && t == 0) __hip_atomic_store((GAS u64*)(ws + OFF_EPOCH), epoch + 1ull, __ATOMIC_RELAXED, __HIP_MEMORY_SCOPE_AGENT);
            }
            STAMP(11);
        }
    } else {
        const int yi = bid - 128, h = yi & 31, mg = yi >> 5;
        const rsrc_t rq = mkrs(wq, DM * 6144 * 2);
        u32x3 qw[10];
        {
            const u32 base = (u32)(((mg * 160 + w * 20 + (lane >> 5)) * 32 + h) * 192 + 6 * (lane & 31)) * 2;
#pragma unroll
            for (int j = 0; j < 10; ++j) qw[j] = ld3(rq, base + j * 2 * 12288, AUX_NT);
            SB();
        }
        {
            const int seg0 = w * 16;
#pragma unroll
            for (int j = 0; j < 6; ++j) {
                const int idx = j * 64 + lane, seg = seg0 + idx / 24, ch = idx % 24;
                st4(rws, OFF_O + (u32)(seg * 128 + yi) * 384 + ch * 16, PZ);
            }
            if (w == 0 && lane < 32) st2(rws, OFF_ML + lane * 1024 + yi * 8, u32x2{POISON, POISON});
            if (w == 1 && lane < 16 && yi < 120) st4(rws, OFF_E6 + ((yi % 12) * 640 + (yi / 12) * 64) * 4 + lane * 16, PZ);
            SB();
        }
        const u64 tag = (epoch + 1ull) * TAGMUL;
        const u32 tlo = (u32)tag, thi = (u32)(tag >> 32);
        {
            const int rg = lane >> 4, g = lane & 15;
            const bool act = g < 14;
            const u32 goff = g < 10 ? OFF_E1 + (rg * 320 + mg * 80 + w * 10 + g) * 16 : OFF_PF + (w * 16 + rg * 4 + (g - 10)) * 16;
            u32x4 gv = {0u, 0u, tlo, thi};
            for (int it = 0; it < SPIN; ++it) { CFENCE();
                if (act) gv = ld4(rws, goff, AUX_SC1);
                if (!anyl((gv.z != tlo) | (gv.w != thi))) break;
            }
            STAMP(1);
            float s = rs32(__uint_as_float(gv.x), __uint_as_float(gv.y));
            s = rs16(s, s);
            float a[6];
#pragma unroll
            for (int i = 0; i < 6; ++i) a[i] = 0.f;
#define QSTEP(j) { const float hv = dpp<0x150 + j>(s); float f[6]; unp6(qw[j], f); _Pragma("unroll") for (int i = 0; i < 6; ++i) a[i] += hv * f[i]; }
            QSTEP(0) QSTEP(1) QSTEP(2) QSTEP(3) QSTEP(4) QSTEP(5) QSTEP(6) QSTEP(7) QSTEP(8) QSTEP(9)
#undef QSTEP
#pragma unroll
            for (int i = 0; i < 3; ++i) red[w * 192 + 6 * (lane & 31) + 3 * (lane >> 5) + i] = rs32(a[i], a[i + 3]);
            DRAIN();
            BAR();
            if (t < 96) {
                f32x2 s2 = {0.f, 0.f};
#pragma unroll
                for (int i = 0; i < 8; ++i) s2 += *(LAS f32x2*)(red + i * 192 + t * 2);
                st4(rws, OFF_E2 + ((mg * 32 + h) * 96 + t) * 16, u32x4{__float_as_uint(s2.x), __float_as_uint(s2.y), tlo, thi});
            }
            if (t == 128) st4(rws, OFF_PF + bid * 16, u32x4{0u, 0u, tlo, thi});
        }
        STAMP(2);
        {
            const unsigned char* src = (const unsigned char*)ckv + (size_t)yi * 64 * 1536;
#pragma unroll
            for (int j = 0; j < 12; ++j) {
                const int idx = (w * 12 + j) * 64 + lane, row = idx / 96, p = idx % 96;
                const int ch = (p & ~15) | ((p ^ row) & 15);
                __builtin_amdgcn_global_load_lds((const GAS u32*)(src + row * 1536 + ch * 16), (LAS u32*)(L + L_KV + (w * 12 + j) * 1024), 16, 0, AUX_NT);
            }
            SB();
        }
        const int kq = w >> 1, ht = w & 1;
        u32x4 qf[12];
        {
            const u32 qoff = OFF_E3 + (u32)((ht * 16 + (lane & 15)) * 4 + kq) * 768 + (lane >> 4) * 16;
            for (int it = 0; it < SPIN; ++it) { CFENCE();
                qf[0] = ld4(rws, qoff, AUX_SC1);
                if (!anyl(pois4(qf[0]))) break;
            }
            STAMP(3);
#pragma unroll
            for (int j = 1; j < 12; ++j) qf[j] = ld4(rws, qoff + (j % 6) * 64 + (j / 6) * 384, AUX_SC1);
            SB();
            for (int it = 0; it < SPIN; ++it) { CFENCE();
                u32 bad = 0;
#pragma unroll
                for (int j = 1; j < 12; ++j) if (anyl(pois4(qf[j]))) bad |= 1u << j;
                if (!bad) break;
#pragma unroll
                for (int j = 1; j < 12; ++j) if (bad & (1u << j)) qf[j] = ld4(rws, qoff + (j % 6) * 64 + (j / 6) * 384, AUX_SC1);
            }
        }
        DRAIN();
        BAR();
        STAMP(4);
        {
            const int r = lane & 15;
            const int keyr = 8 * (r >> 3) + 2 * (r & 3) + ((r >> 2) & 1);
            f32x4 acc[4];
#pragma unroll
            for (int kt = 0; kt < 4; ++kt) acc[kt] = f32x4{0.f, 0.f, 0.f, 0.f};
#pragma unroll
            for (int ks = 0; ks < 6; ++ks) {
                const int ch = kq * 24 + ks * 4 + (lane >> 4);
                const int p = (ch & ~15) | ((ch ^ keyr) & 15);
#pragma unroll
                for (int kt = 0; kt < 4; ++kt) {
                    const u32x4 a = *(LAS u32x4*)(L + L_KV + (kt * 16 + keyr) * 1536 + p * 16);
                    acc[kt] = mfma(a, qf[ks], acc[kt]);
                    acc[kt] = mfma(a, qf[6 + ks], acc[kt]);
                }
            }
            LAS float* sp = (LAS float*)(L + L_SP);
#pragma unroll
            for (int kt = 0; kt < 4; ++kt) *(LAS f32x4*)(sp + (kq * 32 + ht * 16 + (lane & 15)) * 68 + kt * 16 + 4 * (lane >> 4)) = acc[kt];
        }
        BAR();
        {
            LAS float* sp = (LAS float*)(L + L_SP);
            const int hd = t >> 4, qd = t & 15;
            f32x4 s = *(LAS f32x4*)(sp + hd * 68 + qd * 4);
#pragma unroll
            for (int k = 1; k < 4; ++k) s += *(LAS f32x4*)(sp + (k * 32 + hd) * 68 + qd * 4);
            const float m = row_max(fmaxf(fmaxf(s.x, s.y), fmaxf(s.z, s.w)));
            const float p0 = __builtin_amdgcn_exp2f(s.x - m), p1 = __builtin_amdgcn_exp2f(s.y - m), p2 = __builtin_amdgcn_exp2f(s.z - m), p3 = __builtin_amdgcn_exp2f(s.w - m);
            const u32 b01 = cvtpk(p0, p1), b23 = cvtpk(p2, p3);
            const float l = row_sum((bflo(b01) + bfhi(b01)) + (bflo(b23) + bfhi(b23)));
            *(LAS u32x2*)(L + L_P + hd * 144 + qd * 8) = u32x2{b01, b23};
            if (qd == 0) st2(rws, OFF_ML + hd * 1024 + yi * 8, u32x2{canonf(m), canonf(l)});
        }
        BAR();
        STAMP(5);
        {
            u32x4 pb[2][2];
#pragma unroll
            for (int hh = 0; hh < 2; ++hh)
#pragma unroll
                for (int k2 = 0; k2 < 2; ++k2) pb[hh][k2] = *(LAS u32x4*)(L + L_P + (hh * 16 + (lane & 15)) * 144 + k2 * 64 + (lane >> 4) * 16);
            f32x4 acc[6][2];
#pragma unroll
            for (int ct = 0; ct < 6; ++ct) { acc[ct][0] = f32x4{0.f, 0.f, 0.f, 0.f}; acc[ct][1] = f32x4{0.f, 0.f, 0.f, 0.f}; }
            const int g = lane >> 4, e = (lane >> 2) & 3, pp = lane & 3;
#pragma unroll
            for (int ct = 0; ct < 6; ++ct) {
                const int ch = w * 12 + ct * 2 + (pp >> 1);
#pragma unroll
                for (int k2 = 0; k2 < 2; ++k2) {
                    const int ka = k2 * 32 + 8 * g + 2 * e, kb = ka + 1;
                    const int pa = (ch & ~15) | ((ch ^ ka) & 15), pbx = (ch & ~15) | ((ch ^ kb) & 15);
                    const s16x4 t0 = __builtin_amdgcn_ds_read_tr16_b64_v4i16((LAS s16x4*)(L + L_KV + ka * 1536 + pa * 16 + (pp & 1) * 8));
                    const s16x4 t1 = __builtin_amdgcn_ds_read_tr16_b64_v4i16((LAS s16x4*)(L + L_KV + kb * 1536 + pbx * 16 + (pp & 1) * 8));
                    const u32x2 a0 = __builtin_bit_cast(u32x2, t0), a1 = __builtin_bit_cast(u32x2, t1);
                    const u32x4 a = {a0.x, a0.y, a1.x, a1.y};
                    acc[ct][0] = mfma(a, pb[0][k2], acc[ct][0]);
                    acc[ct][1] = mfma(a, pb[1][k2], acc[ct][1]);
                }
            }
            const int ocq = w >> 1;
#pragma unroll
            for (int hh = 0; hh < 2; ++hh)
#pragma unroll
                for (int cp = 0; cp < 3; ++cp) {
                    const f32x4 A = acc[2 * cp][hh], B = acc[2 * cp + 1][hh];
                    const u32 ax = cvtpk(A.x, A.y), ay = cvtpk(A.z, A.w), bx = cvtpk(B.x, B.y), by = cvtpk(B.z, B.w);
                    auto r0 = __builtin_amdgcn_permlane16_swap(ax, bx, false, false);
                    auto r1 = __builtin_amdgcn_permlane16_swap(ay, by, false, false);
                    const u32x4 v = {canon(r0[0]), canon(r1[0]), canon(r0[1]), canon(r1[1])};
                    const int c = (w & 1) * 96 + (2 * cp + (g & 1)) * 16 + (g >> 1) * 8;
                    st4(rws, OFF_O + (u32)(((hh * 16 + (lane & 15)) * 4 + ocq) * 128 + yi) * 384 + c * 2, v);
                }
        }
        STAMP(6);
        if (yi < 120) {
            const int rg = yi % 12, cs = yi / 12;
            const rsrc_t ro = mkrs(wo, 6144 * DM * 2);
            u32x4 ow[8];
            const u32 base = (u32)((rg * 512 + w * 64 + (lane >> 3) * 8) * DM + cs * 64 + (lane & 7) * 8) * 2;
#pragma unroll
            for (int j = 0; j < 8; ++j) ow[j] = ld4(ro, base + j * 1280, AUX_NT);
            SB();
            u32x4 g;
            const u32 goff = OFF_E5 + ((lane >> 4) * 6144 + rg * 512 + w * 64 + (lane & 15) * 4) * 4;
            for (int it = 0; it < SPIN; ++it) { CFENCE();
                g = ld4(rws, goff, AUX_SC1);
                if (!anyl(pois4(g))) break;
            }
            STAMP(7);
            f32x4 s = __builtin_bit_cast(f32x4, g);
#pragma unroll
            for (int i = 0; i < 4; ++i) { float v = s[i]; v = rs16(v, v); v = rs32(v, v); s[i] = v; }
            LAS float* cs_ = (LAS float*)(L + L_SP + w * 256);
            if (lane < 16) *(LAS f32x4*)(cs_ + lane * 4) = s;
            LGKM0();
            const f32x4 c0 = *(LAS f32x4*)(cs_ + (lane >> 3) * 8), c1 = *(LAS f32x4*)(cs_ + (lane >> 3) * 8 + 4);
            const float cv[8] = {c0.x, c0.y, c0.z, c0.w, c1.x, c1.y, c1.z, c1.w};
            float a[8];
#pragma unroll
            for (int i = 0; i < 8; ++i) a[i] = 0.f;
#pragma unroll
            for (int j = 0; j < 8; ++j) {
                float f[8]; unp8(ow[j], f);
#pragma unroll
                for (int i = 0; i < 8; ++i) a[i] += cv[j] * f[i];
            }
            const float v = rscat8(a, lane);
            const int e = ((lane >> 5) & 1) * 4 + ((lane >> 4) & 1) * 2 + ((lane >> 3) & 1);
            red[w * 64 + (lane & 7) * 8 + e] = v;
            BAR();
            if (w == 0 && lane < 16) {
                f32x4 s4 = {0.f, 0.f, 0.f, 0.f};
#pragma unroll
                for (int i = 0; i < 8; ++i) s4 += *(LAS f32x4*)(red + i * 64 + lane * 4);
                st4(rws, OFF_E6 + (rg * 640 + cs * 64) * 4 + lane * 16, u32x4{canonf(s4.x), canonf(s4.y), canonf(s4.z), canonf(s4.w)});
            }
            STAMP(8);
        }
    }
    if (bid == AMP_WG && t == 0) {
        const u64 now = __builtin_amdgcn_s_memrealtime();
        const u64 tgt = now + 0;
        for (int it = 0; it < 2000000; ++it) { if (__builtin_amdgcn_s_memrealtime() >= tgt) break; __builtin_amdgcn_s_sleep(1); }
    }
}

extern "C" void kernel_launch(void* const* d_in, const int* in_sizes, int n_in, void* d_out, int out_size, void* d_ws, size_t ws_size, hipStream_t stream) {
    (void)in_sizes; (void)n_in; (void)out_size; (void)ws_size;
    hipFuncSetAttribute((const void*)adaln_mla_mlp_kernel, hipFuncAttributeMaxDynamicSharedMemorySize, LDS_BYTES);
    adaln_mla_mlp_kernel<<<NWG, NT, LDS_BYTES, stream>>>(
        (const u16*)d_in[0], (const u16*)d_in[1], (const u16*)d_in[2], (const u16*)d_in[3], (const u16*)d_in[4], (const u16*)d_in[5],
        (const u16*)d_in[7], (const u16*)d_in[8], (const u16*)d_in[9], (const u16*)d_in[10], (u16*)d_out, (unsigned char*)d_ws);
}
```

```cpp
#define AMP_WG 0
#define AMP_FROM 0
#define AMP_TO 11
#define AMP_K 1
#include <hip/hip_runtime.h>
#include <stdint.h>

typedef unsigned u32;
typedef unsigned long long u64;
typedef unsigned short u16;
typedef u32 u32x2 __attribute__((ext_vector_type(2)));
typedef u32 u32x3 __attribute__((ext_vector_type(3)));
typedef u32 u32x4 __attribute__((ext_vector_type(4)));
typedef float f32x2 __attribute__((ext_vector_type(2)));
typedef float f32x4 __attribute__((ext_vector_type(4)));
typedef short bf16x8 __attribute__((ext_vector_type(8)));
typedef short s16x4 __attribute__((ext_vector_type(4)));
typedef __amdgpu_buffer_rsrc_t rsrc_t;
#define LAS __attribute__((address_space(3)))
#define GAS __attribute__((address_space(1)))

constexpr int NH = 32, SK = 8192, HD = 192, DM = 640, DC = 768, DFF = 1536;
constexpr int NWG = 256, NT = 512;

constexpr u32 OFF_EPOCH = 0;
constexpr u32 OFF_PF = 256;
constexpr u32 OFF_E1 = OFF_PF + 256 * 16;
constexpr u32 OFF_E2 = OFF_E1 + 4 * 320 * 16;
constexpr u32 OFF_E3 = OFF_E2 + 4 * 32 * 96 * 16;
constexpr u32 OFF_ML = OFF_E3 + 32 * 4 * 768;
constexpr u32 OFF_E5 = OFF_ML + 32 * 128 * 8;
constexpr u32 OFF_E6 = OFF_E5 + 4 * 6144 * 4;
constexpr u32 OFF_G = OFF_E6 + 12 * 640 * 4;
constexpr u32 OFF_E7 = OFF_G + 2 * 2560 * 4;
constexpr u32 OFF_O = OFF_E7 + 48 * 640 * 4;
constexpr u32 WS_BYTES = OFF_O + 32 * 4 * 128 * 384;
constexpr u32 OFF_DIAG = WS_BYTES;
static_assert(OFF_O % 128 == 0 && OFF_E3 % 128 == 0 && OFF_E5 % 128 == 0, "alignment");

constexpr int L_KV = 0;
constexpr int L_SP = 98304;
constexpr int L_P = L_SP + 4 * 32 * 68 * 4;
constexpr int L_RED = L_P + 32 * 144;
constexpr int LDS_BYTES = L_RED + 8192;
constexpr int SPIN = 1 << 16;
constexpr u32 POISON = 0xFFFFFFFFu;
constexpr int AUX_SC1 = 16, AUX_NT = 2;
constexpr u64 TAGMUL = 0x9E3779B97F4A7C15ull;

__device__ __forceinline__ float bflo(u32 v) { return __uint_as_float(v << 16); }
__device__ __forceinline__ float bfhi(u32 v) { return __uint_as_float(v & 0xFFFF0000u); }
__device__ __forceinline__ float bf1(u16 v) { return __uint_as_float((u32)v << 16); }
typedef __bf16 bf16x2_t __attribute__((ext_vector_type(2)));
__device__ __forceinline__ u32 cvtpk(float lo, float hi) { f32x2 v = {lo, hi}; return __builtin_bit_cast(u32, __builtin_convertvector(v, bf16x2_t)); }
__device__ __forceinline__ u32 canon(u32 v) { return v < 0xFFFFFFFEu ? v : 0xFFFFFFFEu; }
__device__ __forceinline__ u32 canonf(float v) { return canon(__float_as_uint(v)); }
__device__ __forceinline__ rsrc_t mkrs(const void* p, u32 bytes) { return __builtin_amdgcn_make_buffer_rsrc((void*)p, 0, bytes, 0x00020000); }
__device__ __forceinline__ u32x4 ld4(rsrc_t r, u32 off, int aux) { return aux == AUX_SC1 ? __builtin_amdgcn_raw_buffer_load_b128(r, off, 0, AUX_SC1) : __builtin_amdgcn_raw_buffer_load_b128(r, off, 0, AUX_NT); }
__device__ __forceinline__ u32x3 ld3(rsrc_t r, u32 off, int aux) { return aux == AUX_SC1 ? __builtin_amdgcn_raw_buffer_load_b96(r, off, 0, AUX_SC1) : __builtin_amdgcn_raw_buffer_load_b96(r, off, 0, AUX_NT); }
__device__ __forceinline__ void st4(rsrc_t r, u32 off, u32x4 v) { __builtin_amdgcn_raw_buffer_store_b128(v, r, off, 0, AUX_SC1); }
__device__ __forceinline__ void st2(rsrc_t r, u32 off, u32x2 v) { __builtin_amdgcn_raw_buffer_store_b64(v, r, off, 0, AUX_SC1); }
#define CFENCE() asm volatile("" ::: "memory")
#define SB() __builtin_amdgcn_sched_barrier(0)
#define BAR() asm volatile("s_waitcnt lgkmcnt(0)\n\ts_barrier" ::: "memory")
#define LGKM0() asm volatile("s_waitcnt lgkmcnt(0)" ::: "memory")
#define DRAIN() asm volatile("s_waitcnt vmcnt(0)" ::: "memory")

template <int CTRL> __device__ __forceinline__ float dpp(float x) { return __int_as_float(__builtin_amdgcn_update_dpp(0, __float_as_int(x), CTRL, 0xf, 0xf, true)); }
__device__ __forceinline__ float rs32(float a, float b) { auto r = __builtin_amdgcn_permlane32_swap(__float_as_uint(a), __float_as_uint(b), false, false); return __uint_as_float(r[0]) + __uint_as_float(r[1]); }
__device__ __forceinline__ float rs16(float a, float b) { auto r = __builtin_amdgcn_permlane16_swap(__float_as_uint(a), __float_as_uint(b), false, false); return __uint_as_float(r[0]) + __uint_as_float(r[1]); }
__device__ __forceinline__ float row_sum(float x) { x += dpp<0x128>(x); x += dpp<0x124>(x); x += dpp<0x122>(x); x += dpp<0x121>(x); return x; }
__device__ __forceinline__ float row_max(float x) { x = fmaxf(x, dpp<0x128>(x)); x = fmaxf(x, dpp<0x124>(x)); x = fmaxf(x, dpp<0x122>(x)); x = fmaxf(x, dpp<0x121>(x)); return x; }
__device__ __forceinline__ float wave_sum(float x) { x = row_sum(x); x = rs16(x, x); x = rs32(x, x); return x; }
__device__ __forceinline__ float wave_max(float x) {
    x = row_max(x);
    { auto r = __builtin_amdgcn_permlane16_swap(__float_as_uint(x), __float_as_uint(x), false, false); x = fmaxf(__uint_as_float(r[0]), __uint_as_float(r[1])); }
    { auto r = __builtin_amdgcn_permlane32_swap(__float_as_uint(x), __float_as_uint(x), false, false); x = fmaxf(__uint_as_float(r[0]), __uint_as_float(r[1])); }
    return x;
}
__device__ __forceinline__ float rscat8(const float (&a)[8], int lane) {
    float r0 = rs32(a[0], a[4]), r1 = rs32(a[1], a[5]), r2 = rs32(a[2], a[6]), r3 = rs32(a[3], a[7]);
    float s0 = rs16(r0, r2), s1 = rs16(r1, r3);
    float t0 = s0 + dpp<0x128>(s0), t1 = s1 + dpp<0x128>(s1);
    return (lane & 8) ? t1 : t0;
}
__device__ __forceinline__ bool pois4(u32x4 v) { u32 m = max(max(v.x, v.y), max(v.z, v.w)); return m == POISON; }
__device__ __forceinline__ bool pois3(u32x3 v) { u32 m = max(max(v.x, v.y), v.z); return m == POISON; }
__device__ __forceinline__ bool anyl(bool b) { return __builtin_amdgcn_ballot_w64(b) != 0ull; }
__device__ __forceinline__ f32x4 mfma(u32x4 a, u32x4 b, f32x4 c) { return __builtin_amdgcn_mfma_f32_16x16x32_bf16(__builtin_bit_cast(bf16x8, a), __builtin_bit_cast(bf16x8, b), c, 0, 0, 0); }
__device__ __forceinline__ float silu(float v) { return v / (1.f + __expf(-v)); }
__device__ __forceinline__ void unp8(u32x4 v, float (&f)[8]) { f[0] = bflo(v.x); f[1] = bfhi(v.x); f[2] = bflo(v.y); f[3] = bfhi(v.y); f[4] = bflo(v.z); f[5] = bfhi(v.z); f[6] = bflo(v.w); f[7] = bfhi(v.w); }
__device__ __forceinline__ void unp6(u32x3 v, float (&f)[6]) { f[0] = bflo(v.x); f[1] = bfhi(v.x); f[2] = bflo(v.y); f[3] = bfhi(v.y); f[4] = bflo(v.z); f[5] = bfhi(v.z); }

#define STAMP(i) do { st[i] = __builtin_amdgcn_s_memrealtime(); } while (0)

__device__ __forceinline__ void ln_stats(const float (&v)[10], float& mean, float& rstd) {
    float s = 0.f;
#pragma unroll
    for (int i = 0; i < 10; ++i) s += v[i];
    mean = wave_sum(s) * (1.f / 640.f);
    float q = 0.f;
#pragma unroll
    for (int i = 0; i < 10; ++i) { float d = v[i] - mean; q += d * d; }
    float var = wave_sum(q) * (1.f / 640.f);
    rstd = 1.f / sqrtf(var + 1e-5f);
}

__global__ __launch_bounds__(NT, 2) void adaln_mla_mlp_kernel(
    const u16* __restrict__ x, const u16* __restrict__ ckv, const u16* __restrict__ wq, const u16* __restrict__ wuk,
    const u16* __restrict__ wuv, const u16* __restrict__ wo, const u16* __restrict__ w1, const u16* __restrict__ w2,
    const u16* __restrict__ cc, const u16* __restrict__ wmod, u16* __restrict__ out, unsigned char* __restrict__ ws) {
    extern __shared__ __attribute__((aligned(16))) unsigned char smem[];
    LAS unsigned char* L = (LAS unsigned char*)smem;
    const int t = threadIdx.x, lane = t & 63, w = __builtin_amdgcn_readfirstlane(t >> 6);
    const int bid = blockIdx.x;
    const rsrc_t rws = mkrs(ws, WS_BYTES);
    u64 st[16];
#pragma unroll
    for (int i = 0; i < 16; ++i) st[i] = 0;
    STAMP(0);
    const u64 epoch = __hip_atomic_load((GAS u64*)(ws + OFF_EPOCH), __ATOMIC_RELAXED, __HIP_MEMORY_SCOPE_AGENT);
    const u32x4 PZ = {POISON, POISON, POISON, POISON};
    LAS float* red = (LAS float*)(L + L_RED);

    if (bid < 128) {
        const int xi = bid, h = xi & 31, cq = xi >> 5;
        const rsrc_t rmod = mkrs(wmod, DM * 3840 * 2);
        const bool isM = xi < 80;
        u32x4 mw0, mw1, mw2; u16 c0 = 0, c1 = 0, c2 = 0; u32 xr[5]; u16 xm = 0;
        int m_rg = 0, m_s = 0;
        if (isM) {
            const int x8 = xi & 7, b = (xi >> 3) & 1, slot = xi >> 4, qq = slot * 8 + x8;
            m_rg = qq / 10; m_s = 2 * (qq % 10) + b;
            const int chunk = t & 7, rsub = t >> 3;
            const int col = chunk < 4 ? 32 * m_s + 8 * chunk : 640 + 32 * m_s + 8 * (chunk - 4);
            const int row0 = m_rg * 160 + rsub;
            c0 = cc[row0]; c1 = cc[row0 + 64]; if (w < 4) c2 = cc[row0 + 128];
            if (w == 0) {
#pragma unroll
                for (int i = 0; i < 5; ++i) xr[i] = ((const u32*)x)[lane + 64 * i];
                xm = x[32 * m_s + (lane & 31)];
            }
            const u32 off = (u32)(row0 * 3840 + col) * 2;
            mw0 = ld4(rmod, off, AUX_NT); mw1 = ld4(rmod, off + 64 * 7680, AUX_NT);
            if (w < 4) mw2 = ld4(rmod, off + 128 * 7680, AUX_NT);
            SB();
        }
        if (w == 7) {
            if (lane < 48) { st4(rws, OFF_E3 + (h * 4 + cq) * 768 + lane * 16, PZ); st4(rws, OFF_E5 + (cq * 6144 + h * 192) * 4 + lane * 16, PZ); }
            if (xi < 48) {
                st4(rws, OFF_E7 + xi * 2560 + lane * 16, PZ); st4(rws, OFF_E7 + xi * 2560 + 1024 + lane * 16, PZ);
                if (lane < 32) st4(rws, OFF_E7 + xi * 2560 + 2048 + lane * 16, PZ);
            } else if (lane < 16) {
                const int gi = xi - 48;
                st4(rws, OFF_G + ((gi / 40) * 2560 + (gi % 40) * 64) * 4 + lane * 16, PZ);
            }
            SB();
        }
        const u64 tag = (epoch + 1ull) * TAGMUL;
        const u32 tlo = (u32)tag, thi = (u32)(tag >> 32);
        if (isM) {
            float a[8], f[8];
            const float s0 = silu(bf1(c0)), s1 = silu(bf1(c1));
            unp8(mw0, f);
#pragma unroll
            for (int i = 0; i < 8; ++i) a[i] = s0 * f[i];
            unp8(mw1, f);
#pragma unroll
            for (int i = 0; i < 8; ++i) a[i] += s1 * f[i];
            if (w < 4) {
                const float s2 = silu(bf1(c2));
                unp8(mw2, f);
#pragma unroll
                for (int i = 0; i < 8; ++i) a[i] += s2 * f[i];
            }
            const float v = rscat8(a, lane);
            const int e = ((lane >> 5) & 1) * 4 + ((lane >> 4) & 1) * 2 + ((lane >> 3) & 1);
            red[w * 64 + (lane & 7) * 8 + e] = v;
            float lnx = 0.f;
            if (w == 0) {
                float xv[10];
#pragma unroll
                for (int i = 0; i < 5; ++i) { xv[2 * i] = bflo(xr[i]); xv[2 * i + 1] = bfhi(xr[i]); }
                float mean, rstd; ln_stats(xv, mean, rstd);
                lnx = (bf1(xm) - mean) * rstd;
            }
            BAR();
            if (w == 0) {
                float s = 0.f;
#pragma unroll
                for (int i = 0; i < 8; ++i) s += red[i * 64 + lane];
                auto r = __builtin_amdgcn_permlane32_swap(__float_as_uint(s), __float_as_uint(s), false, false);
                const float sa = __uint_as_float(r[0]), ca = __uint_as_float(r[1]);
                float hv = lnx * ca + sa; if (m_rg == 0) hv += lnx;
                const float hn = dpp<0xB1>(hv);
                if (lane < 32 && !(lane & 1)) st4(rws, OFF_E1 + (m_rg * 320 + m_s * 16 + (lane >> 1)) * 16, u32x4{__float_as_uint(hv), __float_as_uint(hn), tlo, thi});
            }
        }
        STAMP(1);
        if (w == 7) { DRAIN(); if (lane == 0) st4(rws, OFF_PF + bid * 16, u32x4{0u, 0u, tlo, thi}); }
        const rsrc_t ruk = mkrs(wuk, DC * 6144 * 2);
        const int tg = w >> 1, hf = w & 1;
        u32x4 aw[9];
        {
            const u32 base = (u32)(((cq * 192 + tg * 48 + (lane & 15)) * 32 + h) * 192 + hf * 96 + 8 * (lane >> 4)) * 2;
#pragma unroll
            for (int ti = 0; ti < 3; ++ti)
#pragma unroll
                for (int ki = 0; ki < 3; ++ki) aw[ti * 3 + ki] = ld4(ruk, base + ti * 16 * 12288 + ki * 64, AUX_NT);
            SB();
        }
        float qv[24];
        {
            u32x4 g[3];
            const u32 goff = OFF_E2 + (((lane >> 4) * 32 + h) * 96 + hf * 48 + (lane & 15)) * 16;
            for (int it = 0; it < SPIN; ++it) { CFENCE();
#pragma unroll
                for (int i = 0; i < 3; ++i) g[i] = ld4(rws, goff + i * 256, AUX_SC1);
                bool bad = false;
#pragma unroll
                for (int i = 0; i < 3; ++i) bad |= (g[i].z != tlo) | (g[i].w != thi);
                if (!anyl(bad)) break;
            }
            STAMP(2);
            LAS float* qs = (LAS float*)(L + w * 512);
#pragma unroll
            for (int i = 0; i < 3; ++i) {
                float s = rs32(__uint_as_float(g[i].x), __uint_as_float(g[i].y));
                s = rs16(s, s);
                qs[2 * (16 * i + (lane & 15)) + (lane >> 5)] = s;
            }
            LGKM0();
#pragma unroll
            for (int ki = 0; ki < 3; ++ki) {
                const f32x4 u0 = *(LAS f32x4*)(qs + ki * 32 + 8 * (lane >> 4)), u1 = *(LAS f32x4*)(qs + ki * 32 + 8 * (lane >> 4) + 4);
                qv[ki * 8 + 0] = u0.x; qv[ki * 8 + 1] = u0.y; qv[ki * 8 + 2] = u0.z; qv[ki * 8 + 3] = u0.w;
                qv[ki * 8 + 4] = u1.x; qv[ki * 8 + 5] = u1.y; qv[ki * 8 + 6] = u1.z; qv[ki * 8 + 7] = u1.w;
            }
        }
        {
            float acc[3];
#pragma unroll
            for (int ti = 0; ti < 3; ++ti) {
                float s = 0.f;
#pragma unroll
                for (int ki = 0; ki < 3; ++ki) {
                    float f[8]; unp8(aw[ti * 3 + ki], f);
#pragma unroll
                    for (int j = 0; j < 8; ++j) s += f[j] * qv[ki * 8 + j];
                }
                s = rs16(s, s); s = rs32(s, s); acc[ti] = s;
            }
            const int row = lane >> 4;
            const float v = row == 0 ? acc[0] : (row == 1 ? acc[1] : acc[2]);
            if (row < 3) red[hf * 192 + tg * 48 + row * 16 + (lane & 15)] = v;
            BAR();
            if (w == 0 && lane < 48) {
                const int ch = lane < 24 ? lane : lane - 24;
                const float sc = 0.07216878364870322f * 1.4426950408889634f;
                float v8[8];
#pragma unroll
                for (int i = 0; i < 2; ++i) {
                    const f32x4 p0 = *(LAS f32x4*)(red + ch * 8 + i * 4), p1 = *(LAS f32x4*)(red + 192 + ch * 8 + i * 4);
                    v8[i * 4 + 0] = (p0.x + p1.x) * sc; v8[i * 4 + 1] = (p0.y + p1.y) * sc; v8[i * 4 + 2] = (p0.z + p1.z) * sc; v8[i * 4 + 3] = (p0.w + p1.w) * sc;
                }
                u32 o[4];
#pragma unroll
                for (int i = 0; i < 4; ++i) {
                    const u32 hi = cvtpk(v8[2 * i], v8[2 * i + 1]);
                    const u32 lo = cvtpk(v8[2 * i] - bflo(hi), v8[2 * i + 1] - bfhi(hi));
                    o[i] = canon(lane < 24 ? hi : lo);
                }
                st4(rws, OFF_E3 + (h * 4 + cq) * 768 + lane * 16, u32x4{o[0], o[1], o[2], o[3]});
            }
        }
        STAMP(3);
        const rsrc_t ruv = mkrs(wuv, DC * 6144 * 2);
        u32x3 vw[12];
        {
            const u32 base = (u32)(((cq * 192 + w * 24 + (lane >> 5)) * 32 + h) * 192 + 6 * (lane & 31)) * 2;
#pragma unroll
            for (int j = 0; j < 12; ++j) vw[j] = ld3(ruv, base + j * 2 * 12288, AUX_NT);
            SB();
        }
        if (xi >= 48) {
            const int gi = xi - 48, slice = gi % 40, rh = gi / 40;
            const int chunk = t & 7, rsub = t >> 3;
            const int row0 = rh * 320 + rsub;
            u16 cv[5]; u32x4 gw[5];
#pragma unroll
            for (int j = 0; j < 5; ++j) cv[j] = cc[row0 + 64 * j];
            const u32 off = (u32)(row0 * 3840 + 1280 + 64 * slice + 8 * chunk) * 2;
#pragma unroll
            for (int j = 0; j < 5; ++j) gw[j] = ld4(rmod, off + j * 64 * 7680, AUX_NT);
            SB();
            float a[8];
#pragma unroll
            for (int i = 0; i < 8; ++i) a[i] = 0.f;
#pragma unroll
            for (int j = 0; j < 5; ++j) {
                float f[8]; unp8(gw[j], f); const float s = silu(bf1(cv[j]));
#pragma unroll
                for (int i = 0; i < 8; ++i) a[i] += s * f[i];
            }
            const float v = rscat8(a, lane);
            const int e = ((lane >> 5) & 1) * 4 + ((lane >> 4) & 1) * 2 + ((lane >> 3) & 1);
            LAS float* redg = red + 512;
            redg[w * 64 + (lane & 7) * 8 + e] = v;
            BAR();
            if (w == 0 && lane < 16) {
                f32x4 s = {0.f, 0.f, 0.f, 0.f};
#pragma unroll
                for (int i = 0; i < 8; ++i) s += *(LAS f32x4*)(redg + i * 64 + lane * 4);
                st4(rws, OFF_G + (rh * 2560 + slice * 64) * 4 + lane * 16, u32x4{canonf(s.x), canonf(s.y), canonf(s.z), canonf(s.w)});
            }
        }
        {
            if (w < 4) {
                for (int it = 0; it < SPIN; ++it) { CFENCE();
                    const u32x4 g = ld4(rws, OFF_PF + t * 16, AUX_SC1);
                    if (!anyl((g.z != tlo) | (g.w != thi))) break;
                }
            }
            BAR();
        }
        STAMP(4);
        {
            u32x4 ml;
            for (int it = 0; it < SPIN; ++it) { CFENCE();
                ml = ld4(rws, OFF_ML + h * 1024 + lane * 16, AUX_SC1);
                if (!anyl(pois4(ml))) break;
            }
            STAMP(5);
            u32x3 ov[8];
            const u32 obase = OFF_O + (u32)((h * 4 + cq) * 128 + w * 16 + (lane >> 5)) * 384 + (lane & 31) * 12;
#pragma unroll
            for (int j = 0; j < 8; ++j) ov[j] = ld3(rws, obase + j * 768, AUX_SC1);
            SB();
            const float m0 = __uint_as_float(ml.x), l0 = __uint_as_float(ml.y), m1 = __uint_as_float(ml.z), l1 = __uint_as_float(ml.w);
            const float M = wave_max(fmaxf(m0, m1));
            const float w0 = __builtin_amdgcn_exp2f(m0 - M), w1_ = __builtin_amdgcn_exp2f(m1 - M);
            const float Lsum = wave_sum(w0 * l0 + w1_ * l1);
            const float inv = 1.f / Lsum;
            for (int it = 0; it < SPIN; ++it) { CFENCE();
                u32 bad = 0;
#pragma unroll
                for (int j = 0; j < 8; ++j) if (anyl(pois3(ov[j]))) bad |= 1u << j;
                if (!bad) break;
#pragma unroll
                for (int j = 0; j < 8; ++j) if (bad & (1u << j)) ov[j] = ld3(rws, obase + j * 768, AUX_SC1);
            }
            STAMP(6);
            float a[6];
#pragma unroll
            for (int i = 0; i < 6; ++i) a[i] = 0.f;
#pragma unroll
            for (int j = 0; j < 8; ++j) {
                const float wa = __uint_as_float(__builtin_amdgcn_readlane(__float_as_uint(w0), w * 8 + j));
                const float wb = __uint_as_float(__builtin_amdgcn_readlane(__float_as_uint(w1_), w * 8 + j));
                const float ww = (lane >> 5) ? wb : wa;
                float f[6]; unp6(ov[j], f);
#pragma unroll
                for (int i = 0; i < 6; ++i) a[i] += ww * f[i];
            }
#pragma unroll
            for (int i = 0; i < 3; ++i) red[w * 192 + 6 * (lane & 31) + 3 * (lane >> 5) + i] = rs32(a[i], a[i + 3]) * inv;
            BAR();
            LAS float* cl = (LAS float*)(L + w * 128);
            if (lane < 24) {
                float s = 0.f;
#pragma unroll
                for (int i = 0; i < 8; ++i) s += red[i * 192 + w * 24 + lane];
                cl[(lane & 1) * 12 + (lane >> 1)] = s;
            }
            LGKM0();
            float cv[12];
#pragma unroll
            for (int i = 0; i < 3; ++i) { const f32x4 u = *(LAS f32x4*)(cl + (lane >> 5) * 12 + i * 4); cv[i * 4] = u.x; cv[i * 4 + 1] = u.y; cv[i * 4 + 2] = u.z; cv[i * 4 + 3] = u.w; }
#pragma unroll
            for (int i = 0; i < 6; ++i) a[i] = 0.f;
#pragma unroll
            for (int j = 0; j < 12; ++j) {
                float f[6]; unp6(vw[j], f);
#pragma unroll
                for (int i = 0; i < 6; ++i) a[i] += cv[j] * f[i];
            }
            BAR();
#pragma unroll
            for (int i = 0; i < 3; ++i) red[w * 192 + 6 * (lane & 31) + 3 * (lane >> 5) + i] = rs32(a[i], a[i + 3]);
            BAR();
            if (w == 0 && lane < 48) {
                f32x4 s = {0.f, 0.f, 0.f, 0.f};
#pragma unroll
                for (int i = 0; i < 8; ++i) s += *(LAS f32x4*)(red + i * 192 + lane * 4);
                st4(rws, OFF_E5 + (cq * 6144 + h * 192) * 4 + lane * 16, u32x4{canonf(s.x), canonf(s.y), canonf(s.z), canonf(s.w)});
            }
        }
        STAMP(7);
        if (xi < 48) {
            const rsrc_t r1 = mkrs(w1, DM * DFF * 2), r2 = mkrs(w2, DFF * DM * 2);
            u32x4 f1[5], f2a[4]; u32 f2b[4];
            {
                const u32 b1 = (u32)((w * 80 + (lane >> 2)) * DFF + 32 * xi + 8 * (lane & 3)) * 2;
#pragma unroll
                for (int j = 0; j < 5; ++j) f1[j] = ld4(r1, b1 + j * 16 * DFF * 2, AUX_NT);
                const u32 b2 = (u32)((32 * xi + 4 * w) * DM) * 2;
#pragma unroll
                for (int j = 0; j < 4; ++j) { f2a[j] = ld4(r2, b2 + j * 1280 + lane * 16, AUX_NT); f2b[j] = __builtin_amdgcn_raw_buffer_load_b32(r2, b2 + j * 1280 + 1024 + lane * 4, 0, AUX_NT); }
                SB();
            }
            LAS float* gs = (LAS float*)(L + 4096);
            LAS float* xs = (LAS float*)(L + 4096 + 10240);
            LAS float* ap = (LAS float*)(L + 4096 + 10240 + 2560);
            LAS float* hs = (LAS float*)(L + 4096 + 10240 + 2560 + 7680);
            {
                u32x4 g0, g1, g2, g3;
                const int i0 = t, i1 = t + 512;
                for (int it = 0; it < SPIN; ++it) { CFENCE();
                    g0 = ld4(rws, OFF_G + i0 * 16, AUX_SC1); g1 = ld4(rws, OFF_G + 10240 + i0 * 16, AUX_SC1);
                    bool bad = pois4(g0) | pois4(g1);
                    if (t < 128) { g2 = ld4(rws, OFF_G + i1 * 16, AUX_SC1); g3 = ld4(rws, OFF_G + 10240 + i1 * 16, AUX_SC1); bad |= pois4(g2) | pois4(g3); }
                    if (!anyl(bad)) break;
                }
                *(LAS f32x4*)(gs + i0 * 4) = __builtin_bit_cast(f32x4, g0) + __builtin_bit_cast(f32x4, g1);
                if (t < 128) *(LAS f32x4*)(gs + i1 * 4) = __builtin_bit_cast(f32x4, g2) + __builtin_bit_cast(f32x4, g3);
                if (t < 320) { const u32 xx = ((const u32*)x)[t]; xs[2 * t] = bflo(xx); xs[2 * t + 1] = bfhi(xx); }
            }
            STAMP(8);
            {
                const int part = t / 160, i = t % 160;
                if (t < 480) {
                    u32x4 g[4];
                    const u32 goff = OFF_E6 + (part * 4 * 640 + i * 4) * 4;
                    for (int it = 0; it < SPIN; ++it) { CFENCE();
#pragma unroll
                        for (int j = 0; j < 4; ++j) g[j] = ld4(rws, goff + j * 2560, AUX_SC1);
                        if (!anyl(pois4(g[0]) | pois4(g[1]) | pois4(g[2]) | pois4(g[3]))) break;
                    }
                    *(LAS f32x4*)(ap + part * 640 + i * 4) = (__builtin_bit_cast(f32x4, g[0]) + __builtin_bit_cast(f32x4, g[1])) + (__builtin_bit_cast(f32x4, g[2]) + __builtin_bit_cast(f32x4, g[3]));
                }
                BAR();
                STAMP(9);
                float x1[10];
#pragma unroll
                for (int i2 = 0; i2 < 10; ++i2) { const int m = lane + 64 * i2; x1[i2] = xs[m] + gs[m] * ((ap[m] + ap[640 + m]) + ap[1280 + m]); }
                float mean, rstd; ln_stats(x1, mean, rstd);
                BAR();
                if (w == 0) {
#pragma unroll
                    for (int i2 = 0; i2 < 10; ++i2) xs[lane + 64 * i2] = x1[i2];
                }
                if (w == 1) {
#pragma unroll
                    for (int i2 = 0; i2 < 10; ++i2) { const int m = lane + 64 * i2; hs[m] = (x1[i2] - mean) * rstd * (1.f + gs[1280 + m]) + gs[640 + m]; }
                }
                BAR();
                float a[8];
#pragma unroll
                for (int i2 = 0; i2 < 8; ++i2) a[i2] = 0.f;
#pragma unroll
                for (int j = 0; j < 5; ++j) {
                    float f[8]; unp8(f1[j], f); const float hv = hs[w * 80 + j * 16 + (lane >> 2)];
#pragma unroll
                    for (int i2 = 0; i2 < 8; ++i2) a[i2] += hv * f[i2];
                }
                float v = rscat8(a, lane);
                v += dpp<0x104>(v);
                const int e = ((lane >> 5) & 1) * 4 + ((lane >> 4) & 1) * 2 + ((lane >> 3) & 1);
                if (!(lane & 4)) red[w * 32 + (lane & 3) * 8 + e] = v;
                BAR();
                float hid[4];
#pragma unroll
                for (int j = 0; j < 4; ++j) {
                    float s = 0.f;
#pragma unroll
                    for (int i2 = 0; i2 < 8; ++i2) s += red[i2 * 32 + 4 * w + j];
                    hid[j] = fmaxf(s, 0.f);
                }
                float b8[8], b2[2];
#pragma unroll
                for (int i2 = 0; i2 < 8; ++i2) b8[i2] = 0.f;
                b2[0] = b2[1] = 0.f;
#pragma unroll
                for (int j = 0; j < 4; ++j) {
                    float f[8]; unp8(f2a[j], f);
#pragma unroll
                    for (int i2 = 0; i2 < 8; ++i2) b8[i2] += hid[j] * f[i2];
                    b2[0] += hid[j] * bflo(f2b[j]); b2[1] += hid[j] * bfhi(f2b[j]);
                }
                LAS float* fr = (LAS float*)(L + 32768);
                *(LAS f32x4*)(fr + w * 640 + lane * 8) = f32x4{b8[0], b8[1], b8[2], b8[3]};
                *(LAS f32x4*)(fr + w * 640 + lane * 8 + 4) = f32x4{b8[4], b8[5], b8[6], b8[7]};
                *(LAS f32x2*)(fr + w * 640 + 512 + lane * 2) = f32x2{b2[0], b2[1]};
                BAR();
                if (t < 160) {
                    f32x4 s = {0.f, 0.f, 0.f, 0.f};
#pragma unroll
                    for (int i2 = 0; i2 < 8; ++i2) s += *(LAS f32x4*)(fr + i2 * 640 + t * 4);
                    st4(rws, OFF_E7 + xi * 2560 + t * 16, u32x4{canonf(s.x), canonf(s.y), canonf(s.z), canonf(s.w)});
                }
            }
            STAMP(10);
            if (xi < 40 && w < 3) {
                u32x4 g;
                const u32 goff = OFF_E7 + (t >> 2) * 2560 + (16 * xi + 4 * (t & 3)) * 4;
                for (int it = 0; it < SPIN; ++it) { CFENCE();
                    g = ld4(rws, goff, AUX_SC1);
                    if (!anyl(pois4(g))) break;
                }
                f32x4 s = __builtin_bit_cast(f32x4, g);
#pragma unroll
                for (int i = 0; i < 4; ++i) {
                    float v = s[i];
                    v += dpp<0x104>(v);
                    v += dpp<0x128>(v);
                    v = rs16(v, v); v = rs32(v, v);
                    s[i] = v;
                }
                if (lane < 4) *(LAS f32x4*)(red + 512 + w * 16 + lane * 4) = s;
            }
            if (xi < 40) {
                BAR();
                if (t < 8) {
                    const int m = 16 * xi + 2 * t;
                    const float s0 = (red[512 + 2 * t] + red[512 + 16 + 2 * t]) + red[512 + 32 + 2 * t];
                    const float s1 = (red[512 + 2 * t + 1] + red[512 + 16 + 2 * t + 1]) + red[512 + 32 + 2 * t + 1];
                    ((u32*)out)[m >> 1] = cvtpk(xs[m] + gs[1920 + m] * s0, xs[m + 1] + gs[1920 + m + 1] * s1);
                }
                if (xi == 0 && t == 0) __hip_atomic_store((GAS u64*)(ws + OFF_EPOCH), epoch + 1ull, __ATOMIC_RELAXED, __HIP_MEMORY_SCOPE_AGENT);
            }
            STAMP(11);
        }
    } else {
        const int yi = bid - 128, h = yi & 31, mg = yi >> 5;
        const rsrc_t rq = mkrs(wq, DM * 6144 * 2);
        u32x3 qw[10];
        {
            const u32 base = (u32)(((mg * 160 + w * 20 + (lane >> 5)) * 32 + h) * 192 + 6 * (lane & 31)) * 2;
#pragma unroll
            for (int j = 0; j < 10; ++j) qw[j] = ld3(rq, base + j * 2 * 12288, AUX_NT);
            SB();
        }
        {
            const int seg0 = w * 16;
#pragma unroll
            for (int j = 0; j < 6; ++j) {
                const int idx = j * 64 + lane, seg = seg0 + idx / 24, ch = idx % 24;
                st4(rws, OFF_O + (u32)(seg * 128 + yi) * 384 + ch * 16, PZ);
            }
            if (w == 0 && lane < 32) st2(rws, OFF_ML + lane * 1024 + yi * 8, u32x2{POISON, POISON});
            if (w == 1 && lane < 16 && yi < 120) st4(rws, OFF_E6 + ((yi % 12) * 640 + (yi / 12) * 64) * 4 + lane * 16, PZ);
            SB();
        }
        const u64 tag = (epoch + 1ull) * TAGMUL;
        const u32 tlo = (u32)tag, thi = (u32)(tag >> 32);
        {
            const int rg = lane >> 4, g = lane & 15;
            const bool act = g < 14;
            const u32 goff = g < 10 ? OFF_E1 + (rg * 320 + mg * 80 + w * 10 + g) * 16 : OFF_PF + (w * 16 + rg * 4 + (g - 10)) * 16;
            u32x4 gv = {0u, 0u, tlo, thi};
            for (int it = 0; it < SPIN; ++it) { CFENCE();
                if (act) gv = ld4(rws, goff, AUX_SC1);
                if (!anyl((gv.z != tlo) | (gv.w != thi))) break;
            }
            STAMP(1);
            float s = rs32(__uint_as_float(gv.x), __uint_as_float(gv.y));
            s = rs16(s, s);
            float a[6];
#pragma unroll
            for (int i = 0; i < 6; ++i) a[i] = 0.f;
#define QSTEP(j) { const float hv = dpp<0x150 + j>(s); float f[6]; unp6(qw[j], f); _Pragma("unroll") for (int i = 0; i < 6; ++i) a[i] += hv * f[i]; }
            QSTEP(0) QSTEP(1) QSTEP(2) QSTEP(3) QSTEP(4) QSTEP(5) QSTEP(6) QSTEP(7) QSTEP(8) QSTEP(9)
#undef QSTEP
#pragma unroll
            for (int i = 0; i < 3; ++i) red[w * 192 + 6 * (lane & 31) + 3 * (lane >> 5) + i] = rs32(a[i], a[i + 3]);
            DRAIN();
            BAR();
            if (t < 96) {
                f32x2 s2 = {0.f, 0.f};
#pragma unroll
                for (int i = 0; i < 8; ++i) s2 += *(LAS f32x2*)(red + i * 192 + t * 2);
                st4(rws, OFF_E2 + ((mg * 32 + h) * 96 + t) * 16, u32x4{__float_as_uint(s2.x), __float_as_uint(s2.y), tlo, thi});
            }
            if (t == 128) st4(rws, OFF_PF + bid * 16, u32x4{0u, 0u, tlo, thi});
        }
        STAMP(2);
        {
            const unsigned char* src = (const unsigned char*)ckv + (size_t)yi * 64 * 1536;
#pragma unroll
            for (int j = 0; j < 12; ++j) {
                const int idx = (w * 12 + j) * 64 + lane, row = idx / 96, p = idx % 96;
                const int ch = (p & ~15) | ((p ^ row) & 15);
                __builtin_amdgcn_global_load_lds((const GAS u32*)(src + row * 1536 + ch * 16), (LAS u32*)(L + L_KV + (w * 12 + j) * 1024), 16, 0, AUX_NT);
            }
            SB();
        }
        const int kq = w >> 1, ht = w & 1;
        u32x4 qf[12];
        {
            const u32 qoff = OFF_E3 + (u32)((ht * 16 + (lane & 15)) * 4 + kq) * 768 + (lane >> 4) * 16;
            for (int it = 0; it < SPIN; ++it) { CFENCE();
                qf[0] = ld4(rws, qoff, AUX_SC1);
                if (!anyl(pois4(qf[0]))) break;
            }
            STAMP(3);
#pragma unroll
            for (int j = 1; j < 12; ++j) qf[j] = ld4(rws, qoff + (j % 6) * 64 + (j / 6) * 384, AUX_SC1);
            SB();
            for (int it = 0; it < SPIN; ++it) { CFENCE();
                u32 bad = 0;
#pragma unroll
                for (int j = 1; j < 12; ++j) if (anyl(pois4(qf[j]))) bad |= 1u << j;
                if (!bad) break;
#pragma unroll
                for (int j = 1; j < 12; ++j) if (bad & (1u << j)) qf[j] = ld4(rws, qoff + (j % 6) * 64 + (j / 6) * 384, AUX_SC1);
            }
        }
        DRAIN();
        BAR();
        STAMP(4);
        {
            const int r = lane & 15;
            const int keyr = 8 * (r >> 3) + 2 * (r & 3) + ((r >> 2) & 1);
            f32x4 acc[4];
#pragma unroll
            for (int kt = 0; kt < 4; ++kt) acc[kt] = f32x4{0.f, 0.f, 0.f, 0.f};
#pragma unroll
            for (int ks = 0; ks < 6; ++ks) {
                const int ch = kq * 24 + ks * 4 + (lane >> 4);
                const int p = (ch & ~15) | ((ch ^ keyr) & 15);
#pragma unroll
                for (int kt = 0; kt < 4; ++kt) {
                    const u32x4 a = *(LAS u32x4*)(L + L_KV + (kt * 16 + keyr) * 1536 + p * 16);
                    acc[kt] = mfma(a, qf[ks], acc[kt]);
                    acc[kt] = mfma(a, qf[6 + ks], acc[kt]);
                }
            }
            LAS float* sp = (LAS float*)(L + L_SP);
#pragma unroll
            for (int kt = 0; kt < 4; ++kt) *(LAS f32x4*)(sp + (kq * 32 + ht * 16 + (lane & 15)) * 68 + kt * 16 + 4 * (lane >> 4)) = acc[kt];
        }
        BAR();
        {
            LAS float* sp = (LAS float*)(L + L_SP);
            const int hd = t >> 4, qd = t & 15;
            f32x4 s = *(LAS f32x4*)(sp + hd * 68 + qd * 4);
#pragma unroll
            for (int k = 1; k < 4; ++k) s += *(LAS f32x4*)(sp + (k * 32 + hd) * 68 + qd * 4);
            const float m = row_max(fmaxf(fmaxf(s.x, s.y), fmaxf(s.z, s.w)));
            const float p0 = __builtin_amdgcn_exp2f(s.x - m), p1 = __builtin_amdgcn_exp2f(s.y - m), p2 = __builtin_amdgcn_exp2f(s.z - m), p3 = __builtin_amdgcn_exp2f(s.w - m);
            const u32 b01 = cvtpk(p0, p1), b23 = cvtpk(p2, p3);
            const float l = row_sum((bflo(b01) + bfhi(b01)) + (bflo(b23) + bfhi(b23)));
            *(LAS u32x2*)(L + L_P + hd * 144 + qd * 8) = u32x2{b01, b23};
            if (qd == 0) st2(rws, OFF_ML + hd * 1024 + yi * 8, u32x2{canonf(m), canonf(l)});
        }
        BAR();
        STAMP(5);
        {
            u32x4 pb[2][2];
#pragma unroll
            for (int hh = 0; hh < 2; ++hh)
#pragma unroll
                for (int k2 = 0; k2 < 2; ++k2) pb[hh][k2] = *(LAS u32x4*)(L + L_P + (hh * 16 + (lane & 15)) * 144 + k2 * 64 + (lane >> 4) * 16);
            f32x4 acc[6][2];
#pragma unroll
            for (int ct = 0; ct < 6; ++ct) { acc[ct][0] = f32x4{0.f, 0.f, 0.f, 0.f}; acc[ct][1] = f32x4{0.f, 0.f, 0.f, 0.f}; }
            const int g = lane >> 4, e = (lane >> 2) & 3, pp = lane & 3;
#pragma unroll
            for (int ct = 0; ct < 6; ++ct) {
                const int ch = w * 12 + ct * 2 + (pp >> 1);
#pragma unroll
                for (int k2 = 0; k2 < 2; ++k2) {
                    const int ka = k2 * 32 + 8 * g + 2 * e, kb = ka + 1;
                    const int pa = (ch & ~15) | ((ch ^ ka) & 15), pbx = (ch & ~15) | ((ch ^ kb) & 15);
                    const s16x4 t0 = __builtin_amdgcn_ds_read_tr16_b64_v4i16((LAS s16x4*)(L + L_KV + ka * 1536 + pa * 16 + (pp & 1) * 8));
                    const s16x4 t1 = __builtin_amdgcn_ds_read_tr16_b64_v4i16((LAS s16x4*)(L + L_KV + kb * 1536 + pbx * 16 + (pp & 1) * 8));
                    const u32x2 a0 = __builtin_bit_cast(u32x2, t0), a1 = __builtin_bit_cast(u32x2, t1);
                    const u32x4 a = {a0.x, a0.y, a1.x, a1.y};
                    acc[ct][0] = mfma(a, pb[0][k2], acc[ct][0]);
                    acc[ct][1] = mfma(a, pb[1][k2], acc[ct][1]);
                }
            }
            const int ocq = w >> 1;
#pragma unroll
            for (int hh = 0; hh < 2; ++hh)
#pragma unroll
                for (int cp = 0; cp < 3; ++cp) {
                    const f32x4 A = acc[2 * cp][hh], B = acc[2 * cp + 1][hh];
                    const u32 ax = cvtpk(A.x, A.y), ay = cvtpk(A.z, A.w), bx = cvtpk(B.x, B.y), by = cvtpk(B.z, B.w);
                    auto r0 = __builtin_amdgcn_permlane16_swap(ax, bx, false, false);
                    auto r1 = __builtin_amdgcn_permlane16_swap(ay, by, false, false);
                    const u32x4 v = {canon(r0[0]), canon(r1[0]), canon(r0[1]), canon(r1[1])};
                    const int c = (w & 1) * 96 + (2 * cp + (g & 1)) * 16 + (g >> 1) * 8;
                    st4(rws, OFF_O + (u32)(((hh * 16 + (lane & 15)) * 4 + ocq) * 128 + yi) * 384 + c * 2, v);
                }
        }
        STAMP(6);
        if (yi < 120) {
            const int rg = yi % 12, cs = yi / 12;
            const rsrc_t ro = mkrs(wo, 6144 * DM * 2);
            u32x4 ow[8];
            const u32 base = (u32)((rg * 512 + w * 64 + (lane >> 3) * 8) * DM + cs * 64 + (lane & 7) * 8) * 2;
#pragma unroll
            for (int j = 0; j < 8; ++j) ow[j] = ld4(ro, base + j * 1280, AUX_NT);
            SB();
            u32x4 g;
            const u32 goff = OFF_E5 + ((lane >> 4) * 6144 + rg * 512 + w * 64 + (lane & 15) * 4) * 4;
            for (int it = 0; it < SPIN; ++it) { CFENCE();
                g = ld4(rws, goff, AUX_SC1);
                if (!anyl(pois4(g))) break;
            }
            STAMP(7);
            f32x4 s = __builtin_bit_cast(f32x4, g);
#pragma unroll
            for (int i = 0; i < 4; ++i) { float v = s[i]; v = rs16(v, v); v = rs32(v, v); s[i] = v; }
            LAS float* cs_ = (LAS float*)(L + L_SP + w * 256);
            if (lane < 16) *(LAS f32x4*)(cs_ + lane * 4) = s;
            LGKM0();
            const f32x4 c0 = *(LAS f32x4*)(cs_ + (lane >> 3) * 8), c1 = *(LAS f32x4*)(cs_ + (lane >> 3) * 8 + 4);
            const float cv[8] = {c0.x, c0.y, c0.z, c0.w, c1.x, c1.y, c1.z, c1.w};
            float a[8];
#pragma unroll
            for (int i = 0; i < 8; ++i) a[i] = 0.f;
#pragma unroll
            for (int j = 0; j < 8; ++j) {
                float f[8]; unp8(ow[j], f);
#pragma unroll
                for (int i = 0; i < 8; ++i) a[i] += cv[j] * f[i];
            }
            const float v = rscat8(a, lane);
            const int e = ((lane >> 5) & 1) * 4 + ((lane >> 4) & 1) * 2 + ((lane >> 3) & 1);
            red[w * 64 + (lane & 7) * 8 + e] = v;
            BAR();
            if (w == 0 && lane < 16) {
                f32x4 s4 = {0.f, 0.f, 0.f, 0.f};
#pragma unroll
                for (int i = 0; i < 8; ++i) s4 += *(LAS f32x4*)(red + i * 64 + lane * 4);
                st4(rws, OFF_E6 + (rg * 640 + cs * 64) * 4 + lane * 16, u32x4{canonf(s4.x), canonf(s4.y), canonf(s4.z), canonf(s4.w)});
            }
            STAMP(8);
        }
    }
    if (bid == AMP_WG && t == 0) {
        const u64 now = __builtin_amdgcn_s_memrealtime();
        const u64 tk = __hip_atomic_load((GAS u64*)(ws + (8u << 20)), __ATOMIC_RELAXED, __HIP_MEMORY_SCOPE_AGENT);
        const u64 tgt = now + (u64)0 * (st[0] - tk);
        for (int it = 0; it < 2000000; ++it) { if (__builtin_amdgcn_s_memrealtime() >= tgt) break; __builtin_amdgcn_s_sleep(1); }
    }
}

__global__ void stampk(unsigned char* ws) { __hip_atomic_store((GAS u64*)(ws + (8u << 20)), (u64)__builtin_amdgcn_s_memrealtime(), __ATOMIC_RELAXED, __HIP_MEMORY_SCOPE_AGENT); }
extern "C" void kernel_launch(void* const* d_in, const int* in_sizes, int n_in, void* d_out, int out_size, void* d_ws, size_t ws_size, hipStream_t stream) {
    (void)in_sizes; (void)n_in; (void)out_size; (void)ws_size;
    hipFuncSetAttribute((const void*)adaln_mla_mlp_kernel, hipFuncAttributeMaxDynamicSharedMemorySize, LDS_BYTES);
    stampk<<<1, 64, 0, stream>>>((unsigned char*)d_ws);
    adaln_mla_mlp_kernel<<<NWG, NT, LDS_BYTES, stream>>>(
        (const u16*)d_in[0], (const u16*)d_in[1], (const u16*)d_in[2], (const u16*)d_in[3], (const u16*)d_in[4], (const u16*)d_in[5],
        (const u16*)d_in[7], (const u16*)d_in[8], (const u16*)d_in[9], (const u16*)d_in[10], (u16*)d_out, (unsigned char*)d_ws);
}
```

```cpp
#include <hip/hip_runtime.h>
#include <stdint.h>

typedef unsigned u32;
typedef unsigned long long u64;
typedef unsigned short u16;
typedef u32 u32x2 __attribute__((ext_vector_type(2)));
typedef u32 u32x3 __attribute__((ext_vector_type(3)));
typedef u32 u32x4 __attribute__((ext_vector_type(4)));
typedef float f32x2 __attribute__((ext_vector_type(2)));
typedef float f32x4 __attribute__((ext_vector_type(4)));
typedef short bf16x8 __attribute__((ext_vector_type(8)));
typedef short s16x4 __attribute__((ext_vector_type(4)));
typedef __amdgpu_buffer_rsrc_t rsrc_t;
#define LAS __attribute__((address_space(3)))
#define GAS __attribute__((address_space(1)))

constexpr int NH = 32, SK = 8192, HD = 192, DM = 640, DC = 768, DFF = 1536;
constexpr int NWG = 256, NT = 512;

#define UK_EARLY 0
#define D_UK 0
#define D_WQ 40
#define G_POS 1
#define POIS_POS 1
#define CKV_EARLY 0
#define WO_POS 1
constexpr u32 OFF_EPOCH = 0;
constexpr u32 OFF_PF = 256;
constexpr u32 OFF_E1 = OFF_PF + 128 * 16;
constexpr u32 OFF_E2 = OFF_E1 + 8 * 4 * 8 * 256;
constexpr u32 OFF_E3 = OFF_E2 + 4 * 32 * 96 * 16;
constexpr u32 OFF_ML = OFF_E3 + 32 * 4 * 768;
constexpr u32 OFF_E5 = OFF_ML + 32 * 128 * 16;
constexpr u32 OFF_E6 = OFF_E5 + 4 * 3072 * 16;
constexpr u32 OFF_G = OFF_E6 + 12 * 640 * 4;
constexpr u32 OFF_HID = OFF_G + 1280 * 16;
constexpr u32 OFF_O = OFF_HID + 768 * 16;
constexpr u32 WS_BYTES = OFF_O + 32 * 4 * 128 * 384;
constexpr u32 OFF_DIAG = WS_BYTES;
static_assert(OFF_O % 128 == 0 && OFF_E3 % 128 == 0 && OFF_E5 % 128 == 0 && OFF_E2 % 256 == 0 && OFF_E6 % 128 == 0 && OFF_E1 % 256 == 0, "alignment");

constexpr int L_KV = 0;
constexpr int L_SP = 98304;
constexpr int L_P = L_SP + 4 * 32 * 68 * 4;
constexpr int L_RQ = L_P + 32 * 144;
constexpr int L_RW = L_RQ + 8 * 192 * 4;
constexpr int LDS_BYTES = L_RW + 8 * 64 * 4;
constexpr int L_RM = 0;
constexpr int L_Q = L_RM + 4096;
constexpr int L_RA = L_Q + 768;
constexpr int L_RV1 = L_RA + 768;
constexpr int L_RV2 = L_RV1 + 6144;
constexpr int L_RG = L_RV2 + 6144;
constexpr int L_GS = L_RG + 1024;
constexpr int L_RP = L_GS + 7680;
constexpr int L_RF = L_RP + 2048;
constexpr int L_ST = L_RF + 1024;
constexpr int L_R2 = L_ST + 64;
constexpr int L_R6 = L_R2 + 512;
constexpr int SPIN = 1 << 16;
constexpr u32 POISON = 0xFFFFFFFFu;
constexpr int AUX_SC1 = 16, AUX_NT = 2;
constexpr u64 TAGMUL = 0x9E3779B97F4A7C15ull;

__device__ __forceinline__ float bflo(u32 v) { return __uint_as_float(v << 16); }
__device__ __forceinline__ float bfhi(u32 v) { return __uint_as_float(v & 0xFFFF0000u); }
__device__ __forceinline__ float bf1(u16 v) { return __uint_as_float((u32)v << 16); }
typedef __bf16 bf16x2_t __attribute__((ext_vector_type(2)));
__device__ __forceinline__ u32 cvtpk(float lo, float hi) { f32x2 v = {lo, hi}; return __builtin_bit_cast(u32, __builtin_convertvector(v, bf16x2_t)); }
__device__ __forceinline__ u32 canon(u32 v) { return v < 0xFFFFFFFEu ? v : 0xFFFFFFFEu; }
__device__ __forceinline__ u32 canonf(float v) { return canon(__float_as_uint(v)); }
__device__ __forceinline__ rsrc_t mkrs(const void* p, u32 bytes) { return __builtin_amdgcn_make_buffer_rsrc((void*)p, 0, bytes, 0x00020000); }
__device__ __forceinline__ u32x4 ld4(rsrc_t r, u32 off, int aux) { return aux == AUX_SC1 ? __builtin_amdgcn_raw_buffer_load_b128(r, off, 0, AUX_SC1) : __builtin_amdgcn_raw_buffer_load_b128(r, off, 0, AUX_NT); }
__device__ __forceinline__ u32x3 ld3(rsrc_t r, u32 off, int aux) { return aux == AUX_SC1 ? __builtin_amdgcn_raw_buffer_load_b96(r, off, 0, AUX_SC1) : __builtin_amdgcn_raw_buffer_load_b96(r, off, 0, AUX_NT); }
__device__ __forceinline__ void st4(rsrc_t r, u32 off, u32x4 v) { __builtin_amdgcn_raw_buffer_store_b128(v, r, off, 0, AUX_SC1); }
__device__ __forceinline__ void st2(rsrc_t r, u32 off, u32x2 v) { __builtin_amdgcn_raw_buffer_store_b64(v, r, off, 0, AUX_SC1); }
#define CFENCE() asm volatile("" ::: "memory")
#define PSL 0
#define PSLEEP() do { if (PSL > 0) __builtin_amdgcn_s_sleep(PSL); } while (0)
#define SB() __builtin_amdgcn_sched_barrier(0)
#define BAR() asm volatile("s_waitcnt lgkmcnt(0)\n\ts_barrier" ::: "memory")
#define LGKM0() asm volatile("s_waitcnt lgkmcnt(0)" ::: "memory")
#define DRAIN() asm volatile("s_waitcnt vmcnt(0)" ::: "memory")

template <int CTRL> __device__ __forceinline__ float dpp(float x) { return __int_as_float(__builtin_amdgcn_update_dpp(0, __float_as_int(x), CTRL, 0xf, 0xf, true)); }
__device__ __forceinline__ float rs32(float a, float b) { auto r = __builtin_amdgcn_permlane32_swap(__float_as_uint(a), __float_as_uint(b), false, false); return __uint_as_float(r[0]) + __uint_as_float(r[1]); }
__device__ __forceinline__ float rs16(float a, float b) { auto r = __builtin_amdgcn_permlane16_swap(__float_as_uint(a), __float_as_uint(b), false, false); return __uint_as_float(r[0]) + __uint_as_float(r[1]); }
__device__ __forceinline__ float row_sum(float x) { x += dpp<0x128>(x); x += dpp<0x124>(x); x += dpp<0x122>(x); x += dpp<0x121>(x); return x; }
__device__ __forceinline__ float row_max(float x) { x = fmaxf(x, dpp<0x128>(x)); x = fmaxf(x, dpp<0x124>(x)); x = fmaxf(x, dpp<0x122>(x)); x = fmaxf(x, dpp<0x121>(x)); return x; }
__device__ __forceinline__ float wave_sum(float x) { x = row_sum(x); x = rs16(x, x); x = rs32(x, x); return x; }
__device__ __forceinline__ float wave_max(float x) {
    x = row_max(x);
    { auto r = __builtin_amdgcn_permlane16_swap(__float_as_uint(x), __float_as_uint(x), false, false); x = fmaxf(__uint_as_float(r[0]), __uint_as_float(r[1])); }
    { auto r = __builtin_amdgcn_permlane32_swap(__float_as_uint(x), __float_as_uint(x), false, false); x = fmaxf(__uint_as_float(r[0]), __uint_as_float(r[1])); }
    return x;
}
__device__ __forceinline__ float rscat8(const float (&a)[8], int lane) {
    float r0 = rs32(a[0], a[4]), r1 = rs32(a[1], a[5]), r2 = rs32(a[2], a[6]), r3 = rs32(a[3], a[7]);
    float s0 = rs16(r0, r2), s1 = rs16(r1, r3);
    float t0 = s0 + dpp<0x128>(s0), t1 = s1 + dpp<0x128>(s1);
    return (lane & 8) ? t1 : t0;
}
__device__ __forceinline__ bool pois4(u32x4 v) { u32 m = max(max(v.x, v.y), max(v.z, v.w)); return m == POISON; }
__device__ __forceinline__ bool pois3(u32x3 v) { u32 m = max(max(v.x, v.y), v.z); return m == POISON; }
__device__ __forceinline__ bool anyl(bool b) { return __builtin_amdgcn_ballot_w64(b) != 0ull; }
__device__ __forceinline__ f32x4 mfma(u32x4 a, u32x4 b, f32x4 c) { return __builtin_amdgcn_mfma_f32_16x16x32_bf16(__builtin_bit_cast(bf16x8, a), __builtin_bit_cast(bf16x8, b), c, 0, 0, 0); }
__device__ __forceinline__ float silu(float v) { return v / (1.f + __expf(-v)); }
__device__ __forceinline__ void unp8(u32x4 v, float (&f)[8]) { f[0] = bflo(v.x); f[1] = bfhi(v.x); f[2] = bflo(v.y); f[3] = bfhi(v.y); f[4] = bflo(v.z); f[5] = bfhi(v.z); f[6] = bflo(v.w); f[7] = bfhi(v.w); }
__device__ __forceinline__ void unp6(u32x3 v, float (&f)[6]) { f[0] = bflo(v.x); f[1] = bfhi(v.x); f[2] = bflo(v.y); f[3] = bfhi(v.y); f[4] = bflo(v.z); f[5] = bfhi(v.z); }

__device__ __forceinline__ void wait_until(u64 t0, int ticks) {
    if (ticks > 0) { for (int i = 0; i < 4096; ++i) { if (__builtin_amdgcn_s_memrealtime() - t0 >= (u64)ticks) break; __builtin_amdgcn_s_sleep(1); } }
}
#define STAMP(i) do { } while (0)

__device__ __forceinline__ void ln_stats(const float (&v)[10], float& mean, float& rstd) {
    float s = 0.f;
#pragma unroll
    for (int i = 0; i < 10; ++i) s += v[i];
    mean = wave_sum(s) * (1.f / 640.f);
    float q = 0.f;
#pragma unroll
    for (int i = 0; i < 10; ++i) { float d = v[i] - mean; q += d * d; }
    float var = wave_sum(q) * (1.f / 640.f);
    rstd = 1.f / sqrtf(var + 1e-5f);
}

#define CAS __attribute__((address_space(4)))
#define PIN1(a) asm volatile("" : "+v"(a))
template <int CTRL, int BANK> __device__ __forceinline__ float dppb(float old, float x) { return __int_as_float(__builtin_amdgcn_update_dpp(__float_as_int(old), __float_as_int(x), CTRL, 0xf, BANK, false)); }
__device__ __forceinline__ u32x2 ld2(rsrc_t r, u32 off) { return __builtin_amdgcn_raw_buffer_load_b64(r, off, 0, AUX_SC1); }
__device__ __forceinline__ u32 ld1(rsrc_t r, u32 off) { return __builtin_amdgcn_raw_buffer_load_b32(r, off, 0, AUX_SC1); }
__device__ __forceinline__ bool tagbad(u32x4 g, u32 tlo, u32 thi) { return (g.z != tlo) | (g.w != thi); }
__device__ __forceinline__ f32x2 up2(u32 v) { return f32x2{bflo(v), bfhi(v)}; }
__device__ __forceinline__ f32x2 splat(float v) { return f32x2{v, v}; }

__global__ __launch_bounds__(NT, 2) void adaln_mla_mlp_kernel(
    const u16* __restrict__ x, const u16* __restrict__ ckv, const u16* __restrict__ wq, const u16* __restrict__ wuk,
    const u16* __restrict__ wuv, const u16* __restrict__ wo, const u16* __restrict__ w1, const u16* __restrict__ w2,
    const u16* __restrict__ cc, const u16* __restrict__ wmod, u16* __restrict__ out, unsigned char* __restrict__ ws) {
    extern __shared__ __attribute__((aligned(16))) unsigned char smem[];
    LAS unsigned char* L = (LAS unsigned char*)smem;
    STAMP(0);
    const u64 tstart = __builtin_amdgcn_s_memrealtime();
    asm volatile("" ::"s"(x), "s"(ckv), "s"(wq), "s"(wuk), "s"(wuv), "s"(wo), "s"(w1), "s"(w2), "s"(cc), "s"(wmod), "s"(out), "s"(ws));
    const int t = threadIdx.x, lane = t & 63, w = __builtin_amdgcn_readfirstlane(t >> 6);
    const int bid = blockIdx.x;
    const rsrc_t rws = mkrs(ws, WS_BYTES);
    const u64 epoch = *(const CAS u64*)(ws + OFF_EPOCH);
    const u64 tag = (epoch + 1ull) * TAGMUL;
    const u32 tlo = (u32)tag, thi = (u32)(tag >> 32);
    const u32x4 PZ = {POISON, POISON, POISON, POISON};

    if (bid < 128) {
        const int xi = bid, h = xi & 31, cq = xi >> 5;
        const rsrc_t rmod = mkrs(wmod, DM * 3840 * 2);
        const bool isM = xi < 80;
        u32x4 mw[3]; u16 mc[3]; u32 xr[5], xm = 0;
        const int m_rg = xi & 7, m_s = xi >> 3;
        if (isM) {
            const int row0 = m_rg * 80 + w * 10 + (lane >> 4);
            const u32 off = (u32)(row0 * 3840 + ((lane >> 3) & 1) * 640 + m_s * 64 + (lane & 7) * 8) * 2;
            mw[0] = ld4(rmod, off, AUX_NT); mw[1] = ld4(rmod, off + 4 * 7680, AUX_NT); mw[2] = ld4(rmod, lane < 32 ? off + 8 * 7680 : 0x80000000u, AUX_NT);
            mc[0] = cc[row0]; mc[1] = cc[row0 + 4]; mc[2] = cc[row0 + (lane < 32 ? 8 : 0)];
            if (w == 0) {
#pragma unroll
                for (int i = 0; i < 5; ++i) xr[i] = ((const u32*)x)[lane + 64 * i];
                xm = ((const u32*)x)[32 * m_s + (lane & 31)];
            }
            SB();
        }
        const u32 e3off = OFF_E3 + (u32)((cq * 12 + (lane >> 2)) * 32 + h) * 64 + (lane & 3) * 16;
        if (w == 7 && lane < 48) st4(rws, e3off, PZ);
        const u32 opz = OFF_O + (u32)(h * 4 + cq) * 49152 + t * 16;
        const rsrc_t ruk = mkrs(wuk, DC * 6144 * 2);
        const u32 wbase = (u32)(((cq * 192 + w * 24 + (lane >> 5)) * 32 + h) * 192 + 6 * (lane & 31)) * 2;
        u32x3 uw[12];
#pragma unroll
        for (int j = 0; j < UK_EARLY; ++j) uw[j] = ld3(ruk, wbase + j * 2 * 12288, AUX_NT);
        SB();
        if (isM) {
            LAS float* red = (LAS float*)(L + L_RM);
            f32x2 lnx = {0.f, 0.f};
            if (w == 0) {
                float xv[10];
#pragma unroll
                for (int i = 0; i < 5; ++i) { xv[2 * i] = bflo(xr[i]); xv[2 * i + 1] = bfhi(xr[i]); }
                float mean, rstd; ln_stats(xv, mean, rstd);
                lnx = f32x2{(bflo(xm) - mean) * rstd, (bfhi(xm) - mean) * rstd};
            }
            float a[8], f[8];
            const float s0 = silu(bf1(mc[0])), s1 = silu(bf1(mc[1])), s2 = silu(bf1(mc[2]));
            unp8(mw[0], f);
#pragma unroll
            for (int i = 0; i < 8; ++i) a[i] = s0 * f[i];
            unp8(mw[1], f);
#pragma unroll
            for (int i = 0; i < 8; ++i) a[i] += s1 * f[i];
            unp8(mw[2], f);
#pragma unroll
            for (int i = 0; i < 8; ++i) a[i] += s2 * f[i];
            const float r0 = rs32(a[0], a[4]), r1 = rs32(a[1], a[5]), r2 = rs32(a[2], a[6]), r3 = rs32(a[3], a[7]);
            const f32x2 sv = {rs16(r0, r2), rs16(r1, r3)};
            *(LAS f32x2*)(red + w * 128 + ((lane >> 3) & 1) * 64 + (lane & 7) * 8 + 4 * (lane >> 5) + 2 * ((lane >> 4) & 1)) = sv;
            BAR();
            if (w == 0) {
                f32x2 p[8];
#pragma unroll
                for (int k = 0; k < 8; ++k) p[k] = *(LAS f32x2*)(red + k * 128 + 2 * lane);
                const f32x2 s = ((p[0] + p[1]) + (p[2] + p[3])) + ((p[4] + p[5]) + (p[6] + p[7]));
                auto rx = __builtin_amdgcn_permlane32_swap(__float_as_uint(s.x), __float_as_uint(s.x), false, false);
                auto ry = __builtin_amdgcn_permlane32_swap(__float_as_uint(s.y), __float_as_uint(s.y), false, false);
                float hx = lnx.x * __uint_as_float(rx[1]) + __uint_as_float(rx[0]), hy = lnx.y * __uint_as_float(ry[1]) + __uint_as_float(ry[0]);
                if (m_rg == 0) { hx += lnx.x; hy += lnx.y; }
                const int g = 32 * m_s + lane, gm = g / 80, gr = g - gm * 80, gw_ = gr / 10;
                if (lane < 32) st4(rws, OFF_E1 + (u32)(((m_rg * 4 + gm) * 8 + gw_) * 16 + (gr - gw_ * 10)) * 16, u32x4{__float_as_uint(hx), __float_as_uint(hy), tlo, thi});
            }
        }
        STAMP(1);
        wait_until(tstart, D_UK);
#pragma unroll
        for (int j = UK_EARLY; j < 12; ++j) uw[j] = ld3(ruk, wbase + j * 2 * 12288, AUX_NT);
        SB();
#pragma unroll
        for (int j = 0; j < 6; ++j) st4(rws, opz + j * 8192, PZ);
        SB();
        STAMP(11);
        f32x2 wf[12][3];
#pragma unroll
        for (int j = 0; j < 12; ++j) { wf[j][0] = up2(uw[j].x); wf[j][1] = up2(uw[j].y); wf[j][2] = up2(uw[j].z); PIN1(wf[j][0]); PIN1(wf[j][1]); PIN1(wf[j][2]); }
        DRAIN();
        BAR();
        if (w == 7 && lane == 0) st4(rws, OFF_PF + xi * 16, u32x4{0u, 0u, tlo, thi});
        STAMP(12);
        u32x4 gw[5]; u16 cv[5];
        const int rs = lane >> 2, ck = lane & 3;
        int g_s = 0;
        {
            LAS float* qs = (LAS float*)(L + L_Q);
            if (w < 6) {
                const u32 goff = OFF_E2 + (u32)(((lane >> 4) * 32 + h) * 96 + 16 * w + (lane & 15)) * 16;
                u32x4 g;
                for (int it = 0; it < SPIN; ++it) { CFENCE();
                    g = ld4(rws, goff, AUX_SC1);
                    if (!anyl(tagbad(g, tlo, thi))) break; PSLEEP();
                }
                float s = rs32(__uint_as_float(g.x), __uint_as_float(g.y));
                s = rs16(s, s);
                if (!(lane & 16)) qs[2 * (16 * w + (lane & 15)) + (lane >> 5)] = s;
            }
            STAMP(2);
            BAR();
            const f32x2 q0 = *(LAS f32x2*)(qs + 6 * (lane & 31)), q1 = *(LAS f32x2*)(qs + 6 * (lane & 31) + 2), q2 = *(LAS f32x2*)(qs + 6 * (lane & 31) + 4);
            float p[12];
#pragma unroll
            for (int j = 0; j < 12; ++j) { const f32x2 d = wf[j][0] * q0 + wf[j][1] * q1 + wf[j][2] * q2; p[j] = d.x + d.y; }
            float r[6], s[3];
#pragma unroll
            for (int j = 0; j < 6; ++j) r[j] = rs16(p[j], p[j + 6]);
#pragma unroll
            for (int j = 0; j < 3; ++j) {
                const float own = (lane & 8) ? r[j + 3] : r[j], oth = (lane & 8) ? r[j] : r[j + 3];
                float v = own + dpp<0x128>(oth);
                v += dpp<0x141>(v); v += dpp<0x1B>(v); v += dpp<0xB1>(v);
                s[j] = v;
            }
            LAS float* red = (LAS float*)(L + L_RA);
            {
                const int j = lane & 7;
                const float v = j == 0 ? s[0] : (j == 1 ? s[1] : s[2]);
                if (j < 3) red[w * 24 + 2 * (j + 3 * ((lane >> 3) & 1) + 6 * ((lane >> 4) & 1)) + (lane >> 5)] = v;
            }
            BAR();
            if (w == 0 && lane < 48) {
                const float sc = 0.07216878364870322f * 1.4426950408889634f;
                const int c0 = (lane >> 3) * 32 + (lane & 3) * 8;
                const f32x4 p0 = *(LAS f32x4*)(red + c0) * sc, p1 = *(LAS f32x4*)(red + c0 + 4) * sc;
                const float v8[8] = {p0.x, p0.y, p0.z, p0.w, p1.x, p1.y, p1.z, p1.w};
                u32 o[4];
#pragma unroll
                for (int i = 0; i < 4; ++i) {
                    const u32 hi = cvtpk(v8[2 * i], v8[2 * i + 1]);
                    const u32 lo = cvtpk(v8[2 * i] - bflo(hi), v8[2 * i + 1] - bfhi(hi));
                    o[i] = canon((lane & 4) ? lo : hi);
                }
                st4(rws, e3off, u32x4{o[0], o[1], o[2], o[3]});
            }
            BAR();
        }
        STAMP(3);
        const rsrc_t ruv = mkrs(wuv, DC * 6144 * 2);
        u32x3 vw[12];
#pragma unroll
        for (int j = 0; j < 12; ++j) vw[j] = ld3(ruv, wbase + j * 2 * 12288, AUX_NT);
        SB();
        if (xi >= 48) {
            const int gi = xi - 48;
            g_s = 2 * ((gi >> 4) * 8 + (gi & 7)) + ((gi >> 3) & 1);
            const int row0 = t >> 2;
            const u32 off = (u32)(row0 * 3840 + 1280 + 32 * g_s + 8 * ck) * 2;
#pragma unroll
            for (int j = 0; j < 5; ++j) gw[j] = ld4(rmod, off + j * 128 * 7680, AUX_NT);
#pragma unroll
            for (int j = 0; j < 5; ++j) cv[j] = cc[row0 + 128 * j];
        } else {
            const rsrc_t r1 = mkrs(w1, DM * DFF * 2);
            const u32 b1 = (u32)((w * 80 + rs) * DFF + 32 * xi + 8 * ck) * 2;
#pragma unroll
            for (int j = 0; j < 5; ++j) gw[j] = ld4(r1, b1 + j * 16 * DFF * 2, AUX_NT);
#pragma unroll
            for (int j = 0; j < 5; ++j) cv[j] = x[w * 80 + 16 * j + rs];
        }
        SB();
        if (xi >= 48) {
            float a[8];
#pragma unroll
            for (int i = 0; i < 8; ++i) a[i] = 0.f;
#pragma unroll
            for (int j = 0; j < 5; ++j) {
                float f[8]; unp8(gw[j], f); const float s = silu(bf1(cv[j]));
#pragma unroll
                for (int i = 0; i < 8; ++i) a[i] += s * f[i];
            }
            float v = rscat8(a, lane);
            v += dpp<0x104>(v);
            const int e = ((lane >> 5) & 1) * 4 + ((lane >> 4) & 1) * 2 + ((lane >> 3) & 1);
            LAS float* redg = (LAS float*)(L + L_RG);
            if (!(lane & 4)) redg[w * 32 + ck * 8 + e] = v;
            BAR();
            if (w == 0 && lane < 16) {
                f32x2 s = {0.f, 0.f};
#pragma unroll
                for (int i = 0; i < 8; ++i) s += *(LAS f32x2*)(redg + i * 32 + lane * 2);
                st4(rws, OFF_G + (g_s * 16 + lane) * 16, u32x4{__float_as_uint(s.x), __float_as_uint(s.y), tlo, thi});
            }
        }
        f32x2 vf[12][3];
#pragma unroll
        for (int j = 0; j < 12; ++j) { vf[j][0] = up2(vw[j].x); vf[j][1] = up2(vw[j].y); vf[j][2] = up2(vw[j].z); PIN1(vf[j][0]); PIN1(vf[j][1]); PIN1(vf[j][2]); }
        STAMP(4);
        {
            const u32 mlA = OFF_ML + (u32)(h * 128 + lane) * 16;
            const u32 obase = OFF_O + (u32)((h * 4 + cq) * 128 + w * 16 + (lane >> 5)) * 384 + (lane & 31) * 12;
            LAS float* red1 = (LAS float*)(L + L_RV1);
            LAS float* red2 = (LAS float*)(L + L_RV2);
            u32x4 ma, mb;
            for (int it = 0; it < SPIN; ++it) { CFENCE();
                ma = ld4(rws, mlA, AUX_SC1); mb = ld4(rws, mlA + 1024, AUX_SC1);
                if (!anyl(tagbad(ma, tlo, thi) | tagbad(mb, tlo, thi))) break; PSLEEP();
            }
            STAMP(5);
            u32x3 oa[8];
#pragma unroll
            for (int j = 0; j < 8; ++j) oa[j] = ld3(rws, obase + j * 768, AUX_SC1);
            SB();
            float wA;
            {
                const float m0 = __uint_as_float(ma.x), l0 = __uint_as_float(ma.y), m1 = __uint_as_float(mb.x), l1 = __uint_as_float(mb.y);
                const float M = wave_max(fmaxf(m0, m1));
                const float w0 = __builtin_amdgcn_exp2f(m0 - M), w1_ = __builtin_amdgcn_exp2f(m1 - M);
                const float Lsum = wave_sum(w0 * l0 + w1_ * l1);
                const float wn = (w < 4 ? w0 : w1_) * __builtin_amdgcn_rcpf(Lsum);
                const int srcl = (16 * w + 2 * (lane & 7) + (lane >> 5)) & 63;
                wA = __int_as_float(__builtin_amdgcn_ds_bpermute(srcl * 4, __float_as_int(wn)));
            }
            f32x2 pr[8][3];
            u32 pend = 0;
#define OSTEP(j) { if (anyl(pois3(oa[j]))) pend |= 1u << j; else { const f32x2 ww = splat(dpp<0x150 + j>(wA)); pr[j][0] = up2(oa[j].x) * ww; pr[j][1] = up2(oa[j].y) * ww; pr[j][2] = up2(oa[j].z) * ww; } }
            OSTEP(0) OSTEP(1) OSTEP(2) OSTEP(3) OSTEP(4) OSTEP(5) OSTEP(6) OSTEP(7)
#undef OSTEP
            for (int it = 0; it < SPIN && pend; ++it) { CFENCE();
#pragma unroll
                for (int j = 0; j < 8; ++j) if (pend & (1u << j)) oa[j] = ld3(rws, obase + j * 768, AUX_SC1);
#define OSTEP(j) if (pend & (1u << j)) { if (!anyl(pois3(oa[j]))) { pend &= ~(1u << j); const f32x2 ww = splat(dpp<0x150 + j>(wA)); pr[j][0] = up2(oa[j].x) * ww; pr[j][1] = up2(oa[j].y) * ww; pr[j][2] = up2(oa[j].z) * ww; } }
                OSTEP(0) OSTEP(1) OSTEP(2) OSTEP(3) OSTEP(4) OSTEP(5) OSTEP(6) OSTEP(7)
#undef OSTEP
            }
            STAMP(6);
            f32x2 a2[3];
#pragma unroll
            for (int k = 0; k < 3; ++k) a2[k] = ((pr[0][k] + pr[1][k]) + (pr[2][k] + pr[3][k])) + ((pr[4][k] + pr[5][k]) + (pr[6][k] + pr[7][k]));
            {
                LAS float* dst = red1 + w * 192 + 6 * (lane & 31) + 3 * (lane >> 5);
                dst[0] = rs32(a2[0].x, a2[1].y); dst[1] = rs32(a2[0].y, a2[2].x); dst[2] = rs32(a2[1].x, a2[2].y);
            }
            BAR();
            float cx = 0.f;
            {
                const int i = lane & 15, cidx = w * 24 + 2 * (i < 12 ? i : 11) + (lane >> 5);
                float p[8];
#pragma unroll
                for (int k = 0; k < 8; ++k) p[k] = red1[k * 192 + cidx];
                cx = ((p[0] + p[1]) + (p[2] + p[3])) + ((p[4] + p[5]) + (p[6] + p[7]));
            }
            f32x2 c2[3] = {{0.f, 0.f}, {0.f, 0.f}, {0.f, 0.f}};
#define VSTEP(j) { const f32x2 cvv = splat(dpp<0x150 + j>(cx)); c2[0] += cvv * vf[j][0]; c2[1] += cvv * vf[j][1]; c2[2] += cvv * vf[j][2]; }
            VSTEP(0) VSTEP(1) VSTEP(2) VSTEP(3) VSTEP(4) VSTEP(5) VSTEP(6) VSTEP(7) VSTEP(8) VSTEP(9) VSTEP(10) VSTEP(11)
#undef VSTEP
            {
                LAS float* dst = red2 + w * 192 + 6 * (lane & 31) + 3 * (lane >> 5);
                dst[0] = rs32(c2[0].x, c2[1].y); dst[1] = rs32(c2[0].y, c2[2].x); dst[2] = rs32(c2[1].x, c2[2].y);
            }
            BAR();
            if (t < 96) {
                f32x2 p[8];
#pragma unroll
                for (int k = 0; k < 8; ++k) p[k] = *(LAS f32x2*)(red2 + k * 192 + t * 2);
                const f32x2 s = ((p[0] + p[1]) + (p[2] + p[3])) + ((p[4] + p[5]) + (p[6] + p[7]));
                st4(rws, OFF_E5 + (u32)(cq * 3072 + h * 96 + t) * 16, u32x4{__float_as_uint(s.x), __float_as_uint(s.y), tlo, thi});
            }
        }
        STAMP(7);
        if (xi < 48) {
            LAS float* gs = (LAS float*)(L + L_GS);
            {
                u32x4 g0, g1;
                const bool two = t < 448;
                for (int it = 0; it < SPIN; ++it) { CFENCE();
                    g0 = ld4(rws, OFF_G + t * 16, AUX_SC1); bool bad = tagbad(g0, tlo, thi);
                    if (two) { g1 = ld4(rws, OFF_G + (t + 512) * 16, AUX_SC1); bad |= tagbad(g1, tlo, thi); }
                    if (!anyl(bad)) break; PSLEEP();
                }
                *(LAS u32x2*)(gs + 2 * t) = u32x2{g0.x, g0.y};
                if (two) *(LAS u32x2*)(gs + 2 * (t + 512)) = u32x2{g1.x, g1.y};
            }
            BAR();
            f32x2 wA[5][4], u[4], v1[4], v2[4];
            float xv[5], ga[5];
#pragma unroll
            for (int k = 0; k < 4; ++k) { u[k] = f32x2{0.f, 0.f}; v1[k] = f32x2{0.f, 0.f}; v2[k] = f32x2{0.f, 0.f}; }
#pragma unroll
            for (int j = 0; j < 5; ++j) {
                const int m = w * 80 + 16 * j + rs;
                const float gav = gs[m], sbv = gs[640 + m], cbp = 1.f + gs[1280 + m];
                xv[j] = bf1(cv[j]); ga[j] = gav;
                const f32x2 A = splat(gav * cbp), X0 = splat(xv[j] * cbp), C = splat(cbp), S = splat(sbv);
                const f32x2 f[4] = {up2(gw[j].x), up2(gw[j].y), up2(gw[j].z), up2(gw[j].w)};
#pragma unroll
                for (int k = 0; k < 4; ++k) { wA[j][k] = A * f[k]; u[k] += X0 * f[k]; v1[k] += C * f[k]; v2[k] += S * f[k]; PIN1(wA[j][k]); }
            }
            const int e = ((lane >> 5) & 1) * 4 + ((lane >> 4) & 1) * 2 + ((lane >> 3) & 1);
            LAS float* rp = (LAS float*)(L + L_RP);
            {
                float a[8] = {v1[0].x, v1[0].y, v1[1].x, v1[1].y, v1[2].x, v1[2].y, v1[3].x, v1[3].y};
                float v = rscat8(a, lane); v += dpp<0x104>(v);
                if (!(lane & 4)) rp[w * 32 + ck * 8 + e] = v;
                float b[8] = {v2[0].x, v2[0].y, v2[1].x, v2[1].y, v2[2].x, v2[2].y, v2[3].x, v2[3].y};
                v = rscat8(b, lane); v += dpp<0x104>(v);
                if (!(lane & 4)) rp[256 + w * 32 + ck * 8 + e] = v;
            }
            BAR();
            f32x2 V1 = {0.f, 0.f}, V2 = {0.f, 0.f};
            if (w == 0) {
#pragma unroll
                for (int k = 0; k < 8; ++k) { V1 += *(LAS f32x2*)(rp + k * 32 + (lane & 15) * 2); V2 += *(LAS f32x2*)(rp + 256 + k * 32 + (lane & 15) * 2); }
                PIN1(V1); PIN1(V2);
            }
#pragma unroll
            for (int k = 0; k < 4; ++k) PIN1(u[k]);
            STAMP(8);
            LAS float* r6 = (LAS float*)(L + L_R6);
            {
                const bool act = t < 480;
                const int pg = t / 160, q = t - pg * 160;
                const u32 goff = OFF_E6 + (u32)(4 * pg * 640 + 4 * q) * 4;
                u32x4 eg[4];
#pragma unroll
                for (int k = 0; k < 4; ++k) eg[k] = u32x4{0u, 0u, 0u, 0u};
                for (int it = 0; it < SPIN; ++it) { CFENCE();
                    if (act) {
#pragma unroll
                        for (int k = 0; k < 4; ++k) eg[k] = ld4(rws, goff + k * 2560, AUX_SC1);
                    }
                    if (!anyl(pois4(eg[0]) | pois4(eg[1]) | pois4(eg[2]) | pois4(eg[3]))) break; PSLEEP();
                }
                const f32x4 s = (__builtin_bit_cast(f32x4, eg[0]) + __builtin_bit_cast(f32x4, eg[1])) + (__builtin_bit_cast(f32x4, eg[2]) + __builtin_bit_cast(f32x4, eg[3]));
                if (act) *(LAS f32x4*)(r6 + pg * 640 + 4 * q) = s;
            }
            STAMP(9);
            BAR();
            float x1[5], sum = 0.f;
#pragma unroll
            for (int j = 0; j < 5; ++j) {
                const int m = w * 80 + 16 * j + rs;
                const float at = (r6[m] + r6[640 + m]) + r6[1280 + m];
                x1[j] = xv[j] + ga[j] * at; sum += x1[j];
                const f32x2 A = splat(at);
#pragma unroll
                for (int k = 0; k < 4; ++k) u[k] += A * wA[j][k];
            }
            sum += dpp<0x124>(sum); sum += dpp<0x128>(sum); sum = rs16(sum, sum); sum = rs32(sum, sum);
            const float mw_ = sum * (1.f / 80.f);
            float q = 0.f;
#pragma unroll
            for (int j = 0; j < 5; ++j) { const float d = x1[j] - mw_; q += d * d; }
            q += dpp<0x124>(q); q += dpp<0x128>(q); q = rs16(q, q); q = rs32(q, q);
            {
                float a[8] = {u[0].x, u[0].y, u[1].x, u[1].y, u[2].x, u[2].y, u[3].x, u[3].y};
                float v = rscat8(a, lane); v += dpp<0x104>(v);
                LAS float* rf = (LAS float*)(L + L_RF);
                if (!(lane & 4)) rf[w * 32 + ck * 8 + e] = v;
                LAS float* sst = (LAS float*)(L + L_ST);
                if (lane == 0) *(LAS f32x2*)(sst + 2 * w) = f32x2{mw_, q};
                BAR();
                if (w == 0 && lane < 16) {
                    f32x2 p[8], s8[8];
#pragma unroll
                    for (int k = 0; k < 8; ++k) { p[k] = *(LAS f32x2*)(rf + k * 32 + lane * 2); s8[k] = *(LAS f32x2*)(sst + 2 * k); }
                    const f32x2 U = ((p[0] + p[1]) + (p[2] + p[3])) + ((p[4] + p[5]) + (p[6] + p[7]));
                    const f32x2 SS = ((s8[0] + s8[1]) + (s8[2] + s8[3])) + ((s8[4] + s8[5]) + (s8[6] + s8[7]));
                    const float mean = SS.x * 0.125f;
                    float dv = 0.f;
#pragma unroll
                    for (int k = 0; k < 8; ++k) { const float d = s8[k].x - mean; dv += d * d; }
                    const float var = (SS.y + 80.f * dv) * (1.f / 640.f);
                    const float rstd = __builtin_amdgcn_rsqf(var + 1e-5f);
                    const float h0 = fmaxf(rstd * (U.x - mean * V1.x) + V2.x, 0.f), h1 = fmaxf(rstd * (U.y - mean * V1.y) + V2.y, 0.f);
                    st4(rws, OFF_HID + (u32)(xi * 16 + lane) * 16, u32x4{__float_as_uint(h0), __float_as_uint(h1), tlo, thi});
                }
            }
            STAMP(10);
        } else if (xi < 88) {
            const int fi = xi - 48, s2 = 5 * (fi & 7) + (fi >> 3);
            const rsrc_t r2 = mkrs(w2, DFF * DM * 2);
            const int chunk = t & 1, q = t >> 1;
            u32x4 fw[6];
#pragma unroll
            for (int i = 0; i < 3; ++i)
#pragma unroll
                for (int e2 = 0; e2 < 2; ++e2) fw[2 * i + e2] = ld4(r2, (u32)((2 * (q + 256 * i) + e2) * DM + 16 * s2 + 8 * chunk) * 2, AUX_NT);
            SB();
            f32x4 x1 = {0.f, 0.f, 0.f, 0.f}, gb = {0.f, 0.f, 0.f, 0.f};
            if (w == 0) {
                const int c4 = lane & 3, p = lane >> 2;
                const u32x2 xx = *(const u32x2*)(x + 16 * s2 + 4 * c4);
                u32x4 ga0, ga1, gb0, gb1;
                for (int it = 0; it < SPIN; ++it) { CFENCE();
                    ga0 = ld4(rws, OFF_G + (u32)(8 * s2 + 2 * c4) * 16, AUX_SC1); ga1 = ld4(rws, OFF_G + (u32)(8 * s2 + 2 * c4 + 1) * 16, AUX_SC1);
                    gb0 = ld4(rws, OFF_G + (u32)(960 + 8 * s2 + 2 * c4) * 16, AUX_SC1); gb1 = ld4(rws, OFF_G + (u32)(960 + 8 * s2 + 2 * c4 + 1) * 16, AUX_SC1);
                    if (!anyl(tagbad(ga0, tlo, thi) | tagbad(ga1, tlo, thi) | tagbad(gb0, tlo, thi) | tagbad(gb1, tlo, thi))) break; PSLEEP();
                }
                u32x4 g = {0u, 0u, 0u, 0u};
                for (int it = 0; it < SPIN; ++it) { CFENCE();
                    if (p < 12) g = ld4(rws, OFF_E6 + (u32)(p * 640 + 16 * s2 + 4 * c4) * 4, AUX_SC1);
                    if (!anyl(pois4(g))) break; PSLEEP();
                }
                f32x4 at = __builtin_bit_cast(f32x4, g);
#pragma unroll
                for (int k = 0; k < 4; ++k) { float v = at[k]; v += dpp<0x124>(v); v += dpp<0x128>(v); v = rs16(v, v); v = rs32(v, v); at[k] = v; }
                x1 = f32x4{bflo(xx.x) + __uint_as_float(ga0.x) * at.x, bfhi(xx.x) + __uint_as_float(ga0.y) * at.y, bflo(xx.y) + __uint_as_float(ga1.x) * at.z, bfhi(xx.y) + __uint_as_float(ga1.y) * at.w};
                gb = f32x4{__uint_as_float(gb0.x), __uint_as_float(gb0.y), __uint_as_float(gb1.x), __uint_as_float(gb1.y)};
                asm volatile("" : "+v"(x1), "+v"(gb));
            }
            f32x2 ff[6][4];
#pragma unroll
            for (int j = 0; j < 6; ++j) { ff[j][0] = up2(fw[j].x); ff[j][1] = up2(fw[j].y); ff[j][2] = up2(fw[j].z); ff[j][3] = up2(fw[j].w); PIN1(ff[j][0]); PIN1(ff[j][1]); PIN1(ff[j][2]); PIN1(ff[j][3]); }
            STAMP(8);
            u32x4 hg[3];
            for (int it = 0; it < SPIN; ++it) { CFENCE();
#pragma unroll
                for (int i = 0; i < 3; ++i) hg[i] = ld4(rws, OFF_HID + (u32)(q + 256 * i) * 16, AUX_SC1);
                if (!anyl(tagbad(hg[0], tlo, thi) | tagbad(hg[1], tlo, thi) | tagbad(hg[2], tlo, thi))) break; PSLEEP();
            }
            STAMP(9);
            f32x2 acc[4] = {{0.f, 0.f}, {0.f, 0.f}, {0.f, 0.f}, {0.f, 0.f}};
#pragma unroll
            for (int i = 0; i < 3; ++i) {
                const f32x2 h0 = splat(__uint_as_float(hg[i].x)), h1 = splat(__uint_as_float(hg[i].y));
#pragma unroll
                for (int k = 0; k < 4; ++k) { acc[k] += h0 * ff[2 * i][k]; acc[k] += h1 * ff[2 * i + 1][k]; }
            }
            float a[8] = {acc[0].x, acc[0].y, acc[1].x, acc[1].y, acc[2].x, acc[2].y, acc[3].x, acc[3].y};
            float v = rscat8(a, lane);
            v += dpp<0x4E>(v); v += dpp<0x104>(v);
            const int e = ((lane >> 5) & 1) * 4 + ((lane >> 4) & 1) * 2 + ((lane >> 3) & 1);
            LAS float* r2l = (LAS float*)(L + L_R2);
            if (!(lane & 6)) r2l[w * 16 + chunk * 8 + e] = v;
            BAR();
            if (t < 4) {
                f32x4 p[8];
#pragma unroll
                for (int k = 0; k < 8; ++k) p[k] = *(LAS f32x4*)(r2l + k * 16 + t * 4);
                const f32x4 s = ((p[0] + p[1]) + (p[2] + p[3])) + ((p[4] + p[5]) + (p[6] + p[7]));
                const f32x4 o = x1 + gb * s;
                *(u32x2*)(out + 16 * s2 + 4 * t) = u32x2{cvtpk(o.x, o.y), cvtpk(o.z, o.w)};
            }
            if (xi == 48 && t == 0) __hip_atomic_store((GAS u64*)(ws + OFF_EPOCH), epoch + 1ull, __ATOMIC_RELAXED, __HIP_MEMORY_SCOPE_AGENT);
            STAMP(10);
        }
    } else {
        const int yi = bid - 128, h = yi & 31, mg = yi >> 5;
        const rsrc_t rq = mkrs(wq, DM * 6144 * 2);
        u32x3 qw[10];
        wait_until(tstart, D_WQ);
        {
            const u32 base = (u32)(((mg * 160 + w * 20 + (lane >> 5)) * 32 + h) * 192 + 6 * (lane & 31)) * 2;
#pragma unroll
            for (int j = 0; j < 10; ++j) qw[j] = ld3(rq, base + j * 2 * 12288, AUX_NT);
            SB();
        }
        if (w == 1 && lane < 16 && yi < 120) st4(rws, OFF_E6 + (u32)((yi % 12) * 640 + (yi / 12) * 64) * 4 + lane * 16, PZ);
        SB();
        const unsigned char* src = (const unsigned char*)ckv + (size_t)yi * 64 * 1536;
        u32 koff[12];
#pragma unroll
        for (int j = 0; j < 12; ++j) {
            const int idx = j * 64 + lane, row = w * 8 + idx / 96, p = idx % 96;
            const int ch = (p & ~15) | ((p ^ row) & 15);
            koff[j] = (u32)(row * 1536 + ch * 16);
            PIN1(koff[j]);
        }
#pragma unroll
        for (int j = 0; j < CKV_EARLY; ++j)
            __builtin_amdgcn_global_load_lds((const GAS u32*)(src + koff[j]), (LAS u32*)(L + L_KV + (w * 12 + j) * 1024), 16, 0, AUX_NT);
        SB();
        f32x2 qf2[10][3];
#pragma unroll
        for (int j = 0; j < 10; ++j) { qf2[j][0] = up2(qw[j].x); qf2[j][1] = up2(qw[j].y); qf2[j][2] = up2(qw[j].z); PIN1(qf2[j][0]); PIN1(qf2[j][1]); PIN1(qf2[j][2]); }
        STAMP(9);
        {
            const bool act = (lane & 15) < 10;
            const u32 goff = OFF_E1 + (u32)(((lane >> 4) * 4 + mg) * 8 + w) * 256 + (lane & 15) * 16;
            u32x4 g0 = {0u, 0u, tlo, thi}, g1 = {0u, 0u, tlo, thi};
            for (int it = 0; it < SPIN; ++it) { CFENCE();
                if (act) { g0 = ld4(rws, goff, AUX_SC1); g1 = ld4(rws, goff + 32768, AUX_SC1); }
                if (!anyl(tagbad(g0, tlo, thi) | tagbad(g1, tlo, thi))) break; PSLEEP();
            }
            DRAIN();
            STAMP(1);
            float s = rs32(__uint_as_float(g0.x) + __uint_as_float(g1.x), __uint_as_float(g0.y) + __uint_as_float(g1.y));
            s = rs16(s, s);
            f32x2 a[3] = {{0.f, 0.f}, {0.f, 0.f}, {0.f, 0.f}};
#define QSTEP(j) { const f32x2 hv = splat(dpp<0x150 + j>(s)); a[0] += hv * qf2[j][0]; a[1] += hv * qf2[j][1]; a[2] += hv * qf2[j][2]; }
            QSTEP(0) QSTEP(1) QSTEP(2) QSTEP(3) QSTEP(4) QSTEP(5) QSTEP(6) QSTEP(7) QSTEP(8) QSTEP(9)
#undef QSTEP
            LAS float* red = (LAS float*)(L + L_RQ);
            {
                LAS float* dst = red + w * 192 + 6 * (lane & 31) + 3 * (lane >> 5);
                dst[0] = rs32(a[0].x, a[1].y); dst[1] = rs32(a[0].y, a[2].x); dst[2] = rs32(a[1].x, a[2].y);
            }
            BAR();
            if (t < 96) {
                f32x2 p[8];
#pragma unroll
                for (int k = 0; k < 8; ++k) p[k] = *(LAS f32x2*)(red + k * 192 + t * 2);
                const f32x2 v = ((p[0] + p[1]) + (p[2] + p[3])) + ((p[4] + p[5]) + (p[6] + p[7]));
                st4(rws, OFF_E2 + (u32)((mg * 32 + h) * 96 + t) * 16, u32x4{__float_as_uint(v.x), __float_as_uint(v.y), tlo, thi});
            }
            BAR();
        }
        STAMP(2);
#pragma unroll
        for (int j = CKV_EARLY; j < 12; ++j)
            __builtin_amdgcn_global_load_lds((const GAS u32*)(src + koff[j]), (LAS u32*)(L + L_KV + (w * 12 + j) * 1024), 16, 0, AUX_NT);
        SB();
        {
            u32x4 f = {0u, 0u, tlo, thi};
            for (int it = 0; it < SPIN; ++it) { CFENCE();
                if (lane < 16) f = ld4(rws, OFF_PF + (u32)(w * 16 + lane) * 16, AUX_SC1);
                if (!anyl(tagbad(f, tlo, thi))) break; PSLEEP();
            }
        }
        DRAIN();
        BAR();
        STAMP(3);
        const int kq = w >> 1, ht = w & 1;
        {
            const int r = lane & 15;
            const int keyr = 8 * (r >> 3) + 2 * (r & 3) + ((r >> 2) & 1);
            u32x4 af[6][4];
#pragma unroll
            for (int ks = 0; ks < 6; ++ks) {
                const int ch = kq * 24 + ks * 4 + (lane >> 4);
                const int p = (ch & ~15) | ((ch ^ keyr) & 15);
#pragma unroll
                for (int kt = 0; kt < 4; ++kt) { af[ks][kt] = *(LAS u32x4*)(L + L_KV + (kt * 16 + keyr) * 1536 + p * 16); }
            }
#pragma unroll
            for (int ks = 0; ks < 6; ++ks)
#pragma unroll
                for (int kt = 0; kt < 4; ++kt) PIN1(af[ks][kt]);
            u32x4 qf[12];
            const u32 qoff = OFF_E3 + (u32)(kq * 12 * 32 + ht * 16 + (lane & 15)) * 64 + (lane >> 4) * 16;
            for (int it = 0; it < SPIN; ++it) { CFENCE();
                qf[0] = ld4(rws, qoff, AUX_SC1);
                if (!anyl(pois4(qf[0]))) break; PSLEEP();
            }
            STAMP(10);
            f32x4 acc[4];
            for (int it = 0; it < SPIN; ++it) { CFENCE();
#pragma unroll
                for (int j = 1; j < 12; ++j) qf[j] = ld4(rws, qoff + j * 2048, AUX_SC1);
                SB();
#pragma unroll
                for (int kt = 0; kt < 4; ++kt) acc[kt] = f32x4{0.f, 0.f, 0.f, 0.f};
                u32 pm = 0;
#pragma unroll
                for (int j = 0; j < 12; ++j) {
#pragma unroll
                    for (int kt = 0; kt < 4; ++kt) acc[kt] = mfma(af[j >> 1][kt], qf[j], acc[kt]);
                    pm = max(pm, max(max(qf[j].x, qf[j].y), max(qf[j].z, qf[j].w)));
                }
                if (!anyl(pm == POISON)) break; PSLEEP();
            }
            STAMP(4);
            LAS float* sp = (LAS float*)(L + L_SP);
#pragma unroll
            for (int kt = 0; kt < 4; ++kt) *(LAS f32x4*)(sp + (kq * 32 + ht * 16 + (lane & 15)) * 68 + kt * 16 + 4 * (lane >> 4)) = acc[kt];
        }
        const int wrg = yi % 12, wcs = yi / 12;
        u32x4 ow[8];
        BAR();
        const int ocq = w & 3, part = w >> 2;
        u32x4 va[8][2];
        {
            LAS float* sp = (LAS float*)(L + L_SP);
            const int hd = t >> 4, qd = t & 15;
            f32x4 s = *(LAS f32x4*)(sp + hd * 68 + qd * 4);
#pragma unroll
            for (int k = 1; k < 4; ++k) s += *(LAS f32x4*)(sp + (k * 32 + hd) * 68 + qd * 4);
            PIN1(s);
            SB();
            {
                const int g = lane >> 4, e = (lane >> 2) & 3, pp = lane & 3;
                const int tb = ocq * 12 + part * 8;
#pragma unroll
                for (int tl = 0; tl < 8; ++tl) {
                    if (tl < 4 || part == 0) {
                        const int ch = (tb + tl) * 2 + (pp >> 1);
#pragma unroll
                        for (int k2 = 0; k2 < 2; ++k2) {
                            const int ka = k2 * 32 + 8 * g + 2 * e, kb = ka + 1;
                            const int pa = (ch & ~15) | ((ch ^ ka) & 15), pbx = (ch & ~15) | ((ch ^ kb) & 15);
                            const s16x4 t0 = __builtin_amdgcn_ds_read_tr16_b64_v4i16((LAS s16x4*)(L + L_KV + ka * 1536 + pa * 16 + (pp & 1) * 8));
                            const s16x4 t1 = __builtin_amdgcn_ds_read_tr16_b64_v4i16((LAS s16x4*)(L + L_KV + kb * 1536 + pbx * 16 + (pp & 1) * 8));
                            const u32x2 a0 = __builtin_bit_cast(u32x2, t0), a1 = __builtin_bit_cast(u32x2, t1);
                            va[tl][k2] = u32x4{a0.x, a0.y, a1.x, a1.y};
                        }
                    }
                }
            }
            SB();
            const float m = row_max(fmaxf(fmaxf(s.x, s.y), fmaxf(s.z, s.w)));
            const float p0 = __builtin_amdgcn_exp2f(s.x - m), p1 = __builtin_amdgcn_exp2f(s.y - m), p2 = __builtin_amdgcn_exp2f(s.z - m), p3 = __builtin_amdgcn_exp2f(s.w - m);
            const u32 b01 = cvtpk(p0, p1), b23 = cvtpk(p2, p3);
            *(LAS u32x2*)(L + L_P + hd * 144 + qd * 8) = u32x2{b01, b23};
            const float l = row_sum((bflo(b01) + bfhi(b01)) + (bflo(b23) + bfhi(b23)));
            if (qd == 0) st4(rws, OFF_ML + (u32)(hd * 128 + yi) * 16, u32x4{__float_as_uint(m), __float_as_uint(l), tlo, thi});
        }
        BAR();
        STAMP(5);
        {
            u32x4 pb[2][2];
#pragma unroll
            for (int hh = 0; hh < 2; ++hh)
#pragma unroll
                for (int k2 = 0; k2 < 2; ++k2) pb[hh][k2] = *(LAS u32x4*)(L + L_P + (hh * 16 + (lane & 15)) * 144 + k2 * 64 + (lane >> 4) * 16);
            const int n = lane & 15, g = lane >> 4;
            const u32 obase = OFF_O + (u32)(((n & 7) * 4 + ocq) * 128 + yi) * 384 + part * 256 + (n >> 3) * 64 + (g & 1) * 32 + (g >> 1) * 16;
#pragma unroll
            for (int ln = 0; ln < 2; ++ln) {
                if (ln == 0 || part == 0) {
#pragma unroll
                    for (int hh = 0; hh < 2; ++hh) {
                        u32 xl[4], xh[4];
#pragma unroll
                        for (int q4 = 0; q4 < 4; ++q4) {
                            f32x4 acc = f32x4{0.f, 0.f, 0.f, 0.f};
                            acc = mfma(va[ln * 4 + q4][0], pb[hh][0], acc);
                            acc = mfma(va[ln * 4 + q4][1], pb[hh][1], acc);
                            xl[q4] = cvtpk(acc.x, acc.y); xh[q4] = cvtpk(acc.z, acc.w);
                        }
                        auto r0 = __builtin_amdgcn_permlane16_swap(xl[0], xl[1], false, false);
                        auto r1 = __builtin_amdgcn_permlane16_swap(xh[0], xh[1], false, false);
                        auto r2 = __builtin_amdgcn_permlane16_swap(xl[2], xl[3], false, false);
                        auto r3 = __builtin_amdgcn_permlane16_swap(xh[2], xh[3], false, false);
                        const u32 x01[4] = {r0[0], r1[0], r0[1], r1[1]}, x23[4] = {r2[0], r3[0], r2[1], r3[1]};
                        u32 A[4], B[4];
#pragma unroll
                        for (int i = 0; i < 4; ++i) {
                            A[i] = canon((u32)__builtin_amdgcn_update_dpp((int)x01[i], (int)x23[i], 0x128, 0xf, 0xC, false));
                            B[i] = canon((u32)__builtin_amdgcn_update_dpp((int)x23[i], (int)x01[i], 0x128, 0xf, 0x3, false));
                        }
                        if (ln == 0 && hh == 0) STAMP(11);
                        st4(rws, obase + (u32)(hh * 16 * 4) * 49152 + ln * 128, u32x4{A[0], A[1], A[2], A[3]});
                        st4(rws, obase + (u32)((hh * 16 + 8) * 4) * 49152 + ln * 128, u32x4{B[0], B[1], B[2], B[3]});
                    }
                    if (ln == 0) STAMP(12);
                }
            }
        }
        STAMP(6);
        if (yi < 120) {
            {
                const rsrc_t ro = mkrs(wo, 6144 * DM * 2);
                const u32 base = (u32)((wrg * 512 + w * 64 + (lane >> 3) * 8) * DM + wcs * 64 + (lane & 7) * 8) * 2;
#pragma unroll
                for (int j = 0; j < 8; ++j) ow[j] = ld4(ro, base + j * 1280, AUX_NT);
                SB();
            }
            f32x2 of[8][4];
#pragma unroll
            for (int j = 0; j < 8; ++j) { of[j][0] = up2(ow[j].x); of[j][1] = up2(ow[j].y); of[j][2] = up2(ow[j].z); of[j][3] = up2(ow[j].w); PIN1(of[j][0]); PIN1(of[j][1]); PIN1(of[j][2]); PIN1(of[j][3]); }
            const int cc8 = lane & 7;
            const u32 goff = OFF_E5 + (u32)((cc8 < 4 ? 0 : 2) * 3072 + wrg * 256 + w * 32 + (lane >> 3) * 4 + (cc8 < 4 ? cc8 : 7 - cc8)) * 16;
            u32x4 g0, g1;
            for (int it = 0; it < SPIN; ++it) { CFENCE();
                g0 = ld4(rws, goff, AUX_SC1); g1 = ld4(rws, goff + 3072 * 16, AUX_SC1);
                if (!anyl(tagbad(g0, tlo, thi) | tagbad(g1, tlo, thi))) break; PSLEEP();
            }
            STAMP(7);
            float vx = __uint_as_float(g0.x) + __uint_as_float(g1.x), vy = __uint_as_float(g0.y) + __uint_as_float(g1.y);
            vx += dpp<0x141>(vx); vy += dpp<0x141>(vy);
            vx = dppb<0x1B, 0xA>(vx, vx); vy = dppb<0x1B, 0xA>(vy, vy);
            f32x2 a2[4] = {{0.f, 0.f}, {0.f, 0.f}, {0.f, 0.f}, {0.f, 0.f}};
#define WSTEP(k) { const f32x2 hx = splat(dpp<k * 0x55>(vx)), hy = splat(dpp<k * 0x55>(vy)); _Pragma("unroll") for (int i = 0; i < 4; ++i) { a2[i] += hx * of[2 * k][i]; a2[i] += hy * of[2 * k + 1][i]; } }
            WSTEP(0) WSTEP(1) WSTEP(2) WSTEP(3)
#undef WSTEP
            float a[8] = {a2[0].x, a2[0].y, a2[1].x, a2[1].y, a2[2].x, a2[2].y, a2[3].x, a2[3].y};
            const float v = rscat8(a, lane);
            const int e = ((lane >> 5) & 1) * 4 + ((lane >> 4) & 1) * 2 + ((lane >> 3) & 1);
            LAS float* red = (LAS float*)(L + L_RW);
            red[w * 64 + (lane & 7) * 8 + e] = v;
            BAR();
            if (w == 0 && lane < 16) {
                f32x4 p[8];
#pragma unroll
                for (int k = 0; k < 8; ++k) p[k] = *(LAS f32x4*)(red + k * 64 + lane * 4);
                const f32x4 s4 = ((p[0] + p[1]) + (p[2] + p[3])) + ((p[4] + p[5]) + (p[6] + p[7]));
                st4(rws, OFF_E6 + (u32)(wrg * 640 + wcs * 64) * 4 + lane * 16, u32x4{canonf(s4.x), canonf(s4.y), canonf(s4.z), canonf(s4.w)});
            }
            STAMP(8);
        }
    }
}

extern "C" void kernel_launch(void* const* d_in, const int* in_sizes, int n_in, void* d_out, int out_size, void* d_ws, size_t ws_size, hipStream_t stream) {
    (void)in_sizes; (void)n_in; (void)out_size; (void)ws_size;
    hipFuncSetAttribute((const void*)adaln_mla_mlp_kernel, hipFuncAttributeMaxDynamicSharedMemorySize, LDS_BYTES);
    adaln_mla_mlp_kernel<<<NWG, NT, LDS_BYTES, stream>>>(
        (const u16*)d_in[0], (const u16*)d_in[1], (const u16*)d_in[2], (const u16*)d_in[3], (const u16*)d_in[4], (const u16*)d_in[5],
        (const u16*)d_in[7], (const u16*)d_in[8], (const u16*)d_in[9], (const u16*)d_in[10], (u16*)d_out, (unsigned char*)d_ws);
}
```

```cpp
#include <hip/hip_runtime.h>
#include <stdint.h>

typedef unsigned u32;
typedef unsigned long long u64;
typedef unsigned short u16;
typedef u32 u32x2 __attribute__((ext_vector_type(2)));
typedef u32 u32x3 __attribute__((ext_vector_type(3)));
typedef u32 u32x4 __attribute__((ext_vector_type(4)));
typedef float f32x2 __attribute__((ext_vector_type(2)));
typedef float f32x4 __attribute__((ext_vector_type(4)));
typedef short bf16x8 __attribute__((ext_vector_type(8)));
typedef short s16x4 __attribute__((ext_vector_type(4)));
typedef __amdgpu_buffer_rsrc_t rsrc_t;
#define LAS __attribute__((address_space(3)))
#define GAS __attribute__((address_space(1)))

constexpr int NH = 32, SK = 8192, HD = 192, DM = 640, DC = 768, DFF = 1536;
constexpr int NWG = 256, NT = 512;

#define UK_EARLY 0
#define D_UK 0
#define D_WQ 40
#define G_POS 1
#define POIS_POS 1
#define CKV_EARLY 0
#define WO_POS 1
#define ML_POS 0
#define O_AUX 16
#define O_LAY 0
#define ML_SLEEP 0
constexpr u32 OFF_EPOCH = 0;
constexpr u32 OFF_PF = 256;
constexpr u32 OFF_E1 = OFF_PF + 128 * 16;
constexpr u32 OFF_E2 = OFF_E1 + 8 * 4 * 8 * 256;
constexpr u32 OFF_E3 = OFF_E2 + 4 * 32 * 96 * 16;
constexpr u32 OFF_ML = OFF_E3 + 32 * 4 * 768;
constexpr u32 OFF_E5 = OFF_ML + 32 * 128 * 16;
constexpr u32 OFF_E6 = OFF_E5 + 4 * 3072 * 16;
constexpr u32 OFF_G = OFF_E6 + 12 * 640 * 4;
constexpr u32 OFF_HID = OFF_G + 1280 * 16;
constexpr u32 OFF_O = OFF_HID + 768 * 16;
constexpr u32 WS_BYTES = OFF_O + 32 * 4 * 128 * 384;
constexpr u32 OFF_DIAG = WS_BYTES;
static_assert(OFF_O % 128 == 0 && OFF_E3 % 128 == 0 && OFF_E5 % 128 == 0 && OFF_E2 % 256 == 0 && OFF_E6 % 128 == 0 && OFF_E1 % 256 == 0, "alignment");

constexpr int L_KV = 0;
constexpr int L_SP = 98304;
constexpr int L_P = L_SP + 4 * 32 * 68 * 4;
constexpr int L_RQ = L_P + 32 * 144;
constexpr int L_RW = L_RQ + 8 * 192 * 4;
constexpr int LDS_BYTES = L_RW + 8 * 64 * 4;
constexpr int L_RM = 0;
constexpr int L_Q = L_RM + 4096;
constexpr int L_RA = L_Q + 768;
constexpr int L_RV1 = L_RA + 768;
constexpr int L_RV2 = L_RV1 + 6144;
constexpr int L_RG = L_RV2 + 6144;
constexpr int L_GS = L_RG + 1024;
constexpr int L_RP = L_GS + 7680;
constexpr int L_RF = L_RP + 2048;
constexpr int L_ST = L_RF + 1024;
constexpr int L_R2 = L_ST + 64;
constexpr int L_R6 = L_R2 + 512;
constexpr int SPIN = 1 << 16;
constexpr u32 POISON = 0xFFFFFFFFu;
constexpr int AUX_SC1 = 16, AUX_NT = 2;
constexpr u64 TAGMUL = 0x9E3779B97F4A7C15ull;

__device__ __forceinline__ float bflo(u32 v) { return __uint_as_float(v << 16); }
__device__ __forceinline__ float bfhi(u32 v) { return __uint_as_float(v & 0xFFFF0000u); }
__device__ __forceinline__ float bf1(u16 v) { return __uint_as_float((u32)v << 16); }
typedef __bf16 bf16x2_t __attribute__((ext_vector_type(2)));
__device__ __forceinline__ u32 cvtpk(float lo, float hi) { f32x2 v = {lo, hi}; return __builtin_bit_cast(u32, __builtin_convertvector(v, bf16x2_t)); }
__device__ __forceinline__ u32 canon(u32 v) { return v < 0xFFFFFFFEu ? v : 0xFFFFFFFEu; }
__device__ __forceinline__ u32 canonf(float v) { return canon(__float_as_uint(v)); }
__device__ __forceinline__ rsrc_t mkrs(const void* p, u32 bytes) { return __builtin_amdgcn_make_buffer_rsrc((void*)p, 0, bytes, 0x00020000); }
__device__ __forceinline__ u32x4 ld4(rsrc_t r, u32 off, int aux) { return aux == AUX_SC1 ? __builtin_amdgcn_raw_buffer_load_b128(r, off, 0, AUX_SC1) : __builtin_amdgcn_raw_buffer_load_b128(r, off, 0, AUX_NT); }
__device__ __forceinline__ u32x3 ld3(rsrc_t r, u32 off, int aux) { return aux == AUX_SC1 ? __builtin_amdgcn_raw_buffer_load_b96(r, off, 0, AUX_SC1) : __builtin_amdgcn_raw_buffer_load_b96(r, off, 0, AUX_NT); }
__device__ __forceinline__ void st4(rsrc_t r, u32 off, u32x4 v) { __builtin_amdgcn_raw_buffer_store_b128(v, r, off, 0, AUX_SC1); }
__device__ __forceinline__ void st2(rsrc_t r, u32 off, u32x2 v) { __builtin_amdgcn_raw_buffer_store_b64(v, r, off, 0, AUX_SC1); }
#define CFENCE() asm volatile("" ::: "memory")
#define PSL 0
#define PSLEEP() do { if (PSL > 0) __builtin_amdgcn_s_sleep(PSL); } while (0)
#define SB() __builtin_amdgcn_sched_barrier(0)
#define BAR() asm volatile("s_waitcnt lgkmcnt(0)\n\ts_barrier" ::: "memory")
#define LGKM0() asm volatile("s_waitcnt lgkmcnt(0)" ::: "memory")
#define DRAIN() asm volatile("s_waitcnt vmcnt(0)" ::: "memory")

template <int CTRL> __device__ __forceinline__ float dpp(float x) { return __int_as_float(__builtin_amdgcn_update_dpp(0, __float_as_int(x), CTRL, 0xf, 0xf, true)); }
__device__ __forceinline__ float rs32(float a, float b) { auto r = __builtin_amdgcn_permlane32_swap(__float_as_uint(a), __float_as_uint(b), false, false); return __uint_as_float(r[0]) + __uint_as_float(r[1]); }
__device__ __forceinline__ float rs16(float a, float b) { auto r = __builtin_amdgcn_permlane16_swap(__float_as_uint(a), __float_as_uint(b), false, false); return __uint_as_float(r[0]) + __uint_as_float(r[1]); }
__device__ __forceinline__ float row_sum(float x) { x += dpp<0x128>(x); x += dpp<0x124>(x); x += dpp<0x122>(x); x += dpp<0x121>(x); return x; }
__device__ __forceinline__ float row_max(float x) { x = fmaxf(x, dpp<0x128>(x)); x = fmaxf(x, dpp<0x124>(x)); x = fmaxf(x, dpp<0x122>(x)); x = fmaxf(x, dpp<0x121>(x)); return x; }
__device__ __forceinline__ float wave_sum(float x) { x = row_sum(x); x = rs16(x, x); x = rs32(x, x); return x; }
__device__ __forceinline__ float wave_max(float x) {
    x = row_max(x);
    { auto r = __builtin_amdgcn_permlane16_swap(__float_as_uint(x), __float_as_uint(x), false, false); x = fmaxf(__uint_as_float(r[0]), __uint_as_float(r[1])); }
    { auto r = __builtin_amdgcn_permlane32_swap(__float_as_uint(x), __float_as_uint(x), false, false); x = fmaxf(__uint_as_float(r[0]), __uint_as_float(r[1])); }
    return x;
}
__device__ __forceinline__ float rscat8(const float (&a)[8], int lane) {
    float r0 = rs32(a[0], a[4]), r1 = rs32(a[1], a[5]), r2 = rs32(a[2], a[6]), r3 = rs32(a[3], a[7]);
    float s0 = rs16(r0, r2), s1 = rs16(r1, r3);
    float t0 = s0 + dpp<0x128>(s0), t1 = s1 + dpp<0x128>(s1);
    return (lane & 8) ? t1 : t0;
}
__device__ __forceinline__ bool pois4(u32x4 v) { u32 m = max(max(v.x, v.y), max(v.z, v.w)); return m == POISON; }
__device__ __forceinline__ bool pois3(u32x3 v) { u32 m = max(max(v.x, v.y), v.z); return m == POISON; }
__device__ __forceinline__ bool anyl(bool b) { return __builtin_amdgcn_ballot_w64(b) != 0ull; }
__device__ __forceinline__ f32x4 mfma(u32x4 a, u32x4 b, f32x4 c) { return __builtin_amdgcn_mfma_f32_16x16x32_bf16(__builtin_bit_cast(bf16x8, a), __builtin_bit_cast(bf16x8, b), c, 0, 0, 0); }
__device__ __forceinline__ float silu(float v) { return v / (1.f + __expf(-v)); }
__device__ __forceinline__ void unp8(u32x4 v, float (&f)[8]) { f[0] = bflo(v.x); f[1] = bfhi(v.x); f[2] = bflo(v.y); f[3] = bfhi(v.y); f[4] = bflo(v.z); f[5] = bfhi(v.z); f[6] = bflo(v.w); f[7] = bfhi(v.w); }
__device__ __forceinline__ void unp6(u32x3 v, float (&f)[6]) { f[0] = bflo(v.x); f[1] = bfhi(v.x); f[2] = bflo(v.y); f[3] = bfhi(v.y); f[4] = bflo(v.z); f[5] = bfhi(v.z); }

__device__ __forceinline__ void wait_until(u64 t0, int ticks) {
    if (ticks > 0) { for (int i = 0; i < 4096; ++i) { if (__builtin_amdgcn_s_memrealtime() - t0 >= (u64)ticks) break; __builtin_amdgcn_s_sleep(1); } }
}
#define STAMP(i) do { } while (0)

__device__ __forceinline__ void ln_stats(const float (&v)[10], float& mean, float& rstd) {
    float s = 0.f;
#pragma unroll
    for (int i = 0; i < 10; ++i) s += v[i];
    mean = wave_sum(s) * (1.f / 640.f);
    float q = 0.f;
#pragma unroll
    for (int i = 0; i < 10; ++i) { float d = v[i] - mean; q += d * d; }
    float var = wave_sum(q) * (1.f / 640.f);
    rstd = 1.f / sqrtf(var + 1e-5f);
}

#define CAS __attribute__((address_space(4)))
#define PIN1(a) asm volatile("" : "+v"(a))
template <int CTRL, int BANK> __device__ __forceinline__ float dppb(float old, float x) { return __int_as_float(__builtin_amdgcn_update_dpp(__float_as_int(old), __float_as_int(x), CTRL, 0xf, BANK, false)); }
__device__ __forceinline__ u32x2 ld2(rsrc_t r, u32 off) { return __builtin_amdgcn_raw_buffer_load_b64(r, off, 0, AUX_SC1); }
__device__ __forceinline__ u32 ld1(rsrc_t r, u32 off) { return __builtin_amdgcn_raw_buffer_load_b32(r, off, 0, AUX_SC1); }
__device__ __forceinline__ bool tagbad(u32x4 g, u32 tlo, u32 thi) { return (g.z != tlo) | (g.w != thi); }
__device__ __forceinline__ f32x2 up2(u32 v) { return f32x2{bflo(v), bfhi(v)}; }
__device__ __forceinline__ f32x2 splat(float v) { return f32x2{v, v}; }

__global__ __launch_bounds__(NT, 2) void adaln_mla_mlp_kernel(
    const u16* __restrict__ x, const u16* __restrict__ ckv, const u16* __restrict__ wq, const u16* __restrict__ wuk,
    const u16* __restrict__ wuv, const u16* __restrict__ wo, const u16* __restrict__ w1, const u16* __restrict__ w2,
    const u16* __restrict__ cc, const u16* __restrict__ wmod, u16* __restrict__ out, unsigned char* __restrict__ ws) {
    extern __shared__ __attribute__((aligned(16))) unsigned char smem[];
    LAS unsigned char* L = (LAS unsigned char*)smem;
    STAMP(0);
    const u64 tstart = __builtin_amdgcn_s_memrealtime();
    asm volatile("" ::"s"(x), "s"(ckv), "s"(wq), "s"(wuk), "s"(wuv), "s"(wo), "s"(w1), "s"(w2), "s"(cc), "s"(wmod), "s"(out), "s"(ws));
    const int t = threadIdx.x, lane = t & 63, w = __builtin_amdgcn_readfirstlane(t >> 6);
    const int bid = blockIdx.x;
    const rsrc_t rws = mkrs(ws, WS_BYTES);
    const u64 epoch = *(const CAS u64*)(ws + OFF_EPOCH);
    const u64 tag = (epoch + 1ull) * TAGMUL;
    const u32 tlo = (u32)tag, thi = (u32)(tag >> 32);
    const u32x4 PZ = {POISON, POISON, POISON, POISON};

    if (bid < 128) {
        const int xi = bid, h = xi & 31, cq = xi >> 5;
        const rsrc_t rmod = mkrs(wmod, DM * 3840 * 2);
        const bool isM = xi < 80;
        u32x4 mw[3]; u16 mc[3]; u32 xr[5], xm = 0;
        const int m_rg = xi & 7, m_s = xi >> 3;
        if (isM) {
            const int row0 = m_rg * 80 + w * 10 + (lane >> 4);
            const u32 off = (u32)(row0 * 3840 + ((lane >> 3) & 1) * 640 + m_s * 64 + (lane & 7) * 8) * 2;
            mw[0] = ld4(rmod, off, AUX_NT); mw[1] = ld4(rmod, off + 4 * 7680, AUX_NT); mw[2] = ld4(rmod, lane < 32 ? off + 8 * 7680 : 0x80000000u, AUX_NT);
            mc[0] = cc[row0]; mc[1] = cc[row0 + 4]; mc[2] = cc[row0 + (lane < 32 ? 8 : 0)];
            if (w == 0) {
#pragma unroll
                for (int i = 0; i < 5; ++i) xr[i] = ((const u32*)x)[lane + 64 * i];
                xm = ((const u32*)x)[32 * m_s + (lane & 31)];
            }
            SB();
        }
        const u32 e3off = OFF_E3 + (u32)((cq * 12 + (lane >> 2)) * 32 + h) * 64 + (lane & 3) * 16;
        if (w == 7 && lane < 48) st4(rws, e3off, PZ);
        const u32 opz = OFF_O + (u32)(h * 4 + cq) * 49152 + t * 16;
        const rsrc_t ruk = mkrs(wuk, DC * 6144 * 2);
        const u32 wbase = (u32)(((cq * 192 + w * 24 + (lane >> 5)) * 32 + h) * 192 + 6 * (lane & 31)) * 2;
        u32x3 uw[12];
#pragma unroll
        for (int j = 0; j < UK_EARLY; ++j) uw[j] = ld3(ruk, wbase + j * 2 * 12288, AUX_NT);
        SB();
        if (isM) {
            LAS float* red = (LAS float*)(L + L_RM);
            f32x2 lnx = {0.f, 0.f};
            if (w == 0) {
                float xv[10];
#pragma unroll
                for (int i = 0; i < 5; ++i) { xv[2 * i] = bflo(xr[i]); xv[2 * i + 1] = bfhi(xr[i]); }
                float mean, rstd; ln_stats(xv, mean, rstd);
                lnx = f32x2{(bflo(xm) - mean) * rstd, (bfhi(xm) - mean) * rstd};
            }
            float a[8], f[8];
            const float s0 = silu(bf1(mc[0])), s1 = silu(bf1(mc[1])), s2 = silu(bf1(mc[2]));
            unp8(mw[0], f);
#pragma unroll
            for (int i = 0; i < 8; ++i) a[i] = s0 * f[i];
            unp8(mw[1], f);
#pragma unroll
            for (int i = 0; i < 8; ++i) a[i] += s1 * f[i];
            unp8(mw[2], f);
#pragma unroll
            for (int i = 0; i < 8; ++i) a[i] += s2 * f[i];
            const float r0 = rs32(a[0], a[4]), r1 = rs32(a[1], a[5]), r2 = rs32(a[2], a[6]), r3 = rs32(a[3], a[7]);
            const f32x2 sv = {rs16(r0, r2), rs16(r1, r3)};
            *(LAS f32x2*)(red + w * 128 + ((lane >> 3) & 1) * 64 + (lane & 7) * 8 + 4 * (lane >> 5) + 2 * ((lane >> 4) & 1)) = sv;
            BAR();
            if (w == 0) {
                f32x2 p[8];
#pragma unroll
                for (int k = 0; k < 8; ++k) p[k] = *(LAS f32x2*)(red + k * 128 + 2 * lane);
                const f32x2 s = ((p[0] + p[1]) + (p[2] + p[3])) + ((p[4] + p[5]) + (p[6] + p[7]));
                auto rx = __builtin_amdgcn_permlane32_swap(__float_as_uint(s.x), __float_as_uint(s.x), false, false);
                auto ry = __builtin_amdgcn_permlane32_swap(__float_as_uint(s.y), __float_as_uint(s.y), false, false);
                float hx = lnx.x * __uint_as_float(rx[1]) + __uint_as_float(rx[0]), hy = lnx.y * __uint_as_float(ry[1]) + __uint_as_float(ry[0]);
                if (m_rg == 0) { hx += lnx.x; hy += lnx.y; }
                const int g = 32 * m_s + lane, gm = g / 80, gr = g - gm * 80, gw_ = gr / 10;
                if (lane < 32) st4(rws, OFF_E1 + (u32)(((m_rg * 4 + gm) * 8 + gw_) * 16 + (gr - gw_ * 10)) * 16, u32x4{__float_as_uint(hx), __float_as_uint(hy), tlo, thi});
            }
        }
        STAMP(1);
        wait_until(tstart, D_UK);
#pragma unroll
        for (int j = UK_EARLY; j < 12; ++j) uw[j] = ld3(ruk, wbase + j * 2 * 12288, AUX_NT);
        SB();
#pragma unroll
        for (int j = 0; j < 6; ++j) st4(rws, opz + j * 8192, PZ);
        SB();
        STAMP(11);
        f32x2 wf[12][3];
#pragma unroll
        for (int j = 0; j < 12; ++j) { wf[j][0] = up2(uw[j].x); wf[j][1] = up2(uw[j].y); wf[j][2] = up2(uw[j].z); PIN1(wf[j][0]); PIN1(wf[j][1]); PIN1(wf[j][2]); }
        DRAIN();
        BAR();
        if (w == 7 && lane == 0) st4(rws, OFF_PF + xi * 16, u32x4{0u, 0u, tlo, thi});
        STAMP(12);
        u32x4 gw[5]; u16 cv[5];
        const int rs = lane >> 2, ck = lane & 3;
        int g_s = 0;
        {
            LAS float* qs = (LAS float*)(L + L_Q);
            if (w < 6) {
                const u32 goff = OFF_E2 + (u32)(((lane >> 4) * 32 + h) * 96 + 16 * w + (lane & 15)) * 16;
                u32x4 g;
                for (int it = 0; it < SPIN; ++it) { CFENCE();
                    g = ld4(rws, goff, AUX_SC1);
                    if (!anyl(tagbad(g, tlo, thi))) break; PSLEEP();
                }
                float s = rs32(__uint_as_float(g.x), __uint_as_float(g.y));
                s = rs16(s, s);
                if (!(lane & 16)) qs[2 * (16 * w + (lane & 15)) + (lane >> 5)] = s;
            }
            STAMP(2);
            BAR();
            const f32x2 q0 = *(LAS f32x2*)(qs + 6 * (lane & 31)), q1 = *(LAS f32x2*)(qs + 6 * (lane & 31) + 2), q2 = *(LAS f32x2*)(qs + 6 * (lane & 31) + 4);
            float p[12];
#pragma unroll
            for (int j = 0; j < 12; ++j) { const f32x2 d = wf[j][0] * q0 + wf[j][1] * q1 + wf[j][2] * q2; p[j] = d.x + d.y; }
            float r[6], s[3];
#pragma unroll
            for (int j = 0; j < 6; ++j) r[j] = rs16(p[j], p[j + 6]);
#pragma unroll
            for (int j = 0; j < 3; ++j) {
                const float own = (lane & 8) ? r[j + 3] : r[j], oth = (lane & 8) ? r[j] : r[j + 3];
                float v = own + dpp<0x128>(oth);
                v += dpp<0x141>(v); v += dpp<0x1B>(v); v += dpp<0xB1>(v);
                s[j] = v;
            }
            LAS float* red = (LAS float*)(L + L_RA);
            {
                const int j = lane & 7;
                const float v = j == 0 ? s[0] : (j == 1 ? s[1] : s[2]);
                if (j < 3) red[w * 24 + 2 * (j + 3 * ((lane >> 3) & 1) + 6 * ((lane >> 4) & 1)) + (lane >> 5)] = v;
            }
            BAR();
            if (w == 0 && lane < 48) {
                const float sc = 0.07216878364870322f * 1.4426950408889634f;
                const int c0 = (lane >> 3) * 32 + (lane & 3) * 8;
                const f32x4 p0 = *(LAS f32x4*)(red + c0) * sc, p1 = *(LAS f32x4*)(red + c0 + 4) * sc;
                const float v8[8] = {p0.x, p0.y, p0.z, p0.w, p1.x, p1.y, p1.z, p1.w};
                u32 o[4];
#pragma unroll
                for (int i = 0; i < 4; ++i) {
                    const u32 hi = cvtpk(v8[2 * i], v8[2 * i + 1]);
                    const u32 lo = cvtpk(v8[2 * i] - bflo(hi), v8[2 * i + 1] - bfhi(hi));
                    o[i] = canon((lane & 4) ? lo : hi);
                }
                st4(rws, e3off, u32x4{o[0], o[1], o[2], o[3]});
            }
            BAR();
        }
        STAMP(3);
        const rsrc_t ruv = mkrs(wuv, DC * 6144 * 2);
        u32x3 vw[12];
#pragma unroll
        for (int j = 0; j < 12; ++j) vw[j] = ld3(ruv, wbase + j * 2 * 12288, AUX_NT);
        SB();
        if (xi >= 48) {
            const int gi = xi - 48;
            g_s = 2 * ((gi >> 4) * 8 + (gi & 7)) + ((gi >> 3) & 1);
            const int row0 = t >> 2;
            const u32 off = (u32)(row0 * 3840 + 1280 + 32 * g_s + 8 * ck) * 2;
#pragma unroll
            for (int j = 0; j < 5; ++j) gw[j] = ld4(rmod, off + j * 128 * 7680, AUX_NT);
#pragma unroll
            for (int j = 0; j < 5; ++j) cv[j] = cc[row0 + 128 * j];
        } else {
            const rsrc_t r1 = mkrs(w1, DM * DFF * 2);
            const u32 b1 = (u32)((w * 80 + rs) * DFF + 32 * xi + 8 * ck) * 2;
#pragma unroll
            for (int j = 0; j < 5; ++j) gw[j] = ld4(r1, b1 + j * 16 * DFF * 2, AUX_NT);
#pragma unroll
            for (int j = 0; j < 5; ++j) cv[j] = x[w * 80 + 16 * j + rs];
        }
        SB();
        if (xi >= 48) {
            float a[8];
#pragma unroll
            for (int i = 0; i < 8; ++i) a[i] = 0.f;
#pragma unroll
            for (int j = 0; j < 5; ++j) {
                float f[8]; unp8(gw[j], f); const float s = silu(bf1(cv[j]));
#pragma unroll
                for (int i = 0; i < 8; ++i) a[i] += s * f[i];
            }
            float v = rscat8(a, lane);
            v += dpp<0x104>(v);
            const int e = ((lane >> 5) & 1) * 4 + ((lane >> 4) & 1) * 2 + ((lane >> 3) & 1);
            LAS float* redg = (LAS float*)(L + L_RG);
            if (!(lane & 4)) redg[w * 32 + ck * 8 + e] = v;
            BAR();
            if (w == 0 && lane < 16) {
                f32x2 s = {0.f, 0.f};
#pragma unroll
                for (int i = 0; i < 8; ++i) s += *(LAS f32x2*)(redg + i * 32 + lane * 2);
                st4(rws, OFF_G + (g_s * 16 + lane) * 16, u32x4{__float_as_uint(s.x), __float_as_uint(s.y), tlo, thi});
            }
        }
        f32x2 vf[12][3];
#pragma unroll
        for (int j = 0; j < 12; ++j) { vf[j][0] = up2(vw[j].x); vf[j][1] = up2(vw[j].y); vf[j][2] = up2(vw[j].z); PIN1(vf[j][0]); PIN1(vf[j][1]); PIN1(vf[j][2]); }
        STAMP(4);
        {
            const u32 mlA = OFF_ML + (u32)(h * 128 + lane) * 16;
            const u32 obase = OFF_O + (u32)((h * 4 + cq) * 128 + w * 16 + (lane >> 5)) * 384 + (lane & 31) * 12;
            constexpr u32 OJ = 768;
            LAS float* red1 = (LAS float*)(L + L_RV1);
            LAS float* red2 = (LAS float*)(L + L_RV2);
            u32x4 ma, mb;
            for (int it = 0; it < SPIN; ++it) { CFENCE();
                ma = ld4(rws, mlA, AUX_SC1); mb = ld4(rws, mlA + 1024, AUX_SC1);
                if (!anyl(tagbad(ma, tlo, thi) | tagbad(mb, tlo, thi))) break; if (ML_SLEEP > 0) __builtin_amdgcn_s_sleep(ML_SLEEP);
            }
            STAMP(5);
            u32x3 oa[8];
#pragma unroll
            for (int j = 0; j < 8; ++j) oa[j] = ld3(rws, obase + j * OJ, AUX_SC1);
            SB();
            float wA;
            {
                const float m0 = __uint_as_float(ma.x), l0 = __uint_as_float(ma.y), m1 = __uint_as_float(mb.x), l1 = __uint_as_float(mb.y);
                const float M = wave_max(fmaxf(m0, m1));
                const float w0 = __builtin_amdgcn_exp2f(m0 - M), w1_ = __builtin_amdgcn_exp2f(m1 - M);
                const float Lsum = wave_sum(w0 * l0 + w1_ * l1);
                const float wn = (w < 4 ? w0 : w1_) * __builtin_amdgcn_rcpf(Lsum);
                const int srcl = (16 * w + 2 * (lane & 7) + (lane >> 5)) & 63;
                wA = __int_as_float(__builtin_amdgcn_ds_bpermute(srcl * 4, __float_as_int(wn)));
            }
            f32x2 pr[8][3];
            u32 pend = 0;
#define OSTEP(j) { if (anyl(pois3(oa[j]))) pend |= 1u << j; else { const f32x2 ww = splat(dpp<0x150 + j>(wA)); pr[j][0] = up2(oa[j].x) * ww; pr[j][1] = up2(oa[j].y) * ww; pr[j][2] = up2(oa[j].z) * ww; } }
            OSTEP(0) OSTEP(1) OSTEP(2) OSTEP(3) OSTEP(4) OSTEP(5) OSTEP(6) OSTEP(7)
#undef OSTEP
            for (int it = 0; it < SPIN && pend; ++it) { CFENCE();
#pragma unroll
                for (int j = 0; j < 8; ++j) if (pend & (1u << j)) oa[j] = ld3(rws, obase + j * OJ, AUX_SC1);
#define OSTEP(j) if (pend & (1u << j)) { if (!anyl(pois3(oa[j]))) { pend &= ~(1u << j); const f32x2 ww = splat(dpp<0x150 + j>(wA)); pr[j][0] = up2(oa[j].x) * ww; pr[j][1] = up2(oa[j].y) * ww; pr[j][2] = up2(oa[j].z) * ww; } }
                OSTEP(0) OSTEP(1) OSTEP(2) OSTEP(3) OSTEP(4) OSTEP(5) OSTEP(6) OSTEP(7)
#undef OSTEP
            }
            STAMP(6);
            f32x2 a2[3];
#pragma unroll
            for (int k = 0; k < 3; ++k) a2[k] = ((pr[0][k] + pr[1][k]) + (pr[2][k] + pr[3][k])) + ((pr[4][k] + pr[5][k]) + (pr[6][k] + pr[7][k]));
            {
                LAS float* dst = red1 + w * 192 + 6 * (lane & 31) + 3 * (lane >> 5);
                dst[0] = rs32(a2[0].x, a2[1].y); dst[1] = rs32(a2[0].y, a2[2].x); dst[2] = rs32(a2[1].x, a2[2].y);
            }
            BAR();
            float cx = 0.f;
            {
                const int i = lane & 15, cidx = w * 24 + 2 * (i < 12 ? i : 11) + (lane >> 5);
                float p[8];
#pragma unroll
                for (int k = 0; k < 8; ++k) p[k] = red1[k * 192 + cidx];
                cx = ((p[0] + p[1]) + (p[2] + p[3])) + ((p[4] + p[5]) + (p[6] + p[7]));
            }
            f32x2 c2[3] = {{0.f, 0.f}, {0.f, 0.f}, {0.f, 0.f}};
#define VSTEP(j) { const f32x2 cvv = splat(dpp<0x150 + j>(cx)); c2[0] += cvv * vf[j][0]; c2[1] += cvv * vf[j][1]; c2[2] += cvv * vf[j][2]; }
            VSTEP(0) VSTEP(1) VSTEP(2) VSTEP(3) VSTEP(4) VSTEP(5) VSTEP(6) VSTEP(7) VSTEP(8) VSTEP(9) VSTEP(10) VSTEP(11)
#undef VSTEP
            {
                LAS float* dst = red2 + w * 192 + 6 * (lane & 31) + 3 * (lane >> 5);
                dst[0] = rs32(c2[0].x, c2[1].y); dst[1] = rs32(c2[0].y, c2[2].x); dst[2] = rs32(c2[1].x, c2[2].y);
            }
            BAR();
            if (t < 96) {
                f32x2 p[8];
#pragma unroll
                for (int k = 0; k < 8; ++k) p[k] = *(LAS f32x2*)(red2 + k * 192 + t * 2);
                const f32x2 s = ((p[0] + p[1]) + (p[2] + p[3])) + ((p[4] + p[5]) + (p[6] + p[7]));
                st4(rws, OFF_E5 + (u32)(cq * 3072 + h * 96 + t) * 16, u32x4{__float_as_uint(s.x), __float_as_uint(s.y), tlo, thi});
            }
        }
        BAR();
        STAMP(7);
        if (xi < 48) {
            LAS float* gs = (LAS float*)(L + L_GS);
            {
                u32x4 g0, g1;
                const bool two = t < 448;
                for (int it = 0; it < SPIN; ++it) { CFENCE();
                    g0 = ld4(rws, OFF_G + t * 16, AUX_SC1); bool bad = tagbad(g0, tlo, thi);
                    if (two) { g1 = ld4(rws, OFF_G + (t + 512) * 16, AUX_SC1); bad |= tagbad(g1, tlo, thi); }
                    if (!anyl(bad)) break; PSLEEP();
                }
                *(LAS u32x2*)(gs + 2 * t) = u32x2{g0.x, g0.y};
                if (two) *(LAS u32x2*)(gs + 2 * (t + 512)) = u32x2{g1.x, g1.y};
            }
            BAR();
            f32x2 wA[5][4], u[4], v1[4], v2[4];
            float xv[5], ga[5];
#pragma unroll
            for (int k = 0; k < 4; ++k) { u[k] = f32x2{0.f, 0.f}; v1[k] = f32x2{0.f, 0.f}; v2[k] = f32x2{0.f, 0.f}; }
#pragma unroll
            for (int j = 0; j < 5; ++j) {
                const int m = w * 80 + 16 * j + rs;
                const float gav = gs[m], sbv = gs[640 + m], cbp = 1.f + gs[1280 + m];
                xv[j] = bf1(cv[j]); ga[j] = gav;
                const f32x2 A = splat(gav * cbp), X0 = splat(xv[j] * cbp), C = splat(cbp), S = splat(sbv);
                const f32x2 f[4] = {up2(gw[j].x), up2(gw[j].y), up2(gw[j].z), up2(gw[j].w)};
#pragma unroll
                for (int k = 0; k < 4; ++k) { wA[j][k] = A * f[k]; u[k] += X0 * f[k]; v1[k] += C * f[k]; v2[k] += S * f[k]; PIN1(wA[j][k]); }
            }
            const int e = ((lane >> 5) & 1) * 4 + ((lane >> 4) & 1) * 2 + ((lane >> 3) & 1);
            LAS float* rp = (LAS float*)(L + L_RP);
            {
                float a[8] = {v1[0].x, v1[0].y, v1[1].x, v1[1].y, v1[2].x, v1[2].y, v1[3].x, v1[3].y};
                float v = rscat8(a, lane); v += dpp<0x104>(v);
                if (!(lane & 4)) rp[w * 32 + ck * 8 + e] = v;
                float b[8] = {v2[0].x, v2[0].y, v2[1].x, v2[1].y, v2[2].x, v2[2].y, v2[3].x, v2[3].y};
                v = rscat8(b, lane); v += dpp<0x104>(v);
                if (!(lane & 4)) rp[256 + w * 32 + ck * 8 + e] = v;
            }
            BAR();
            f32x2 V1 = {0.f, 0.f}, V2 = {0.f, 0.f};
            if (w == 0) {
#pragma unroll
                for (int k = 0; k < 8; ++k) { V1 += *(LAS f32x2*)(rp + k * 32 + (lane & 15) * 2); V2 += *(LAS f32x2*)(rp + 256 + k * 32 + (lane & 15) * 2); }
                PIN1(V1); PIN1(V2);
            }
#pragma unroll
            for (int k = 0; k < 4; ++k) PIN1(u[k]);
            STAMP(8);
            LAS float* r6 = (LAS float*)(L + L_R6);
            {
                const bool act = t < 480;
                const int pg = t / 160, q = t - pg * 160;
                const u32 goff = OFF_E6 + (u32)(4 * pg * 640 + 4 * q) * 4;
                u32x4 eg[4];
#pragma unroll
                for (int k = 0; k < 4; ++k) eg[k] = u32x4{0u, 0u, 0u, 0u};
                for (int it = 0; it < SPIN; ++it) { CFENCE();
                    if (act) {
#pragma unroll
                        for (int k = 0; k < 4; ++k) eg[k] = ld4(rws, goff + k * 2560, AUX_SC1);
                    }
                    if (!anyl(pois4(eg[0]) | pois4(eg[1]) | pois4(eg[2]) | pois4(eg[3]))) break; PSLEEP();
                }
                const f32x4 s = (__builtin_bit_cast(f32x4, eg[0]) + __builtin_bit_cast(f32x4, eg[1])) + (__builtin_bit_cast(f32x4, eg[2]) + __builtin_bit_cast(f32x4, eg[3]));
                if (act) *(LAS f32x4*)(r6 + pg * 640 + 4 * q) = s;
            }
            STAMP(9);
            BAR();
            float x1[5], sum = 0.f;
#pragma unroll
            for (int j = 0; j < 5; ++j) {
                const int m = w * 80 + 16 * j + rs;
                const float at = (r6[m] + r6[640 + m]) + r6[1280 + m];
                x1[j] = xv[j] + ga[j] * at; sum += x1[j];
                const f32x2 A = splat(at);
#pragma unroll
                for (int k = 0; k < 4; ++k) u[k] += A * wA[j][k];
            }
            sum += dpp<0x124>(sum); sum += dpp<0x128>(sum); sum = rs16(sum, sum); sum = rs32(sum, sum);
            const float mw_ = sum * (1.f / 80.f);
            float q = 0.f;
#pragma unroll
            for (int j = 0; j < 5; ++j) { const float d = x1[j] - mw_; q += d * d; }
            q += dpp<0x124>(q); q += dpp<0x128>(q); q = rs16(q, q); q = rs32(q, q);
            {
                float a[8] = {u[0].x, u[0].y, u[1].x, u[1].y, u[2].x, u[2].y, u[3].x, u[3].y};
                float v = rscat8(a, lane); v += dpp<0x104>(v);
                LAS float* rf = (LAS float*)(L + L_RF);
                if (!(lane & 4)) rf[w * 32 + ck * 8 + e] = v;
                LAS float* sst = (LAS float*)(L + L_ST);
                if (lane == 0) *(LAS f32x2*)(sst + 2 * w) = f32x2{mw_, q};
                BAR();
                if (w == 0 && lane < 16) {
                    f32x2 p[8], s8[8];
#pragma unroll
                    for (int k = 0; k < 8; ++k) { p[k] = *(LAS f32x2*)(rf + k * 32 + lane * 2); s8[k] = *(LAS f32x2*)(sst + 2 * k); }
                    const f32x2 U = ((p[0] + p[1]) + (p[2] + p[3])) + ((p[4] + p[5]) + (p[6] + p[7]));
                    const f32x2 SS = ((s8[0] + s8[1]) + (s8[2] + s8[3])) + ((s8[4] + s8[5]) + (s8[6] + s8[7]));
                    const float mean = SS.x * 0.125f;
                    float dv = 0.f;
#pragma unroll
                    for (int k = 0; k < 8; ++k) { const float d = s8[k].x - mean; dv += d * d; }
                    const float var = (SS.y + 80.f * dv) * (1.f / 640.f);
                    const float rstd = __builtin_amdgcn_rsqf(var + 1e-5f);
                    const float h0 = fmaxf(rstd * (U.x - mean * V1.x) + V2.x, 0.f), h1 = fmaxf(rstd * (U.y - mean * V1.y) + V2.y, 0.f);
                    st4(rws, OFF_HID + (u32)(xi * 16 + lane) * 16, u32x4{__float_as_uint(h0), __float_as_uint(h1), tlo, thi});
                }
            }
            STAMP(10);
        } else if (xi < 88) {
            const int fi = xi - 48, s2 = 5 * (fi & 7) + (fi >> 3);
            const rsrc_t r2 = mkrs(w2, DFF * DM * 2);
            const int chunk = t & 1, q = t >> 1;
            u32x4 fw[6];
#pragma unroll
            for (int i = 0; i < 3; ++i)
#pragma unroll
                for (int e2 = 0; e2 < 2; ++e2) fw[2 * i + e2] = ld4(r2, (u32)((2 * (q + 256 * i) + e2) * DM + 16 * s2 + 8 * chunk) * 2, AUX_NT);
            SB();
            f32x4 x1 = {0.f, 0.f, 0.f, 0.f}, gb = {0.f, 0.f, 0.f, 0.f};
            if (w == 0) {
                const int c4 = lane & 3, p = lane >> 2;
                const u32x2 xx = *(const u32x2*)(x + 16 * s2 + 4 * c4);
                u32x4 ga0, ga1, gb0, gb1;
                for (int it = 0; it < SPIN; ++it) { CFENCE();
                    ga0 = ld4(rws, OFF_G + (u32)(8 * s2 + 2 * c4) * 16, AUX_SC1); ga1 = ld4(rws, OFF_G + (u32)(8 * s2 + 2 * c4 + 1) * 16, AUX_SC1);
                    gb0 = ld4(rws, OFF_G + (u32)(960 + 8 * s2 + 2 * c4) * 16, AUX_SC1); gb1 = ld4(rws, OFF_G + (u32)(960 + 8 * s2 + 2 * c4 + 1) * 16, AUX_SC1);
                    if (!anyl(tagbad(ga0, tlo, thi) | tagbad(ga1, tlo, thi) | tagbad(gb0, tlo, thi) | tagbad(gb1, tlo, thi))) break; PSLEEP();
                }
                u32x4 g = {0u, 0u, 0u, 0u};
                for (int it = 0; it < SPIN; ++it) { CFENCE();
                    if (p < 12) g = ld4(rws, OFF_E6 + (u32)(p * 640 + 16 * s2 + 4 * c4) * 4, AUX_SC1);
                    if (!anyl(pois4(g))) break; PSLEEP();
                }
                f32x4 at = __builtin_bit_cast(f32x4, g);
#pragma unroll
                for (int k = 0; k < 4; ++k) { float v = at[k]; v += dpp<0x124>(v); v += dpp<0x128>(v); v = rs16(v, v); v = rs32(v, v); at[k] = v; }
                x1 = f32x4{bflo(xx.x) + __uint_as_float(ga0.x) * at.x, bfhi(xx.x) + __uint_as_float(ga0.y) * at.y, bflo(xx.y) + __uint_as_float(ga1.x) * at.z, bfhi(xx.y) + __uint_as_float(ga1.y) * at.w};
                gb = f32x4{__uint_as_float(gb0.x), __uint_as_float(gb0.y), __uint_as_float(gb1.x), __uint_as_float(gb1.y)};
                asm volatile("" : "+v"(x1), "+v"(gb));
            }
            f32x2 ff[6][4];
#pragma unroll
            for (int j = 0; j < 6; ++j) { ff[j][0] = up2(fw[j].x); ff[j][1] = up2(fw[j].y); ff[j][2] = up2(fw[j].z); ff[j][3] = up2(fw[j].w); PIN1(ff[j][0]); PIN1(ff[j][1]); PIN1(ff[j][2]); PIN1(ff[j][3]); }
            STAMP(8);
            u32x4 hg[3];
            for (int it = 0; it < SPIN; ++it) { CFENCE();
#pragma unroll
                for (int i = 0; i < 3; ++i) hg[i] = ld4(rws, OFF_HID + (u32)(q + 256 * i) * 16, AUX_SC1);
                if (!anyl(tagbad(hg[0], tlo, thi) | tagbad(hg[1], tlo, thi) | tagbad(hg[2], tlo, thi))) break; PSLEEP();
            }
            STAMP(9);
            f32x2 acc[4] = {{0.f, 0.f}, {0.f, 0.f}, {0.f, 0.f}, {0.f, 0.f}};
#pragma unroll
            for (int i = 0; i < 3; ++i) {
                const f32x2 h0 = splat(__uint_as_float(hg[i].x)), h1 = splat(__uint_as_float(hg[i].y));
#pragma unroll
                for (int k = 0; k < 4; ++k) { acc[k] += h0 * ff[2 * i][k]; acc[k] += h1 * ff[2 * i + 1][k]; }
            }
            float a[8] = {acc[0].x, acc[0].y, acc[1].x, acc[1].y, acc[2].x, acc[2].y, acc[3].x, acc[3].y};
            float v = rscat8(a, lane);
            v += dpp<0x4E>(v); v += dpp<0x104>(v);
            const int e = ((lane >> 5) & 1) * 4 + ((lane >> 4) & 1) * 2 + ((lane >> 3) & 1);
            LAS float* r2l = (LAS float*)(L + L_R2);
            if (!(lane & 6)) r2l[w * 16 + chunk * 8 + e] = v;
            BAR();
            if (t < 4) {
                f32x4 p[8];
#pragma unroll
                for (int k = 0; k < 8; ++k) p[k] = *(LAS f32x4*)(r2l + k * 16 + t * 4);
                const f32x4 s = ((p[0] + p[1]) + (p[2] + p[3])) + ((p[4] + p[5]) + (p[6] + p[7]));
                const f32x4 o = x1 + gb * s;
                *(u32x2*)(out + 16 * s2 + 4 * t) = u32x2{cvtpk(o.x, o.y), cvtpk(o.z, o.w)};
            }
            if (xi == 48 && t == 0) __hip_atomic_store((GAS u64*)(ws + OFF_EPOCH), epoch + 1ull, __ATOMIC_RELAXED, __HIP_MEMORY_SCOPE_AGENT);
            STAMP(10);
        }
    } else {
        const int yi = bid - 128, h = yi & 31, mg = yi >> 5;
        const rsrc_t rq = mkrs(wq, DM * 6144 * 2);
        u32x3 qw[10];
        wait_until(tstart, D_WQ);
        {
            const u32 base = (u32)(((mg * 160 + w * 20 + (lane >> 5)) * 32 + h) * 192 + 6 * (lane & 31)) * 2;
#pragma unroll
            for (int j = 0; j < 10; ++j) qw[j] = ld3(rq, base + j * 2 * 12288, AUX_NT);
            SB();
        }
        if (w == 1 && lane < 16 && yi < 120) st4(rws, OFF_E6 + (u32)((yi % 12) * 640 + (yi / 12) * 64) * 4 + lane * 16, PZ);
        SB();
        const unsigned char* src = (const unsigned char*)ckv + (size_t)yi * 64 * 1536;
        u32 koff[12];
#pragma unroll
        for (int j = 0; j < 12; ++j) {
            const int idx = j * 64 + lane, row = w * 8 + idx / 96, p = idx % 96;
            const int ch = (p & ~15) | ((p ^ row) & 15);
            koff[j] = (u32)(row * 1536 + ch * 16);
            PIN1(koff[j]);
        }
#pragma unroll
        for (int j = 0; j < CKV_EARLY; ++j)
            __builtin_amdgcn_global_load_lds((const GAS u32*)(src + koff[j]), (LAS u32*)(L + L_KV + (w * 12 + j) * 1024), 16, 0, AUX_NT);
        SB();
        f32x2 qf2[10][3];
#pragma unroll
        for (int j = 0; j < 10; ++j) { qf2[j][0] = up2(qw[j].x); qf2[j][1] = up2(qw[j].y); qf2[j][2] = up2(qw[j].z); PIN1(qf2[j][0]); PIN1(qf2[j][1]); PIN1(qf2[j][2]); }
        STAMP(9);
        {
            const bool act = (lane & 15) < 10;
            const u32 goff = OFF_E1 + (u32)(((lane >> 4) * 4 + mg) * 8 + w) * 256 + (lane & 15) * 16;
            u32x4 g0 = {0u, 0u, tlo, thi}, g1 = {0u, 0u, tlo, thi};
            for (int it = 0; it < SPIN; ++it) { CFENCE();
                if (act) { g0 = ld4(rws, goff, AUX_SC1); g1 = ld4(rws, goff + 32768, AUX_SC1); }
                if (!anyl(tagbad(g0, tlo, thi) | tagbad(g1, tlo, thi))) break; PSLEEP();
            }
            DRAIN();
            STAMP(1);
            float s = rs32(__uint_as_float(g0.x) + __uint_as_float(g1.x), __uint_as_float(g0.y) + __uint_as_float(g1.y));
            s = rs16(s, s);
            f32x2 a[3] = {{0.f, 0.f}, {0.f, 0.f}, {0.f, 0.f}};
#define QSTEP(j) { const f32x2 hv = splat(dpp<0x150 + j>(s)); a[0] += hv * qf2[j][0]; a[1] += hv * qf2[j][1]; a[2] += hv * qf2[j][2]; }
            QSTEP(0) QSTEP(1) QSTEP(2) QSTEP(3) QSTEP(4) QSTEP(5) QSTEP(6) QSTEP(7) QSTEP(8) QSTEP(9)
#undef QSTEP
            LAS float* red = (LAS float*)(L + L_RQ);
            {
                LAS float* dst = red + w * 192 + 6 * (lane & 31) + 3 * (lane >> 5);
                dst[0] = rs32(a[0].x, a[1].y); dst[1] = rs32(a[0].y, a[2].x); dst[2] = rs32(a[1].x, a[2].y);
            }
            BAR();
            if (t < 96) {
                f32x2 p[8];
#pragma unroll
                for (int k = 0; k < 8; ++k) p[k] = *(LAS f32x2*)(red + k * 192 + t * 2);
                const f32x2 v = ((p[0] + p[1]) + (p[2] + p[3])) + ((p[4] + p[5]) + (p[6] + p[7]));
                st4(rws, OFF_E2 + (u32)((mg * 32 + h) * 96 + t) * 16, u32x4{__float_as_uint(v.x), __float_as_uint(v.y), tlo, thi});
            }
            BAR();
        }
        STAMP(2);
#pragma unroll
        for (int j = CKV_EARLY; j < 12; ++j)
            __builtin_amdgcn_global_load_lds((const GAS u32*)(src + koff[j]), (LAS u32*)(L + L_KV + (w * 12 + j) * 1024), 16, 0, AUX_NT);
        SB();
        {
            u32x4 f = {0u, 0u, tlo, thi};
            for (int it = 0; it < SPIN; ++it) { CFENCE();
                if (lane < 16) f = ld4(rws, OFF_PF + (u32)(w * 16 + lane) * 16, AUX_SC1);
                if (!anyl(tagbad(f, tlo, thi))) break; PSLEEP();
            }
        }
        DRAIN();
        BAR();
        STAMP(3);
        const int kq = w >> 1, ht = w & 1;
        {
            const int r = lane & 15;
            const int keyr = 8 * (r >> 3) + 2 * (r & 3) + ((r >> 2) & 1);
            u32x4 af[6][4];
#pragma unroll
            for (int ks = 0; ks < 6; ++ks) {
                const int ch = kq * 24 + ks * 4 + (lane >> 4);
                const int p = (ch & ~15) | ((ch ^ keyr) & 15);
#pragma unroll
                for (int kt = 0; kt < 4; ++kt) { af[ks][kt] = *(LAS u32x4*)(L + L_KV + (kt * 16 + keyr) * 1536 + p * 16); }
            }
#pragma unroll
            for (int ks = 0; ks < 6; ++ks)
#pragma unroll
                for (int kt = 0; kt < 4; ++kt) PIN1(af[ks][kt]);
            u32x4 qf[12];
            const u32 qoff = OFF_E3 + (u32)(kq * 12 * 32 + ht * 16 + (lane & 15)) * 64 + (lane >> 4) * 16;
            for (int it = 0; it < SPIN; ++it) { CFENCE();
                qf[0] = ld4(rws, qoff, AUX_SC1);
                if (!anyl(pois4(qf[0]))) break; PSLEEP();
            }
            STAMP(10);
            f32x4 acc[4];
            for (int it = 0; it < SPIN; ++it) { CFENCE();
#pragma unroll
                for (int j = 1; j < 12; ++j) qf[j] = ld4(rws, qoff + j * 2048, AUX_SC1);
                SB();
#pragma unroll
                for (int kt = 0; kt < 4; ++kt) acc[kt] = f32x4{0.f, 0.f, 0.f, 0.f};
                u32 pm = 0;
#pragma unroll
                for (int j = 0; j < 12; ++j) {
#pragma unroll
                    for (int kt = 0; kt < 4; ++kt) acc[kt] = mfma(af[j >> 1][kt], qf[j], acc[kt]);
                    pm = max(pm, max(max(qf[j].x, qf[j].y), max(qf[j].z, qf[j].w)));
                }
                if (!anyl(pm == POISON)) break; PSLEEP();
            }
            STAMP(4);
            LAS float* sp = (LAS float*)(L + L_SP);
#pragma unroll
            for (int kt = 0; kt < 4; ++kt) *(LAS f32x4*)(sp + (kq * 32 + ht * 16 + (lane & 15)) * 68 + kt * 16 + 4 * (lane >> 4)) = acc[kt];
        }
        const int wrg = yi % 12, wcs = yi / 12;
        u32x4 ow[8];
        BAR();
        const int ocq = w & 3, part = w >> 2;
        u32x4 va[8][2];
        float mlm = 0.f, mll = 0.f;
        {
            LAS float* sp = (LAS float*)(L + L_SP);
            const int hd = t >> 4, qd = t & 15;
            f32x4 s = *(LAS f32x4*)(sp + hd * 68 + qd * 4);
#pragma unroll
            for (int k = 1; k < 4; ++k) s += *(LAS f32x4*)(sp + (k * 32 + hd) * 68 + qd * 4);
            PIN1(s);
            SB();
            {
                const int g = lane >> 4, e = (lane >> 2) & 3, pp = lane & 3;
                const int tb = ocq * 12 + part * 8;
#pragma unroll
                for (int tl = 0; tl < 8; ++tl) {
                    if (tl < 4 || part == 0) {
                        const int ch = (tb + tl) * 2 + (pp >> 1);
#pragma unroll
                        for (int k2 = 0; k2 < 2; ++k2) {
                            const int ka = k2 * 32 + 8 * g + 2 * e, kb = ka + 1;
                            const int pa = (ch & ~15) | ((ch ^ ka) & 15), pbx = (ch & ~15) | ((ch ^ kb) & 15);
                            const s16x4 t0 = __builtin_amdgcn_ds_read_tr16_b64_v4i16((LAS s16x4*)(L + L_KV + ka * 1536 + pa * 16 + (pp & 1) * 8));
                            const s16x4 t1 = __builtin_amdgcn_ds_read_tr16_b64_v4i16((LAS s16x4*)(L + L_KV + kb * 1536 + pbx * 16 + (pp & 1) * 8));
                            const u32x2 a0 = __builtin_bit_cast(u32x2, t0), a1 = __builtin_bit_cast(u32x2, t1);
                            va[tl][k2] = u32x4{a0.x, a0.y, a1.x, a1.y};
                        }
                    }
                }
            }
            SB();
            const float m = row_max(fmaxf(fmaxf(s.x, s.y), fmaxf(s.z, s.w)));
            const float p0 = __builtin_amdgcn_exp2f(s.x - m), p1 = __builtin_amdgcn_exp2f(s.y - m), p2 = __builtin_amdgcn_exp2f(s.z - m), p3 = __builtin_amdgcn_exp2f(s.w - m);
            const u32 b01 = cvtpk(p0, p1), b23 = cvtpk(p2, p3);
            *(LAS u32x2*)(L + L_P + hd * 144 + qd * 8) = u32x2{b01, b23};
            const float l = row_sum((bflo(b01) + bfhi(b01)) + (bflo(b23) + bfhi(b23)));
            if (qd == 0) st4(rws, OFF_ML + (u32)(hd * 128 + yi) * 16, u32x4{__float_as_uint(m), __float_as_uint(l), tlo, thi});
        }
        BAR();
        STAMP(5);
        {
            u32x4 pb[2][2];
#pragma unroll
            for (int hh = 0; hh < 2; ++hh)
#pragma unroll
                for (int k2 = 0; k2 < 2; ++k2) pb[hh][k2] = *(LAS u32x4*)(L + L_P + (hh * 16 + (lane & 15)) * 144 + k2 * 64 + (lane >> 4) * 16);
            const int n = lane & 15, g = lane >> 4;
            const u32 obase = OFF_O + (u32)(((n & 7) * 4 + ocq) * 128 + yi) * 384 + part * 256 + (n >> 3) * 64 + (g & 1) * 32 + (g >> 1) * 16;
            constexpr u32 HS = 4 * 49152;
#pragma unroll
            for (int ln = 0; ln < 2; ++ln) {
                if (ln == 0 || part == 0) {
#pragma unroll
                    for (int hh = 0; hh < 2; ++hh) {
                        u32 xl[4], xh[4];
#pragma unroll
                        for (int q4 = 0; q4 < 4; ++q4) {
                            f32x4 acc = f32x4{0.f, 0.f, 0.f, 0.f};
                            acc = mfma(va[ln * 4 + q4][0], pb[hh][0], acc);
                            acc = mfma(va[ln * 4 + q4][1], pb[hh][1], acc);
                            xl[q4] = cvtpk(acc.x, acc.y); xh[q4] = cvtpk(acc.z, acc.w);
                        }
                        auto r0 = __builtin_amdgcn_permlane16_swap(xl[0], xl[1], false, false);
                        auto r1 = __builtin_amdgcn_permlane16_swap(xh[0], xh[1], false, false);
                        auto r2 = __builtin_amdgcn_permlane16_swap(xl[2], xl[3], false, false);
                        auto r3 = __builtin_amdgcn_permlane16_swap(xh[2], xh[3], false, false);
                        const u32 x01[4] = {r0[0], r1[0], r0[1], r1[1]}, x23[4] = {r2[0], r3[0], r2[1], r3[1]};
                        u32 A[4], B[4];
#pragma unroll
                        for (int i = 0; i < 4; ++i) {
                            A[i] = canon((u32)__builtin_amdgcn_update_dpp((int)x01[i], (int)x23[i], 0x128, 0xf, 0xC, false));
                            B[i] = canon((u32)__builtin_amdgcn_update_dpp((int)x23[i], (int)x01[i], 0x128, 0xf, 0x3, false));
                        }
                        if (ln == 0 && hh == 0) STAMP(11);
                        __builtin_amdgcn_raw_buffer_store_b128(u32x4{A[0], A[1], A[2], A[3]}, rws, obase + (u32)(hh * 16) * HS + ln * 128, 0, O_AUX);
                        __builtin_amdgcn_raw_buffer_store_b128(u32x4{B[0], B[1], B[2], B[3]}, rws, obase + (u32)(hh * 16 + 8) * HS + ln * 128, 0, O_AUX);
                    }
                    if (ln == 0) STAMP(12);
                }
            }
        }
        STAMP(6);
        if (yi < 120) {
            BAR();
            {
                const rsrc_t ro = mkrs(wo, 6144 * DM * 2);
                const u32 base = (u32)((wrg * 512 + w * 64 + (lane >> 3) * 8) * DM + wcs * 64 + (lane & 7) * 8) * 2;
#pragma unroll
                for (int j = 0; j < 8; ++j) ow[j] = ld4(ro, base + j * 1280, AUX_NT);
                SB();
            }
            f32x2 of[8][4];
#pragma unroll
            for (int j = 0; j < 8; ++j) { of[j][0] = up2(ow[j].x); of[j][1] = up2(ow[j].y); of[j][2] = up2(ow[j].z); of[j][3] = up2(ow[j].w); PIN1(of[j][0]); PIN1(of[j][1]); PIN1(of[j][2]); PIN1(of[j][3]); }
            const int cc8 = lane & 7;
            const u32 goff = OFF_E5 + (u32)((cc8 < 4 ? 0 : 2) * 3072 + wrg * 256 + w * 32 + (lane >> 3) * 4 + (cc8 < 4 ? cc8 : 7 - cc8)) * 16;
            u32x4 g0, g1;
            for (int it = 0; it < SPIN; ++it) { CFENCE();
                g0 = ld4(rws, goff, AUX_SC1); g1 = ld4(rws, goff + 3072 * 16, AUX_SC1);
                if (!anyl(tagbad(g0, tlo, thi) | tagbad(g1, tlo, thi))) break; PSLEEP();
            }
            STAMP(7);
            float vx = __uint_as_float(g0.x) + __uint_as_float(g1.x), vy = __uint_as_float(g0.y) + __uint_as_float(g1.y);
            vx += dpp<0x141>(vx); vy += dpp<0x141>(vy);
            vx = dppb<0x1B, 0xA>(vx, vx); vy = dppb<0x1B, 0xA>(vy, vy);
            f32x2 a2[4] = {{0.f, 0.f}, {0.f, 0.f}, {0.f, 0.f}, {0.f, 0.f}};
#define WSTEP(k) { const f32x2 hx = splat(dpp<k * 0x55>(vx)), hy = splat(dpp<k * 0x55>(vy)); _Pragma("unroll") for (int i = 0; i < 4; ++i) { a2[i] += hx * of[2 * k][i]; a2[i] += hy * of[2 * k + 1][i]; } }
            WSTEP(0) WSTEP(1) WSTEP(2) WSTEP(3)
#undef WSTEP
            float a[8] = {a2[0].x, a2[0].y, a2[1].x, a2[1].y, a2[2].x, a2[2].y, a2[3].x, a2[3].y};
            const float v = rscat8(a, lane);
            const int e = ((lane >> 5) & 1) * 4 + ((lane >> 4) & 1) * 2 + ((lane >> 3) & 1);
            LAS float* red = (LAS float*)(L + L_RW);
            red[w * 64 + (lane & 7) * 8 + e] = v;
            BAR();
            if (w == 0 && lane < 16) {
                f32x4 p[8];
#pragma unroll
                for (int k = 0; k < 8; ++k) p[k] = *(LAS f32x4*)(red + k * 64 + lane * 4);
                const f32x4 s4 = ((p[0] + p[1]) + (p[2] + p[3])) + ((p[4] + p[5]) + (p[6] + p[7]));
                st4(rws, OFF_E6 + (u32)(wrg * 640 + wcs * 64) * 4 + lane * 16, u32x4{canonf(s4.x), canonf(s4.y), canonf(s4.z), canonf(s4.w)});
            }
            STAMP(8);
        }
    }
}

extern "C" void kernel_launch(void* const* d_in, const int* in_sizes, int n_in, void* d_out, int out_size, void* d_ws, size_t ws_size, hipStream_t stream) {
    (void)in_sizes; (void)n_in; (void)out_size; (void)ws_size;
    hipFuncSetAttribute((const void*)adaln_mla_mlp_kernel, hipFuncAttributeMaxDynamicSharedMemorySize, LDS_BYTES);
    adaln_mla_mlp_kernel<<<NWG, NT, LDS_BYTES, stream>>>(
        (const u16*)d_in[0], (const u16*)d_in[1], (const u16*)d_in[2], (const u16*)d_in[3], (const u16*)d_in[4], (const u16*)d_in[5],
        (const u16*)d_in[7], (const u16*)d_in[8], (const u16*)d_in[9], (const u16*)d_in[10], (u16*)d_out, (unsigned char*)d_ws);
}
```

```cpp
#include <hip/hip_runtime.h>
#include <stdint.h>

typedef unsigned u32;
typedef unsigned long long u64;
typedef unsigned short u16;
typedef u32 u32x2 __attribute__((ext_vector_type(2)));
typedef u32 u32x3 __attribute__((ext_vector_type(3)));
typedef u32 u32x4 __attribute__((ext_vector_type(4)));
typedef float f32x2 __attribute__((ext_vector_type(2)));
typedef float f32x4 __attribute__((ext_vector_type(4)));
typedef short bf16x8 __attribute__((ext_vector_type(8)));
typedef short s16x4 __attribute__((ext_vector_type(4)));
typedef __amdgpu_buffer_rsrc_t rsrc_t;
#define LAS __attribute__((address_space(3)))
#define GAS __attribute__((address_space(1)))

constexpr int NH = 32, SK = 8192, HD = 192, DM = 640, DC = 768, DFF = 1536;
constexpr int NWG = 256, NT = 512;

#define UK1 6
#define D_WQ 0
#define D_CKV 100
#define CKV1 5
#define POIS_POS 1
#define ML_POS 0
#define ML_SLEEP 4
constexpr u32 OFF_EPOCH = 0;
constexpr u32 OFF_PF = 256;
constexpr u32 OFF_E1 = OFF_PF + 128 * 16;
constexpr u32 OFF_E2 = OFF_E1 + 8 * 4 * 8 * 256;
constexpr u32 OFF_E3 = OFF_E2 + 4 * 32 * 96 * 16;
constexpr u32 OFF_ML = OFF_E3 + 32 * 4 * 768;
constexpr u32 OFF_E5 = OFF_ML + 32 * 128 * 16;
constexpr u32 OFF_E6 = OFF_E5 + 4 * 3072 * 16;
constexpr u32 OFF_G = OFF_E6 + 12 * 640 * 4;
constexpr u32 OFF_HID = OFF_G + 1280 * 16;
constexpr u32 OFF_O = OFF_HID + 768 * 16;
constexpr u32 WS_BYTES = OFF_O + 32 * 4 * 128 * 384;
constexpr u32 OFF_DIAG = WS_BYTES;
static_assert(OFF_O % 128 == 0 && OFF_E3 % 128 == 0 && OFF_E5 % 128 == 0 && OFF_E2 % 256 == 0 && OFF_E6 % 128 == 0 && OFF_E1 % 256 == 0, "alignment");

constexpr int L_KV = 0;
constexpr int OSTR = 1552;
constexpr int L_SP = 98304;
constexpr int L_P = L_SP + 4 * 32 * 68 * 4;
constexpr int L_RM = L_P + 32 * 144;
constexpr int L_RW = L_RM + 8 * 128 * 4;
constexpr int L_ML = L_RW + 8 * 64 * 4;
constexpr int LDS_BYTES = L_ML + 256;
constexpr int L_RQ = 0;
constexpr int L_Q = L_RQ + 6144;
constexpr int L_RA = L_Q + 768;
constexpr int L_WL = L_RA + 768;
constexpr int L_RV1 = L_WL + 1024;
constexpr int L_RV2 = L_RV1 + 6144;
constexpr int L_RG = L_RV2 + 6144;
constexpr int L_GS = L_RG + 1024;
constexpr int L_RP = L_GS + 7680;
constexpr int L_RF = L_RP + 2048;
constexpr int L_ST = L_RF + 1024;
constexpr int L_R2 = L_ST + 64;
constexpr int L_R6 = L_R2 + 512;
constexpr int SPIN = 1 << 16;
constexpr u32 POISON = 0xFFFFFFFFu;
constexpr int AUX_SC1 = 16, AUX_NT = 2;
constexpr u64 TAGMUL = 0x9E3779B97F4A7C15ull;

__device__ __forceinline__ float bflo(u32 v) { return __uint_as_float(v << 16); }
__device__ __forceinline__ float bfhi(u32 v) { return __uint_as_float(v & 0xFFFF0000u); }
__device__ __forceinline__ float bf1(u16 v) { return __uint_as_float((u32)v << 16); }
typedef __bf16 bf16x2_t __attribute__((ext_vector_type(2)));
__device__ __forceinline__ u32 cvtpk(float lo, float hi) { f32x2 v = {lo, hi}; return __builtin_bit_cast(u32, __builtin_convertvector(v, bf16x2_t)); }
__device__ __forceinline__ u32 canon(u32 v) { return v < 0xFFFFFFFEu ? v : 0xFFFFFFFEu; }
__device__ __forceinline__ u32 canonf(float v) { return canon(__float_as_uint(v)); }
__device__ __forceinline__ rsrc_t mkrs(const void* p, u32 bytes) { return __builtin_amdgcn_make_buffer_rsrc((void*)p, 0, bytes, 0x00020000); }
__device__ __forceinline__ u32x4 ld4(rsrc_t r, u32 off, int aux) { return aux == AUX_SC1 ? __builtin_amdgcn_raw_buffer_load_b128(r, off, 0, AUX_SC1) : __builtin_amdgcn_raw_buffer_load_b128(r, off, 0, AUX_NT); }
__device__ __forceinline__ u32x3 ld3(rsrc_t r, u32 off, int aux) { return aux == AUX_SC1 ? __builtin_amdgcn_raw_buffer_load_b96(r, off, 0, AUX_SC1) : __builtin_amdgcn_raw_buffer_load_b96(r, off, 0, AUX_NT); }
__device__ __forceinline__ void st4(rsrc_t r, u32 off, u32x4 v) { __builtin_amdgcn_raw_buffer_store_b128(v, r, off, 0, AUX_SC1); }
__device__ __forceinline__ void st2(rsrc_t r, u32 off, u32x2 v) { __builtin_amdgcn_raw_buffer_store_b64(v, r, off, 0, AUX_SC1); }
#define CFENCE() asm volatile("" ::: "memory")
#define PSL 0
#define PSLEEP() do { if (PSL > 0) __builtin_amdgcn_s_sleep(PSL); } while (0)
#define SB() __builtin_amdgcn_sched_barrier(0)
#define BAR() asm volatile("s_waitcnt lgkmcnt(0)\n\ts_barrier" ::: "memory")
#define LGKM0() asm volatile("s_waitcnt lgkmcnt(0)" ::: "memory")
#define DRAIN() asm volatile("s_waitcnt vmcnt(0)" ::: "memory")

template <int CTRL> __device__ __forceinline__ float dpp(float x) { return __int_as_float(__builtin_amdgcn_update_dpp(0, __float_as_int(x), CTRL, 0xf, 0xf, true)); }
__device__ __forceinline__ float rs32(float a, float b) { auto r = __builtin_amdgcn_permlane32_swap(__float_as_uint(a), __float_as_uint(b), false, false); return __uint_as_float(r[0]) + __uint_as_float(r[1]); }
__device__ __forceinline__ float rs16(float a, float b) { auto r = __builtin_amdgcn_permlane16_swap(__float_as_uint(a), __float_as_uint(b), false, false); return __uint_as_float(r[0]) + __uint_as_float(r[1]); }
__device__ __forceinline__ float row_sum(float x) { x += dpp<0x128>(x); x += dpp<0x124>(x); x += dpp<0x122>(x); x += dpp<0x121>(x); return x; }
__device__ __forceinline__ float row_max(float x) { x = fmaxf(x, dpp<0x128>(x)); x = fmaxf(x, dpp<0x124>(x)); x = fmaxf(x, dpp<0x122>(x)); x = fmaxf(x, dpp<0x121>(x)); return x; }
__device__ __forceinline__ float wave_sum(float x) { x = row_sum(x); x = rs16(x, x); x = rs32(x, x); return x; }
__device__ __forceinline__ float wave_max(float x) {
    x = row_max(x);
    { auto r = __builtin_amdgcn_permlane16_swap(__float_as_uint(x), __float_as_uint(x), false, false); x = fmaxf(__uint_as_float(r[0]), __uint_as_float(r[1])); }
    { auto r = __builtin_amdgcn_permlane32_swap(__float_as_uint(x), __float_as_uint(x), false, false); x = fmaxf(__uint_as_float(r[0]), __uint_as_float(r[1])); }
    return x;
}
__device__ __forceinline__ float rscat8(const float (&a)[8], int lane) {
    float r0 = rs32(a[0], a[4]), r1 = rs32(a[1], a[5]), r2 = rs32(a[2], a[6]), r3 = rs32(a[3], a[7]);
    float s0 = rs16(r0, r2), s1 = rs16(r1, r3);
    float t0 = s0 + dpp<0x128>(s0), t1 = s1 + dpp<0x128>(s1);
    return (lane & 8) ? t1 : t0;
}
__device__ __forceinline__ bool pois4(u32x4 v) { u32 m = max(max(v.x, v.y), max(v.z, v.w)); return m == POISON; }
__device__ __forceinline__ bool pois3(u32x3 v) { u32 m = max(max(v.x, v.y), v.z); return m == POISON; }
__device__ __forceinline__ bool anyl(bool b) { return __builtin_amdgcn_ballot_w64(b) != 0ull; }
__device__ __forceinline__ f32x4 mfma(u32x4 a, u32x4 b, f32x4 c) { return __builtin_amdgcn_mfma_f32_16x16x32_bf16(__builtin_bit_cast(bf16x8, a), __builtin_bit_cast(bf16x8, b), c, 0, 0, 0); }
__device__ __forceinline__ float silu(float v) { return v / (1.f + __expf(-v)); }
__device__ __forceinline__ void unp8(u32x4 v, float (&f)[8]) { f[0] = bflo(v.x); f[1] = bfhi(v.x); f[2] = bflo(v.y); f[3] = bfhi(v.y); f[4] = bflo(v.z); f[5] = bfhi(v.z); f[6] = bflo(v.w); f[7] = bfhi(v.w); }
__device__ __forceinline__ void unp6(u32x3 v, float (&f)[6]) { f[0] = bflo(v.x); f[1] = bfhi(v.x); f[2] = bflo(v.y); f[3] = bfhi(v.y); f[4] = bflo(v.z); f[5] = bfhi(v.z); }

__device__ __forceinline__ void wait_until(u64 t0, int ticks) {
    if (ticks > 0) { for (int i = 0; i < 4096; ++i) { if (__builtin_amdgcn_s_memrealtime() - t0 >= (u64)ticks) break; __builtin_amdgcn_s_sleep(1); } }
}
#define STAMP(i) do { } while (0)

__device__ __forceinline__ void ln_stats(const float (&v)[10], float& mean, float& rstd) {
    float s = 0.f;
#pragma unroll
    for (int i = 0; i < 10; ++i) s += v[i];
    mean = wave_sum(s) * (1.f / 640.f);
    float q = 0.f;
#pragma unroll
    for (int i = 0; i < 10; ++i) { float d = v[i] - mean; q += d * d; }
    float var = wave_sum(q) * (1.f / 640.f);
    rstd = 1.f / sqrtf(var + 1e-5f);
}

#define CAS __attribute__((address_space(4)))
#define PIN1(a) asm volatile("" : "+v"(a))
template <int CTRL, int BANK> __device__ __forceinline__ float dppb(float old, float x) { return __int_as_float(__builtin_amdgcn_update_dpp(__float_as_int(old), __float_as_int(x), CTRL, 0xf, BANK, false)); }
__device__ __forceinline__ u32x2 ld2(rsrc_t r, u32 off) { return __builtin_amdgcn_raw_buffer_load_b64(r, off, 0, AUX_SC1); }
__device__ __forceinline__ u32 ld1(rsrc_t r, u32 off) { return __builtin_amdgcn_raw_buffer_load_b32(r, off, 0, AUX_SC1); }
__device__ __forceinline__ bool tagbad(u32x4 g, u32 tlo, u32 thi) { return (g.z != tlo) | (g.w != thi); }
__device__ __forceinline__ f32x2 up2(u32 v) { return f32x2{bflo(v), bfhi(v)}; }
__device__ __forceinline__ f32x2 splat(float v) { return f32x2{v, v}; }

__global__ __launch_bounds__(NT, 2) void adaln_mla_mlp_kernel(
    const u16* __restrict__ x, const u16* __restrict__ ckv, const u16* __restrict__ wq, const u16* __restrict__ wuk,
    const u16* __restrict__ wuv, const u16* __restrict__ wo, const u16* __restrict__ w1, const u16* __restrict__ w2,
    const u16* __restrict__ cc, const u16* __restrict__ wmod, u16* __restrict__ out, unsigned char* __restrict__ ws) {
    extern __shared__ __attribute__((aligned(16))) unsigned char smem[];
    LAS unsigned char* L = (LAS unsigned char*)smem;
    STAMP(0);
    const u64 tstart = __builtin_amdgcn_s_memrealtime();
    asm volatile("" ::"s"(x), "s"(ckv), "s"(wq), "s"(wuk), "s"(wuv), "s"(wo), "s"(w1), "s"(w2), "s"(cc), "s"(wmod), "s"(out), "s"(ws));
    const int t = threadIdx.x, lane = t & 63, w = __builtin_amdgcn_readfirstlane(t >> 6);
    const int bid = blockIdx.x;
    const rsrc_t rws = mkrs(ws, WS_BYTES);
    const u64 epoch = *(const CAS u64*)(ws + OFF_EPOCH);
    const u64 tag = (epoch + 1ull) * TAGMUL;
    const u32 tlo = (u32)tag, thi = (u32)(tag >> 32);
    const u32x4 PZ = {POISON, POISON, POISON, POISON};

    if (bid < 128) {
        const int xi = bid, h = xi & 31, cq = xi >> 5;
        const rsrc_t rmod = mkrs(wmod, DM * 3840 * 2);
        const u32 e3off = OFF_E3 + (u32)((cq * 12 + (lane >> 2)) * 32 + h) * 64 + (lane & 3) * 16;
        if (w == 7 && lane < 48) st4(rws, e3off, PZ);
        const rsrc_t rq = mkrs(wq, DM * 6144 * 2);
        const rsrc_t ruk = mkrs(wuk, DC * 6144 * 2);
        const u32 wbase = (u32)(((cq * 192 + w * 24 + (lane >> 5)) * 32 + h) * 192 + 6 * (lane & 31)) * 2;
        u32x3 qw[10], uw[12];
        wait_until(tstart, D_WQ);
        {
            const u32 base = (u32)(((cq * 160 + w * 20 + (lane >> 5)) * 32 + h) * 192 + 6 * (lane & 31)) * 2;
#pragma unroll
            for (int j = 0; j < 10; ++j) qw[j] = ld3(rq, base + j * 2 * 12288, AUX_NT);
            SB();
#pragma unroll
            for (int j = 0; j < UK1; ++j) uw[j] = ld3(ruk, wbase + j * 2 * 12288, AUX_NT);
            SB();
        }
        f32x2 qf2[10][3];
#pragma unroll
        for (int j = 0; j < 10; ++j) { qf2[j][0] = up2(qw[j].x); qf2[j][1] = up2(qw[j].y); qf2[j][2] = up2(qw[j].z); PIN1(qf2[j][0]); PIN1(qf2[j][1]); PIN1(qf2[j][2]); }
        STAMP(1);
        {
            const bool act = (lane & 15) < 10;
            const u32 goff = OFF_E1 + (u32)(((lane >> 4) * 4 + cq) * 8 + w) * 256 + (lane & 15) * 16;
            u32x4 g0 = {0u, 0u, tlo, thi}, g1 = {0u, 0u, tlo, thi};
            for (int it = 0; it < SPIN; ++it) { CFENCE();
                if (act) { g0 = ld4(rws, goff, AUX_SC1); g1 = ld4(rws, goff + 32768, AUX_SC1); }
                if (!anyl(tagbad(g0, tlo, thi) | tagbad(g1, tlo, thi))) break; PSLEEP();
            }
            DRAIN();
            STAMP(2);
            if (w == 7 && lane == 0) st4(rws, OFF_PF + xi * 16, u32x4{0u, 0u, tlo, thi});
            float s = rs32(__uint_as_float(g0.x) + __uint_as_float(g1.x), __uint_as_float(g0.y) + __uint_as_float(g1.y));
            s = rs16(s, s);
            f32x2 a[3] = {{0.f, 0.f}, {0.f, 0.f}, {0.f, 0.f}};
#define QSTEP(j) { const f32x2 hv = splat(dpp<0x150 + j>(s)); a[0] += hv * qf2[j][0]; a[1] += hv * qf2[j][1]; a[2] += hv * qf2[j][2]; }
            QSTEP(0) QSTEP(1) QSTEP(2) QSTEP(3) QSTEP(4) QSTEP(5) QSTEP(6) QSTEP(7) QSTEP(8) QSTEP(9)
#undef QSTEP
            LAS float* red = (LAS float*)(L + L_RQ);
            {
                LAS float* dst = red + w * 192 + 6 * (lane & 31) + 3 * (lane >> 5);
                dst[0] = rs32(a[0].x, a[1].y); dst[1] = rs32(a[0].y, a[2].x); dst[2] = rs32(a[1].x, a[2].y);
            }
            BAR();
            if (t < 96) {
                f32x2 p[8];
#pragma unroll
                for (int k = 0; k < 8; ++k) p[k] = *(LAS f32x2*)(red + k * 192 + t * 2);
                const f32x2 v = ((p[0] + p[1]) + (p[2] + p[3])) + ((p[4] + p[5]) + (p[6] + p[7]));
                st4(rws, OFF_E2 + (u32)((cq * 32 + h) * 96 + t) * 16, u32x4{__float_as_uint(v.x), __float_as_uint(v.y), tlo, thi});
            }
            BAR();
        }
        STAMP(3);
#pragma unroll
        for (int j = UK1; j < 12; ++j) uw[j] = ld3(ruk, wbase + j * 2 * 12288, AUX_NT);
        SB();
        f32x2 wf[12][3];
#pragma unroll
        for (int j = 0; j < 12; ++j) { wf[j][0] = up2(uw[j].x); wf[j][1] = up2(uw[j].y); wf[j][2] = up2(uw[j].z); PIN1(wf[j][0]); PIN1(wf[j][1]); PIN1(wf[j][2]); }
        STAMP(4);
        u32x4 gw[5]; u16 cv[5];
        const int rs = lane >> 2, ck = lane & 3;
        int g_s = 0;
        {
            LAS float* qs = (LAS float*)(L + L_Q);
            if (w < 6) {
                const u32 goff = OFF_E2 + (u32)(((lane >> 4) * 32 + h) * 96 + 16 * w + (lane & 15)) * 16;
                u32x4 g;
                for (int it = 0; it < SPIN; ++it) { CFENCE();
                    g = ld4(rws, goff, AUX_SC1);
                    if (!anyl(tagbad(g, tlo, thi))) break; PSLEEP();
                }
                float s = rs32(__uint_as_float(g.x), __uint_as_float(g.y));
                s = rs16(s, s);
                if (!(lane & 16)) qs[2 * (16 * w + (lane & 15)) + (lane >> 5)] = s;
            }
            STAMP(5);
            BAR();
            const f32x2 q0 = *(LAS f32x2*)(qs + 6 * (lane & 31)), q1 = *(LAS f32x2*)(qs + 6 * (lane & 31) + 2), q2 = *(LAS f32x2*)(qs + 6 * (lane & 31) + 4);
            float p[12];
#pragma unroll
            for (int j = 0; j < 12; ++j) { const f32x2 d = wf[j][0] * q0 + wf[j][1] * q1 + wf[j][2] * q2; p[j] = d.x + d.y; }
            float r[6], s[3];
#pragma unroll
            for (int j = 0; j < 6; ++j) r[j] = rs16(p[j], p[j + 6]);
#pragma unroll
            for (int j = 0; j < 3; ++j) {
                const float own = (lane & 8) ? r[j + 3] : r[j], oth = (lane & 8) ? r[j] : r[j + 3];
                float v = own + dpp<0x128>(oth);
                v += dpp<0x141>(v); v += dpp<0x1B>(v); v += dpp<0xB1>(v);
                s[j] = v;
            }
            LAS float* red = (LAS float*)(L + L_RA);
            {
                const int j = lane & 7;
                const float v = j == 0 ? s[0] : (j == 1 ? s[1] : s[2]);
                if (j < 3) red[w * 24 + 2 * (j + 3 * ((lane >> 3) & 1) + 6 * ((lane >> 4) & 1)) + (lane >> 5)] = v;
            }
            BAR();
            if (w == 0 && lane < 48) {
                const float sc = 0.07216878364870322f * 1.4426950408889634f;
                const int c0 = (lane >> 3) * 32 + (lane & 3) * 8;
                const f32x4 p0 = *(LAS f32x4*)(red + c0) * sc, p1 = *(LAS f32x4*)(red + c0 + 4) * sc;
                const float v8[8] = {p0.x, p0.y, p0.z, p0.w, p1.x, p1.y, p1.z, p1.w};
                u32 o[4];
#pragma unroll
                for (int i = 0; i < 4; ++i) {
                    const u32 hi = cvtpk(v8[2 * i], v8[2 * i + 1]);
                    const u32 lo = cvtpk(v8[2 * i] - bflo(hi), v8[2 * i + 1] - bfhi(hi));
                    o[i] = canon((lane & 4) ? lo : hi);
                }
                st4(rws, e3off, u32x4{o[0], o[1], o[2], o[3]});
            }
            BAR();
        }
        STAMP(6);
        const rsrc_t ruv = mkrs(wuv, DC * 6144 * 2);
        u32x3 vw[12];
#pragma unroll
        for (int j = 0; j < 12; ++j) vw[j] = ld3(ruv, wbase + j * 2 * 12288, AUX_NT);
        SB();
        if (xi >= 48) {
            const int gi = xi - 48;
            g_s = 2 * ((gi >> 4) * 8 + (gi & 7)) + ((gi >> 3) & 1);
            const int row0 = t >> 2;
            const u32 off = (u32)(row0 * 3840 + 1280 + 32 * g_s + 8 * ck) * 2;
#pragma unroll
            for (int j = 0; j < 5; ++j) gw[j] = ld4(rmod, off + j * 128 * 7680, AUX_NT);
#pragma unroll
            for (int j = 0; j < 5; ++j) cv[j] = cc[row0 + 128 * j];
        } else {
            const rsrc_t r1 = mkrs(w1, DM * DFF * 2);
            const u32 b1 = (u32)((w * 80 + rs) * DFF + 32 * xi + 8 * ck) * 2;
#pragma unroll
            for (int j = 0; j < 5; ++j) gw[j] = ld4(r1, b1 + j * 16 * DFF * 2, AUX_NT);
#pragma unroll
            for (int j = 0; j < 5; ++j) cv[j] = x[w * 80 + 16 * j + rs];
        }
        SB();
        if (xi >= 48) {
            float a[8];
#pragma unroll
            for (int i = 0; i < 8; ++i) a[i] = 0.f;
#pragma unroll
            for (int j = 0; j < 5; ++j) {
                float f[8]; unp8(gw[j], f); const float s = silu(bf1(cv[j]));
#pragma unroll
                for (int i = 0; i < 8; ++i) a[i] += s * f[i];
            }
            float v = rscat8(a, lane);
            v += dpp<0x104>(v);
            const int e = ((lane >> 5) & 1) * 4 + ((lane >> 4) & 1) * 2 + ((lane >> 3) & 1);
            LAS float* redg = (LAS float*)(L + L_RG);
            if (!(lane & 4)) redg[w * 32 + ck * 8 + e] = v;
            BAR();
            if (w == 0 && lane < 16) {
                f32x2 s = {0.f, 0.f};
#pragma unroll
                for (int i = 0; i < 8; ++i) s += *(LAS f32x2*)(redg + i * 32 + lane * 2);
                st4(rws, OFF_G + (g_s * 16 + lane) * 16, u32x4{__float_as_uint(s.x), __float_as_uint(s.y), tlo, thi});
            }
        }
        f32x2 vf[12][3];
#pragma unroll
        for (int j = 0; j < 12; ++j) { vf[j][0] = up2(vw[j].x); vf[j][1] = up2(vw[j].y); vf[j][2] = up2(vw[j].z); PIN1(vf[j][0]); PIN1(vf[j][1]); PIN1(vf[j][2]); }
        STAMP(7);
        {
            const u32 obase = OFF_O + (u32)(w * 16 + (lane >> 5)) * 49152 + (u32)(h * 4 + cq) * 384 + (lane & 31) * 12;
            constexpr u32 OJ = 2 * 49152;
            LAS float* red1 = (LAS float*)(L + L_RV1);
            LAS float* red2 = (LAS float*)(L + L_RV2);
            LAS f32x2* wl = (LAS f32x2*)(L + L_WL);
            if (w == 0) {
                const u32 mlA = OFF_ML + (u32)(h * 128 + lane) * 16;
                u32x4 ma, mb;
                for (int it = 0; it < SPIN; ++it) { CFENCE();
                    ma = ld4(rws, mlA, AUX_SC1); mb = ld4(rws, mlA + 1024, AUX_SC1);
                    if (!anyl(tagbad(ma, tlo, thi) | tagbad(mb, tlo, thi))) break; if (ML_SLEEP > 0) __builtin_amdgcn_s_sleep(ML_SLEEP);
                }
                wl[lane] = f32x2{__uint_as_float(ma.x), __uint_as_float(ma.y)};
                wl[lane + 64] = f32x2{__uint_as_float(mb.x), __uint_as_float(mb.y)};
            }
            BAR();
            STAMP(8);
            u32x3 oa[8];
#pragma unroll
            for (int j = 0; j < 8; ++j) oa[j] = ld3(rws, obase + j * OJ, AUX_SC1);
            SB();
            float wA;
            {
                const f32x2 a = wl[lane], b = wl[lane + 64], own = wl[16 * w + 2 * (lane & 7) + (lane >> 5)];
                const float M = wave_max(fmaxf(a.x, b.x));
                const float Lsum = wave_sum(__builtin_amdgcn_exp2f(a.x - M) * a.y + __builtin_amdgcn_exp2f(b.x - M) * b.y);
                wA = __builtin_amdgcn_exp2f(own.x - M) * __builtin_amdgcn_rcpf(Lsum);
            }
            f32x2 pr[8][3];
            u32 pend = 0;
#define OSTEP(j) { if (anyl(pois3(oa[j]))) pend |= 1u << j; else { const f32x2 ww = splat(dpp<0x150 + j>(wA)); pr[j][0] = up2(oa[j].x) * ww; pr[j][1] = up2(oa[j].y) * ww; pr[j][2] = up2(oa[j].z) * ww; } }
            OSTEP(0) OSTEP(1) OSTEP(2) OSTEP(3) OSTEP(4) OSTEP(5) OSTEP(6) OSTEP(7)
#undef OSTEP
            for (int it = 0; it < SPIN && pend; ++it) { CFENCE();
#pragma unroll
                for (int j = 0; j < 8; ++j) if (pend & (1u << j)) oa[j] = ld3(rws, obase + j * OJ, AUX_SC1);
#define OSTEP(j) if (pend & (1u << j)) { if (!anyl(pois3(oa[j]))) { pend &= ~(1u << j); const f32x2 ww = splat(dpp<0x150 + j>(wA)); pr[j][0] = up2(oa[j].x) * ww; pr[j][1] = up2(oa[j].y) * ww; pr[j][2] = up2(oa[j].z) * ww; } }
                OSTEP(0) OSTEP(1) OSTEP(2) OSTEP(3) OSTEP(4) OSTEP(5) OSTEP(6) OSTEP(7)
#undef OSTEP
            }
            STAMP(9);
            f32x2 a2[3];
#pragma unroll
            for (int k = 0; k < 3; ++k) a2[k] = ((pr[0][k] + pr[1][k]) + (pr[2][k] + pr[3][k])) + ((pr[4][k] + pr[5][k]) + (pr[6][k] + pr[7][k]));
            {
                LAS float* dst = red1 + w * 192 + 6 * (lane & 31) + 3 * (lane >> 5);
                dst[0] = rs32(a2[0].x, a2[1].y); dst[1] = rs32(a2[0].y, a2[2].x); dst[2] = rs32(a2[1].x, a2[2].y);
            }
            BAR();
            float cx = 0.f;
            {
                const int i = lane & 15, c = w * 24 + 2 * (i < 12 ? i : 11) + (lane >> 5), cidx = c ^ ((c % 96) >= 48 ? 4 : 0);
                float p[8];
#pragma unroll
                for (int k = 0; k < 8; ++k) p[k] = red1[k * 192 + cidx];
                cx = ((p[0] + p[1]) + (p[2] + p[3])) + ((p[4] + p[5]) + (p[6] + p[7]));
            }
            f32x2 c2[3] = {{0.f, 0.f}, {0.f, 0.f}, {0.f, 0.f}};
#define VSTEP(j) { const f32x2 cvv = splat(dpp<0x150 + j>(cx)); c2[0] += cvv * vf[j][0]; c2[1] += cvv * vf[j][1]; c2[2] += cvv * vf[j][2]; }
            VSTEP(0) VSTEP(1) VSTEP(2) VSTEP(3) VSTEP(4) VSTEP(5) VSTEP(6) VSTEP(7) VSTEP(8) VSTEP(9) VSTEP(10) VSTEP(11)
#undef VSTEP
            {
                LAS float* dst = red2 + w * 192 + 6 * (lane & 31) + 3 * (lane >> 5);
                dst[0] = rs32(c2[0].x, c2[1].y); dst[1] = rs32(c2[0].y, c2[2].x); dst[2] = rs32(c2[1].x, c2[2].y);
            }
            BAR();
            if (t < 96) {
                f32x2 p[8];
#pragma unroll
                for (int k = 0; k < 8; ++k) p[k] = *(LAS f32x2*)(red2 + k * 192 + t * 2);
                const f32x2 s = ((p[0] + p[1]) + (p[2] + p[3])) + ((p[4] + p[5]) + (p[6] + p[7]));
                st4(rws, OFF_E5 + (u32)(cq * 3072 + h * 96 + t) * 16, u32x4{__float_as_uint(s.x), __float_as_uint(s.y), tlo, thi});
            }
        }
        BAR();
        STAMP(10);
        if (xi < 48) {
            LAS float* gs = (LAS float*)(L + L_GS);
            {
                u32x4 g0, g1;
                const bool two = t < 448;
                for (int it = 0; it < SPIN; ++it) { CFENCE();
                    g0 = ld4(rws, OFF_G + t * 16, AUX_SC1); bool bad = tagbad(g0, tlo, thi);
                    if (two) { g1 = ld4(rws, OFF_G + (t + 512) * 16, AUX_SC1); bad |= tagbad(g1, tlo, thi); }
                    if (!anyl(bad)) break; PSLEEP();
                }
                *(LAS u32x2*)(gs + 2 * t) = u32x2{g0.x, g0.y};
                if (two) *(LAS u32x2*)(gs + 2 * (t + 512)) = u32x2{g1.x, g1.y};
            }
            BAR();
            f32x2 wA[5][4], u[4], v1[4], v2[4];
            float xv[5], ga[5];
#pragma unroll
            for (int k = 0; k < 4; ++k) { u[k] = f32x2{0.f, 0.f}; v1[k] = f32x2{0.f, 0.f}; v2[k] = f32x2{0.f, 0.f}; }
#pragma unroll
            for (int j = 0; j < 5; ++j) {
                const int m = w * 80 + 16 * j + rs;
                const float gav = gs[m], sbv = gs[640 + m], cbp = 1.f + gs[1280 + m];
                xv[j] = bf1(cv[j]); ga[j] = gav;
                const f32x2 A = splat(gav * cbp), X0 = splat(xv[j] * cbp), C = splat(cbp), S = splat(sbv);
                const f32x2 f[4] = {up2(gw[j].x), up2(gw[j].y), up2(gw[j].z), up2(gw[j].w)};
#pragma unroll
                for (int k = 0; k < 4; ++k) { wA[j][k] = A * f[k]; u[k] += X0 * f[k]; v1[k] += C * f[k]; v2[k] += S * f[k]; PIN1(wA[j][k]); }
            }
            const int e = ((lane >> 5) & 1) * 4 + ((lane >> 4) & 1) * 2 + ((lane >> 3) & 1);
            LAS float* rp = (LAS float*)(L + L_RP);
            {
                float a[8] = {v1[0].x, v1[0].y, v1[1].x, v1[1].y, v1[2].x, v1[2].y, v1[3].x, v1[3].y};
                float v = rscat8(a, lane); v += dpp<0x104>(v);
                if (!(lane & 4)) rp[w * 32 + ck * 8 + e] = v;
                float b[8] = {v2[0].x, v2[0].y, v2[1].x, v2[1].y, v2[2].x, v2[2].y, v2[3].x, v2[3].y};
                v = rscat8(b, lane); v += dpp<0x104>(v);
                if (!(lane & 4)) rp[256 + w * 32 + ck * 8 + e] = v;
            }
            BAR();
            f32x2 V1 = {0.f, 0.f}, V2 = {0.f, 0.f};
            if (w == 0) {
#pragma unroll
                for (int k = 0; k < 8; ++k) { V1 += *(LAS f32x2*)(rp + k * 32 + (lane & 15) * 2); V2 += *(LAS f32x2*)(rp + 256 + k * 32 + (lane & 15) * 2); }
                PIN1(V1); PIN1(V2);
            }
#pragma unroll
            for (int k = 0; k < 4; ++k) PIN1(u[k]);
            STAMP(11);
            LAS float* r6 = (LAS float*)(L + L_R6);
            {
                const bool act = t < 480;
                const int pg = t / 160, q = t - pg * 160;
                const u32 goff = OFF_E6 + (u32)(4 * pg * 640 + 4 * q) * 4;
                u32x4 eg[4];
#pragma unroll
                for (int k = 0; k < 4; ++k) eg[k] = u32x4{0u, 0u, 0u, 0u};
                for (int it = 0; it < SPIN; ++it) { CFENCE();
                    if (act) {
#pragma unroll
                        for (int k = 0; k < 4; ++k) eg[k] = ld4(rws, goff + k * 2560, AUX_SC1);
                    }
                    if (!anyl(pois4(eg[0]) | pois4(eg[1]) | pois4(eg[2]) | pois4(eg[3]))) break; PSLEEP();
                }
                const f32x4 s = (__builtin_bit_cast(f32x4, eg[0]) + __builtin_bit_cast(f32x4, eg[1])) + (__builtin_bit_cast(f32x4, eg[2]) + __builtin_bit_cast(f32x4, eg[3]));
                if (act) *(LAS f32x4*)(r6 + pg * 640 + 4 * q) = s;
            }
            STAMP(12);
            BAR();
            float x1[5], sum = 0.f;
#pragma unroll
            for (int j = 0; j < 5; ++j) {
                const int m = w * 80 + 16 * j + rs;
                const float at = (r6[m] + r6[640 + m]) + r6[1280 + m];
                x1[j] = xv[j] + ga[j] * at; sum += x1[j];
                const f32x2 A = splat(at);
#pragma unroll
                for (int k = 0; k < 4; ++k) u[k] += A * wA[j][k];
            }
            sum += dpp<0x124>(sum); sum += dpp<0x128>(sum); sum = rs16(sum, sum); sum = rs32(sum, sum);
            const float mw_ = sum * (1.f / 80.f);
            float q = 0.f;
#pragma unroll
            for (int j = 0; j < 5; ++j) { const float d = x1[j] - mw_; q += d * d; }
            q += dpp<0x124>(q); q += dpp<0x128>(q); q = rs16(q, q); q = rs32(q, q);
            {
                float a[8] = {u[0].x, u[0].y, u[1].x, u[1].y, u[2].x, u[2].y, u[3].x, u[3].y};
                float v = rscat8(a, lane); v += dpp<0x104>(v);
                LAS float* rf = (LAS float*)(L + L_RF);
                if (!(lane & 4)) rf[w * 32 + ck * 8 + e] = v;
                LAS float* sst = (LAS float*)(L + L_ST);
                if (lane == 0) *(LAS f32x2*)(sst + 2 * w) = f32x2{mw_, q};
                BAR();
                if (w == 0 && lane < 16) {
                    f32x2 p[8], s8[8];
#pragma unroll
                    for (int k = 0; k < 8; ++k) { p[k] = *(LAS f32x2*)(rf + k * 32 + lane * 2); s8[k] = *(LAS f32x2*)(sst + 2 * k); }
                    const f32x2 U = ((p[0] + p[1]) + (p[2] + p[3])) + ((p[4] + p[5]) + (p[6] + p[7]));
                    const f32x2 SS = ((s8[0] + s8[1]) + (s8[2] + s8[3])) + ((s8[4] + s8[5]) + (s8[6] + s8[7]));
                    const float mean = SS.x * 0.125f;
                    float dv = 0.f;
#pragma unroll
                    for (int k = 0; k < 8; ++k) { const float d = s8[k].x - mean; dv += d * d; }
                    const float var = (SS.y + 80.f * dv) * (1.f / 640.f);
                    const float rstd = __builtin_amdgcn_rsqf(var + 1e-5f);
                    const float h0 = fmaxf(rstd * (U.x - mean * V1.x) + V2.x, 0.f), h1 = fmaxf(rstd * (U.y - mean * V1.y) + V2.y, 0.f);
                    st4(rws, OFF_HID + (u32)(xi * 16 + lane) * 16, u32x4{__float_as_uint(h0), __float_as_uint(h1), tlo, thi});
                }
            }

        } else if (xi < 88) {
            const int fi = xi - 48, s2 = 5 * (fi & 7) + (fi >> 3);
            const rsrc_t r2 = mkrs(w2, DFF * DM * 2);
            const int chunk = t & 1, q = t >> 1;
            u32x4 fw[6];
#pragma unroll
            for (int i = 0; i < 3; ++i)
#pragma unroll
                for (int e2 = 0; e2 < 2; ++e2) fw[2 * i + e2] = ld4(r2, (u32)((2 * (q + 256 * i) + e2) * DM + 16 * s2 + 8 * chunk) * 2, AUX_NT);
            SB();
            f32x4 x1 = {0.f, 0.f, 0.f, 0.f}, gb = {0.f, 0.f, 0.f, 0.f};
            if (w == 0) {
                const int c4 = lane & 3, p = lane >> 2;
                const u32x2 xx = *(const u32x2*)(x + 16 * s2 + 4 * c4);
                u32x4 ga0, ga1, gb0, gb1;
                for (int it = 0; it < SPIN; ++it) { CFENCE();
                    ga0 = ld4(rws, OFF_G + (u32)(8 * s2 + 2 * c4) * 16, AUX_SC1); ga1 = ld4(rws, OFF_G + (u32)(8 * s2 + 2 * c4 + 1) * 16, AUX_SC1);
                    gb0 = ld4(rws, OFF_G + (u32)(960 + 8 * s2 + 2 * c4) * 16, AUX_SC1); gb1 = ld4(rws, OFF_G + (u32)(960 + 8 * s2 + 2 * c4 + 1) * 16, AUX_SC1);
                    if (!anyl(tagbad(ga0, tlo, thi) | tagbad(ga1, tlo, thi) | tagbad(gb0, tlo, thi) | tagbad(gb1, tlo, thi))) break; PSLEEP();
                }
                u32x4 g = {0u, 0u, 0u, 0u};
                for (int it = 0; it < SPIN; ++it) { CFENCE();
                    if (p < 12) g = ld4(rws, OFF_E6 + (u32)(p * 640 + 16 * s2 + 4 * c4) * 4, AUX_SC1);
                    if (!anyl(pois4(g))) break; PSLEEP();
                }
                f32x4 at = __builtin_bit_cast(f32x4, g);
#pragma unroll
                for (int k = 0; k < 4; ++k) { float v = at[k]; v += dpp<0x124>(v); v += dpp<0x128>(v); v = rs16(v, v); v = rs32(v, v); at[k] = v; }
                x1 = f32x4{bflo(xx.x) + __uint_as_float(ga0.x) * at.x, bfhi(xx.x) + __uint_as_float(ga0.y) * at.y, bflo(xx.y) + __uint_as_float(ga1.x) * at.z, bfhi(xx.y) + __uint_as_float(ga1.y) * at.w};
                gb = f32x4{__uint_as_float(gb0.x), __uint_as_float(gb0.y), __uint_as_float(gb1.x), __uint_as_float(gb1.y)};
                asm volatile("" : "+v"(x1), "+v"(gb));
            }
            f32x2 ff[6][4];
#pragma unroll
            for (int j = 0; j < 6; ++j) { ff[j][0] = up2(fw[j].x); ff[j][1] = up2(fw[j].y); ff[j][2] = up2(fw[j].z); ff[j][3] = up2(fw[j].w); PIN1(ff[j][0]); PIN1(ff[j][1]); PIN1(ff[j][2]); PIN1(ff[j][3]); }
            STAMP(11);
            u32x4 hg[3];
            for (int it = 0; it < SPIN; ++it) { CFENCE();
#pragma unroll
                for (int i = 0; i < 3; ++i) hg[i] = ld4(rws, OFF_HID + (u32)(q + 256 * i) * 16, AUX_SC1);
                if (!anyl(tagbad(hg[0], tlo, thi) | tagbad(hg[1], tlo, thi) | tagbad(hg[2], tlo, thi))) break; PSLEEP();
            }
            STAMP(12);
            f32x2 acc[4] = {{0.f, 0.f}, {0.f, 0.f}, {0.f, 0.f}, {0.f, 0.f}};
#pragma unroll
            for (int i = 0; i < 3; ++i) {
                const f32x2 h0 = splat(__uint_as_float(hg[i].x)), h1 = splat(__uint_as_float(hg[i].y));
#pragma unroll
                for (int k = 0; k < 4; ++k) { acc[k] += h0 * ff[2 * i][k]; acc[k] += h1 * ff[2 * i + 1][k]; }
            }
            float a[8] = {acc[0].x, acc[0].y, acc[1].x, acc[1].y, acc[2].x, acc[2].y, acc[3].x, acc[3].y};
            float v = rscat8(a, lane);
            v += dpp<0x4E>(v); v += dpp<0x104>(v);
            const int e = ((lane >> 5) & 1) * 4 + ((lane >> 4) & 1) * 2 + ((lane >> 3) & 1);
            LAS float* r2l = (LAS float*)(L + L_R2);
            if (!(lane & 6)) r2l[w * 16 + chunk * 8 + e] = v;
            BAR();
            if (t < 4) {
                f32x4 p[8];
#pragma unroll
                for (int k = 0; k < 8; ++k) p[k] = *(LAS f32x4*)(r2l + k * 16 + t * 4);
                const f32x4 s = ((p[0] + p[1]) + (p[2] + p[3])) + ((p[4] + p[5]) + (p[6] + p[7]));
                const f32x4 o = x1 + gb * s;
                *(u32x2*)(out + 16 * s2 + 4 * t) = u32x2{cvtpk(o.x, o.y), cvtpk(o.z, o.w)};
            }
            if (xi == 48 && t == 0) __hip_atomic_store((GAS u64*)(ws + OFF_EPOCH), epoch + 1ull, __ATOMIC_RELAXED, __HIP_MEMORY_SCOPE_AGENT);

        }
    } else {
        const int yi = bid - 128;
        const bool isM = yi < 80;
        const unsigned char* src = (const unsigned char*)ckv + (size_t)yi * 64 * 1536;
        u32 koff[12];
#define KOFF() _Pragma("unroll") for (int j = 0; j < 12; ++j) { const int idx = j * 64 + lane, row = w * 8 + idx / 96, p = idx % 96; const int ch = (p & ~15) | ((p ^ row) & 15); koff[j] = (u32)(row * 1536 + ch * 16); PIN1(koff[j]); }
        if (isM) {
            u32x4 mw[3]; u16 mc[3]; u32 xr[5], xm = 0;
            const int m_rg = yi & 7, m_s = yi >> 3;
            const rsrc_t rmod = mkrs(wmod, DM * 3840 * 2);
            const int row0 = m_rg * 80 + w * 10 + (lane >> 4);
            const u32 off = (u32)(row0 * 3840 + ((lane >> 3) & 1) * 640 + m_s * 64 + (lane & 7) * 8) * 2;
            if (w == 0) {
#pragma unroll
                for (int i = 0; i < 5; ++i) xr[i] = ((const u32*)x)[lane + 64 * i];
                xm = ((const u32*)x)[32 * m_s + (lane & 31)];
            }
            SB();
            mw[0] = ld4(rmod, off, AUX_NT); mw[1] = ld4(rmod, off + 4 * 7680, AUX_NT); mw[2] = ld4(rmod, lane < 32 ? off + 8 * 7680 : 0x80000000u, AUX_NT);
            mc[0] = cc[row0]; mc[1] = cc[row0 + 4]; mc[2] = cc[row0 + (lane < 32 ? 8 : 0)];
            SB();
            KOFF()
            wait_until(tstart, D_CKV);
            u32x4 k1[CKV1 > 0 ? CKV1 : 1];
#pragma unroll
            for (int j = 0; j < CKV1; ++j) k1[j] = __builtin_nontemporal_load((const GAS u32x4*)(src + koff[j]));
            SB();
            f32x2 lnx = {0.f, 0.f};
            if (w == 0) {
                float xv[10];
#pragma unroll
                for (int i = 0; i < 5; ++i) { xv[2 * i] = bflo(xr[i]); xv[2 * i + 1] = bfhi(xr[i]); }
                float mean, rstd; ln_stats(xv, mean, rstd);
                lnx = f32x2{(bflo(xm) - mean) * rstd, (bfhi(xm) - mean) * rstd};
            }
            float a[8], f[8];
            const float s0 = silu(bf1(mc[0])), s1 = silu(bf1(mc[1])), s2 = silu(bf1(mc[2]));
            unp8(mw[0], f);
#pragma unroll
            for (int i = 0; i < 8; ++i) a[i] = s0 * f[i];
            unp8(mw[1], f);
#pragma unroll
            for (int i = 0; i < 8; ++i) a[i] += s1 * f[i];
            unp8(mw[2], f);
#pragma unroll
            for (int i = 0; i < 8; ++i) a[i] += s2 * f[i];
            const float r0 = rs32(a[0], a[4]), r1 = rs32(a[1], a[5]), r2 = rs32(a[2], a[6]), r3 = rs32(a[3], a[7]);
            const f32x2 sv = {rs16(r0, r2), rs16(r1, r3)};
            LAS float* red = (LAS float*)(L + L_RM);
            *(LAS f32x2*)(red + w * 128 + ((lane >> 3) & 1) * 64 + (lane & 7) * 8 + 4 * (lane >> 5) + 2 * ((lane >> 4) & 1)) = sv;
            BAR();
            float hx = 0.f, hy = 0.f;
            u32 poff = 0x80000000u;
            if (w == 0) {
                f32x2 p[8];
#pragma unroll
                for (int k = 0; k < 8; ++k) p[k] = *(LAS f32x2*)(red + k * 128 + 2 * lane);
                const f32x2 s = ((p[0] + p[1]) + (p[2] + p[3])) + ((p[4] + p[5]) + (p[6] + p[7]));
                auto rx = __builtin_amdgcn_permlane32_swap(__float_as_uint(s.x), __float_as_uint(s.x), false, false);
                auto ry = __builtin_amdgcn_permlane32_swap(__float_as_uint(s.y), __float_as_uint(s.y), false, false);
                hx = lnx.x * __uint_as_float(rx[1]) + __uint_as_float(rx[0]); hy = lnx.y * __uint_as_float(ry[1]) + __uint_as_float(ry[0]);
                if (m_rg == 0) { hx += lnx.x; hy += lnx.y; }
                const int g = 32 * m_s + lane, gm = g / 80, gr = g - gm * 80, gw_ = gr / 10;
                if (lane < 32) poff = OFF_E1 + (u32)(((m_rg * 4 + gm) * 8 + gw_) * 16 + (gr - gw_ * 10)) * 16;
            }
            st4(rws, poff, u32x4{__float_as_uint(hx), __float_as_uint(hy), tlo, thi});
            SB();
#pragma unroll
            for (int j = 0; j < CKV1; ++j) *(LAS u32x4*)(L + L_KV + (w * 12 + j) * 1024 + lane * 16) = k1[j];
            BAR();
        } else {
            KOFF()
            wait_until(tstart, D_CKV);
        }
        STAMP(1);
        if (!isM) {
#pragma unroll
            for (int j = 0; j < CKV1; ++j)
                __builtin_amdgcn_global_load_lds((const GAS u32*)(src + koff[j]), (LAS u32*)(L + L_KV + (w * 12 + j) * 1024), 16, 0, AUX_NT);
            SB();
        }
        const u32 opz = OFF_O + (u32)yi * 49152 + t * 16;
#pragma unroll
        for (int j = CKV1; j < 12; ++j)
            __builtin_amdgcn_global_load_lds((const GAS u32*)(src + koff[j]), (LAS u32*)(L + L_KV + (w * 12 + j) * 1024), 16, 0, AUX_NT);
        SB();
#pragma unroll
        for (int j = 0; j < 6; ++j) st4(rws, opz + j * 8192, PZ);
        if (w == 1 && lane < 16 && yi < 120) st4(rws, OFF_E6 + (u32)((yi % 12) * 640 + (yi / 12) * 64) * 4 + lane * 16, PZ);
        SB();
        STAMP(2);
        {
            u32x4 f = {0u, 0u, tlo, thi};
            for (int it = 0; it < SPIN; ++it) { CFENCE();
                if (lane < 16) f = ld4(rws, OFF_PF + (u32)(w * 16 + lane) * 16, AUX_SC1);
                if (!anyl(tagbad(f, tlo, thi))) break; PSLEEP();
            }
        }
        DRAIN();
        BAR();
        STAMP(3);
        const int kq = w >> 1, ht = w & 1;
        u32x4 va[6][2];
        {
            const int r = lane & 15;
            const int keyr = 8 * (r >> 3) + 2 * (r & 3) + ((r >> 2) & 1);
            u32x4 af[6][4];
#pragma unroll
            for (int ks = 0; ks < 6; ++ks) {
                const int ch = kq * 24 + ks * 4 + (lane >> 4);
                const int p = (ch & ~15) | ((ch ^ keyr) & 15);
#pragma unroll
                for (int kt = 0; kt < 4; ++kt) { af[ks][kt] = *(LAS u32x4*)(L + L_KV + (kt * 16 + keyr) * 1536 + p * 16); }
            }
            {
                const int g = lane >> 4, e = (lane >> 2) & 3, pp = lane & 3;
#pragma unroll
                for (int q = 0; q < 6; ++q) {
                    const int ch = 12 * w + 3 * pp + (q >> 1), hf = (q ^ (pp >> 1)) & 1;
#pragma unroll
                    for (int k2 = 0; k2 < 2; ++k2) {
                        const int ka = k2 * 32 + 8 * g + 2 * e, kb = ka + 1;
                        const int pa = (ch & ~15) | ((ch ^ ka) & 15), pbx = (ch & ~15) | ((ch ^ kb) & 15);
                        const s16x4 t0 = __builtin_amdgcn_ds_read_tr16_b64_v4i16((LAS s16x4*)(L + L_KV + ka * 1536 + pa * 16 + hf * 8));
                        const s16x4 t1 = __builtin_amdgcn_ds_read_tr16_b64_v4i16((LAS s16x4*)(L + L_KV + kb * 1536 + pbx * 16 + hf * 8));
                        const u32x2 a0 = __builtin_bit_cast(u32x2, t0), a1 = __builtin_bit_cast(u32x2, t1);
                        va[q][k2] = u32x4{a0.x, a0.y, a1.x, a1.y};
                    }
                }
            }
#pragma unroll
            for (int ks = 0; ks < 6; ++ks)
#pragma unroll
                for (int kt = 0; kt < 4; ++kt) PIN1(af[ks][kt]);
#pragma unroll
            for (int q = 0; q < 6; ++q) { PIN1(va[q][0]); PIN1(va[q][1]); }
            STAMP(4);
            u32x4 qf[12];
            const u32 qoff = OFF_E3 + (u32)(kq * 12 * 32 + ht * 16 + (lane & 15)) * 64 + (lane >> 4) * 16;
            for (int it = 0; it < SPIN; ++it) { CFENCE();
                qf[0] = ld4(rws, qoff, AUX_SC1);
                if (!anyl(pois4(qf[0]))) break; PSLEEP();
            }
            STAMP(5);
            f32x4 acc[4];
            for (int it = 0; it < SPIN; ++it) { CFENCE();
#pragma unroll
                for (int j = 1; j < 12; ++j) qf[j] = ld4(rws, qoff + j * 2048, AUX_SC1);
                SB();
#pragma unroll
                for (int kt = 0; kt < 4; ++kt) acc[kt] = f32x4{0.f, 0.f, 0.f, 0.f};
                u32 pm = 0;
#pragma unroll
                for (int j = 0; j < 12; ++j) {
#pragma unroll
                    for (int kt = 0; kt < 4; ++kt) acc[kt] = mfma(af[j >> 1][kt], qf[j], acc[kt]);
                    pm = max(pm, max(max(qf[j].x, qf[j].y), max(qf[j].z, qf[j].w)));
                }
                if (!anyl(pm == POISON)) break; PSLEEP();
            }
            STAMP(6);
            LAS float* sp = (LAS float*)(L + L_SP);
#pragma unroll
            for (int kt = 0; kt < 4; ++kt) *(LAS f32x4*)(sp + (kq * 32 + ht * 16 + (lane & 15)) * 68 + kt * 16 + 4 * (lane >> 4)) = acc[kt];
        }
        BAR();
        float mlm, mll;
        {
            LAS float* sp = (LAS float*)(L + L_SP);
            const int hd = t >> 4, qd = t & 15;
            f32x4 s = *(LAS f32x4*)(sp + hd * 68 + qd * 4);
#pragma unroll
            for (int k = 1; k < 4; ++k) s += *(LAS f32x4*)(sp + (k * 32 + hd) * 68 + qd * 4);
            const float m = row_max(fmaxf(fmaxf(s.x, s.y), fmaxf(s.z, s.w)));
            const float p0 = __builtin_amdgcn_exp2f(s.x - m), p1 = __builtin_amdgcn_exp2f(s.y - m), p2 = __builtin_amdgcn_exp2f(s.z - m), p3 = __builtin_amdgcn_exp2f(s.w - m);
            const u32 b01 = cvtpk(p0, p1), b23 = cvtpk(p2, p3);
            *(LAS u32x2*)(L + L_P + hd * 144 + qd * 8) = u32x2{b01, b23};
            const float l = row_sum((bflo(b01) + bfhi(b01)) + (bflo(b23) + bfhi(b23)));
            mlm = m; mll = l;
            if (qd == 0) st4(rws, OFF_ML + (u32)(hd * 128 + yi) * 16, u32x4{__float_as_uint(m), __float_as_uint(l), tlo, thi});
        }
        BAR();
        STAMP(7);
        {
            u32x4 pb[2][2];
#pragma unroll
            for (int hh = 0; hh < 2; ++hh)
#pragma unroll
                for (int k2 = 0; k2 < 2; ++k2) pb[hh][k2] = *(LAS u32x4*)(L + L_P + (hh * 16 + (lane & 15)) * 144 + k2 * 64 + (lane >> 4) * 16);
            LAS unsigned char* sw = L + L_KV + (lane & 15) * OSTR + 192 * w + 48 * (lane >> 4);
#pragma unroll
            for (int hh = 0; hh < 2; ++hh) {
#pragma unroll
                for (int j = 0; j < 3; ++j) {
                    f32x4 a0 = {0.f, 0.f, 0.f, 0.f}, a1 = {0.f, 0.f, 0.f, 0.f};
                    a0 = mfma(va[2 * j][0], pb[hh][0], a0); a1 = mfma(va[2 * j + 1][0], pb[hh][0], a1);
                    a0 = mfma(va[2 * j][1], pb[hh][1], a0); a1 = mfma(va[2 * j + 1][1], pb[hh][1], a1);
                    *(LAS u32x4*)(sw + hh * 16 * OSTR + j * 16) = u32x4{canon(cvtpk(a0.x, a0.y)), canon(cvtpk(a0.z, a0.w)), canon(cvtpk(a1.x, a1.y)), canon(cvtpk(a1.z, a1.w))};
                }
            }
            BAR();
            STAMP(8);
            u32x4 ov[6];
#pragma unroll
            for (int j = 0; j < 6; ++j) { const int p = j * 512 + t, hd = p / 96; ov[j] = *(LAS u32x4*)(L + L_KV + hd * (OSTR - 1536) + p * 16); }
#pragma unroll
            for (int j = 0; j < 6; ++j) st4(rws, opz + j * 8192, ov[j]);
        }
        BAR();
        asm volatile("" ::"v"(mlm), "v"(mll));
        STAMP(9);
        if (yi < 120) {
            const int wrg = yi % 12, wcs = yi / 12;
            u32x4 ow[8];
            {
                const rsrc_t ro = mkrs(wo, 6144 * DM * 2);
                const u32 base = (u32)((wrg * 512 + w * 64 + (lane >> 3) * 8) * DM + wcs * 64 + (lane & 7) * 8) * 2;
#pragma unroll
                for (int j = 0; j < 8; ++j) ow[j] = ld4(ro, base + j * 1280, AUX_NT);
                SB();
            }
            f32x2 of[8][4];
#pragma unroll
            for (int j = 0; j < 8; ++j) { of[j][0] = up2(ow[j].x); of[j][1] = up2(ow[j].y); of[j][2] = up2(ow[j].z); of[j][3] = up2(ow[j].w); PIN1(of[j][0]); PIN1(of[j][1]); PIN1(of[j][2]); PIN1(of[j][3]); }
            STAMP(10);
            const int cc8 = lane & 7;
            const u32 goff = OFF_E5 + (u32)((cc8 < 4 ? 0 : 2) * 3072 + wrg * 256 + w * 32 + (lane >> 3) * 4 + (cc8 < 4 ? cc8 : 7 - cc8)) * 16;
            u32x4 g0, g1;
            for (int it = 0; it < SPIN; ++it) { CFENCE();
                g0 = ld4(rws, goff, AUX_SC1); g1 = ld4(rws, goff + 3072 * 16, AUX_SC1);
                if (!anyl(tagbad(g0, tlo, thi) | tagbad(g1, tlo, thi))) break; PSLEEP();
            }
            STAMP(11);
            float vx = __uint_as_float(g0.x) + __uint_as_float(g1.x), vy = __uint_as_float(g0.y) + __uint_as_float(g1.y);
            vx += dpp<0x141>(vx); vy += dpp<0x141>(vy);
            vx = dppb<0x1B, 0xA>(vx, vx); vy = dppb<0x1B, 0xA>(vy, vy);
            f32x2 a2[4] = {{0.f, 0.f}, {0.f, 0.f}, {0.f, 0.f}, {0.f, 0.f}};
#define WSTEP(k) { const f32x2 hx = splat(dpp<k * 0x55>(vx)), hy = splat(dpp<k * 0x55>(vy)); _Pragma("unroll") for (int i = 0; i < 4; ++i) { a2[i] += hx * of[2 * k][i]; a2[i] += hy * of[2 * k + 1][i]; } }
            WSTEP(0) WSTEP(1) WSTEP(2) WSTEP(3)
#undef WSTEP
            float a[8] = {a2[0].x, a2[0].y, a2[1].x, a2[1].y, a2[2].x, a2[2].y, a2[3].x, a2[3].y};
            const float v = rscat8(a, lane);
            const int e = ((lane >> 5) & 1) * 4 + ((lane >> 4) & 1) * 2 + ((lane >> 3) & 1);
            LAS float* red = (LAS float*)(L + L_RW);
            red[w * 64 + (lane & 7) * 8 + e] = v;
            BAR();
            if (w == 0 && lane < 16) {
                f32x4 p[8];
#pragma unroll
                for (int k = 0; k < 8; ++k) p[k] = *(LAS f32x4*)(red + k * 64 + lane * 4);
                const f32x4 s4 = ((p[0] + p[1]) + (p[2] + p[3])) + ((p[4] + p[5]) + (p[6] + p[7]));
                st4(rws, OFF_E6 + (u32)(wrg * 640 + wcs * 64) * 4 + lane * 16, u32x4{canonf(s4.x), canonf(s4.y), canonf(s4.z), canonf(s4.w)});
            }
            STAMP(12);
        }
    }
}

extern "C" void kernel_launch(void* const* d_in, const int* in_sizes, int n_in, void* d_out, int out_size, void* d_ws, size_t ws_size, hipStream_t stream) {
    (void)in_sizes; (void)n_in; (void)out_size;
    const size_t wsoff = (ws_size / 2) & ~(size_t)4095;
    hipFuncSetAttribute((const void*)adaln_mla_mlp_kernel, hipFuncAttributeMaxDynamicSharedMemorySize, LDS_BYTES);
    adaln_mla_mlp_kernel<<<NWG, NT, LDS_BYTES, stream>>>(
        (const u16*)d_in[0], (const u16*)d_in[1], (const u16*)d_in[2], (const u16*)d_in[3], (const u16*)d_in[4], (const u16*)d_in[5],
        (const u16*)d_in[7], (const u16*)d_in[8], (const u16*)d_in[9], (const u16*)d_in[10], (u16*)d_out, (unsigned char*)d_ws + wsoff);
}
```

```cpp
#include <hip/hip_runtime.h>
#include <stdint.h>

typedef unsigned u32;
typedef unsigned long long u64;
typedef unsigned short u16;
typedef u32 u32x2 __attribute__((ext_vector_type(2)));
typedef u32 u32x3 __attribute__((ext_vector_type(3)));
typedef u32 u32x4 __attribute__((ext_vector_type(4)));
typedef float f32x2 __attribute__((ext_vector_type(2)));
typedef float f32x4 __attribute__((ext_vector_type(4)));
typedef short bf16x8 __attribute__((ext_vector_type(8)));
typedef short s16x4 __attribute__((ext_vector_type(4)));
typedef __amdgpu_buffer_rsrc_t rsrc_t;
#define LAS __attribute__((address_space(3)))
#define GAS __attribute__((address_space(1)))

constexpr int NH = 32, SK = 8192, HD = 192, DM = 640, DC = 768, DFF = 1536;
constexpr int NWG = 256, NT = 512;

#pragma clang diagnostic ignored "-Winline-asm"
#define UK1 4
#define D_WQ 0
#define D_CKV 350
#define D_CKV2 350
#define CKV1 0
#define D_PO 560
#define D_UV 0
#define XW 4
#define YW 3
#define D_E1 260
#define D_E2 40
#define JPF 7
#define ML_SLEEP 4
#define WO1 0
#define FIX1 1
#define FIX2 1
#define GPOS 0
constexpr u32 OFF_EPOCH = 0;
constexpr u32 OFF_PF = 256;
constexpr u32 OFF_PF2 = OFF_PF + 128 * 16;
constexpr u32 OFF_E1 = OFF_PF2 + 128 * 16;
constexpr u32 OFF_E2 = OFF_E1 + 8 * 4 * 8 * 256;
constexpr u32 OFF_E3 = OFF_E2 + 4 * 32 * 96 * 16;
constexpr u32 OFF_ML = OFF_E3 + 32 * 4 * 768;
constexpr u32 OFF_E5 = OFF_ML + 32 * 128 * 16;
constexpr u32 OFF_E6 = OFF_E5 + 4 * 3072 * 16;
constexpr u32 OFF_G = OFF_E6 + 12 * 640 * 4;
constexpr u32 OFF_HID = OFF_G + 1280 * 16;
constexpr u32 OFF_O = OFF_HID + 768 * 16;
constexpr u32 WS_BYTES = OFF_O + 32 * 4 * 128 * 384;
constexpr u32 OFF_DIAG = WS_BYTES;
static_assert(OFF_O % 128 == 0 && OFF_E3 % 128 == 0 && OFF_E5 % 128 == 0 && OFF_E2 % 256 == 0 && OFF_E6 % 128 == 0 && OFF_E1 % 256 == 0, "alignment");

constexpr int L_KV = 0;
constexpr int OSTR = 1552;
constexpr int L_SP = 98304;
constexpr int L_P = L_SP + 4 * 32 * 68 * 4;
constexpr int L_RM = L_P + 32 * 144;
constexpr int L_RW = L_RM + 8 * 128 * 4;
constexpr int L_ML = L_RW + 8 * 64 * 4;
constexpr int LDS_BYTES = L_ML + 256;
constexpr int L_RQ = 0;
constexpr int L_Q = L_RQ + 6144;
constexpr int L_RA = L_Q + 768;
constexpr int L_WL = L_RA + 768;
constexpr int L_RV1 = L_WL + 1024;
constexpr int L_RV2 = L_RV1 + 6144;
constexpr int L_RG = L_RV2 + 6144;
constexpr int L_GS = L_RG + 1024;
constexpr int L_RP = L_GS + 7680;
constexpr int L_RF = L_RP + 2048;
constexpr int L_ST = L_RF + 1024;
constexpr int L_R2 = L_ST + 64;
constexpr int L_R6 = L_R2 + 512;
constexpr int SPIN = 1 << 16;
constexpr u32 POISON = 0xFFFFFFFFu;
constexpr u32 OOB = 0x80000000u;
constexpr int AUX_SC1 = 16, AUX_NT = 2;
constexpr u64 TAGMUL = 0x9E3779B97F4A7C15ull;

__device__ __forceinline__ float bflo(u32 v) { return __uint_as_float(v << 16); }
__device__ __forceinline__ float bfhi(u32 v) { return __uint_as_float(v & 0xFFFF0000u); }
__device__ __forceinline__ float bf1(u16 v) { return __uint_as_float((u32)v << 16); }
typedef __bf16 bf16x2_t __attribute__((ext_vector_type(2)));
__device__ __forceinline__ u32 cvtpk(float lo, float hi) { f32x2 v = {lo, hi}; return __builtin_bit_cast(u32, __builtin_convertvector(v, bf16x2_t)); }
__device__ __forceinline__ u32 canon(u32 v) { return v < 0xFFFFFFFEu ? v : 0xFFFFFFFEu; }
__device__ __forceinline__ u32 canonf(float v) { return canon(__float_as_uint(v)); }
__device__ __forceinline__ rsrc_t mkrs(const void* p, u32 bytes) { return __builtin_amdgcn_make_buffer_rsrc((void*)p, 0, bytes, 0x00020000); }
__device__ __forceinline__ u32x4 ld4(rsrc_t r, u32 off, int aux) { return aux == AUX_SC1 ? __builtin_amdgcn_raw_buffer_load_b128(r, off, 0, AUX_SC1) : __builtin_amdgcn_raw_buffer_load_b128(r, off, 0, AUX_NT); }
__device__ __forceinline__ u32x3 ld3(rsrc_t r, u32 off, int aux) { return aux == AUX_SC1 ? __builtin_amdgcn_raw_buffer_load_b96(r, off, 0, AUX_SC1) : __builtin_amdgcn_raw_buffer_load_b96(r, off, 0, AUX_NT); }
__device__ __forceinline__ void st4(rsrc_t r, u32 off, u32x4 v) { __builtin_amdgcn_raw_buffer_store_b128(v, r, off, 0, AUX_SC1); }
__device__ __forceinline__ void st2(rsrc_t r, u32 off, u32x2 v) { __builtin_amdgcn_raw_buffer_store_b64(v, r, off, 0, AUX_SC1); }
#define CFENCE() asm volatile("" ::: "memory")
#define PSL 0
#define PSLEEP() do { if (PSL > 0) __builtin_amdgcn_s_sleep(PSL); } while (0)
#define SB() __builtin_amdgcn_sched_barrier(0)
#define BAR() asm volatile("s_waitcnt lgkmcnt(0)\n\ts_barrier" ::: "memory")
#define LGKM0() asm volatile("s_waitcnt lgkmcnt(0)" ::: "memory")
#define DRAIN() asm volatile("s_waitcnt vmcnt(0)" ::: "memory")

template <int CTRL> __device__ __forceinline__ float dpp(float x) { return __int_as_float(__builtin_amdgcn_update_dpp(0, __float_as_int(x), CTRL, 0xf, 0xf, true)); }
__device__ __forceinline__ float rs32(float a, float b) { auto r = __builtin_amdgcn_permlane32_swap(__float_as_uint(a), __float_as_uint(b), false, false); return __uint_as_float(r[0]) + __uint_as_float(r[1]); }
__device__ __forceinline__ float rs16(float a, float b) { auto r = __builtin_amdgcn_permlane16_swap(__float_as_uint(a), __float_as_uint(b), false, false); return __uint_as_float(r[0]) + __uint_as_float(r[1]); }
__device__ __forceinline__ float row_sum(float x) { x += dpp<0x128>(x); x += dpp<0x124>(x); x += dpp<0x122>(x); x += dpp<0x121>(x); return x; }
__device__ __forceinline__ float row_max(float x) { x = fmaxf(x, dpp<0x128>(x)); x = fmaxf(x, dpp<0x124>(x)); x = fmaxf(x, dpp<0x122>(x)); x = fmaxf(x, dpp<0x121>(x)); return x; }
__device__ __forceinline__ float wave_sum(float x) { x = row_sum(x); x = rs16(x, x); x = rs32(x, x); return x; }
__device__ __forceinline__ float wave_max(float x) {
    x = row_max(x);
    { auto r = __builtin_amdgcn_permlane16_swap(__float_as_uint(x), __float_as_uint(x), false, false); x = fmaxf(__uint_as_float(r[0]), __uint_as_float(r[1])); }
    { auto r = __builtin_amdgcn_permlane32_swap(__float_as_uint(x), __float_as_uint(x), false, false); x = fmaxf(__uint_as_float(r[0]), __uint_as_float(r[1])); }
    return x;
}
__device__ __forceinline__ float rscat8(const float (&a)[8], int lane) {
    float r0 = rs32(a[0], a[4]), r1 = rs32(a[1], a[5]), r2 = rs32(a[2], a[6]), r3 = rs32(a[3], a[7]);
    float s0 = rs16(r0, r2), s1 = rs16(r1, r3);
    float t0 = s0 + dpp<0x128>(s0), t1 = s1 + dpp<0x128>(s1);
    return (lane & 8) ? t1 : t0;
}
__device__ __forceinline__ bool pois4(u32x4 v) { u32 m = max(max(v.x, v.y), max(v.z, v.w)); return m == POISON; }
__device__ __forceinline__ bool pois3(u32x3 v) { u32 m = max(max(v.x, v.y), v.z); return m == POISON; }
__device__ __forceinline__ bool anyl(bool b) { return __builtin_amdgcn_ballot_w64(b) != 0ull; }
__device__ __forceinline__ f32x4 mfma(u32x4 a, u32x4 b, f32x4 c) { return __builtin_amdgcn_mfma_f32_16x16x32_bf16(__builtin_bit_cast(bf16x8, a), __builtin_bit_cast(bf16x8, b), c, 0, 0, 0); }
__device__ __forceinline__ float silu(float v) { return v / (1.f + __expf(-v)); }
__device__ __forceinline__ void unp8(u32x4 v, float (&f)[8]) { f[0] = bflo(v.x); f[1] = bfhi(v.x); f[2] = bflo(v.y); f[3] = bfhi(v.y); f[4] = bflo(v.z); f[5] = bfhi(v.z); f[6] = bflo(v.w); f[7] = bfhi(v.w); }
__device__ __forceinline__ void unp6(u32x3 v, float (&f)[6]) { f[0] = bflo(v.x); f[1] = bfhi(v.x); f[2] = bflo(v.y); f[3] = bfhi(v.y); f[4] = bflo(v.z); f[5] = bfhi(v.z); }

__device__ __forceinline__ void wait_until(u64 t0, int ticks) {
    if (ticks > 0) { for (int i = 0; i < 4096; ++i) { if (__builtin_amdgcn_s_memrealtime() - t0 >= (u64)ticks) break; __builtin_amdgcn_s_sleep(1); } }
}
#define STAMP(i) do { } while (0)

__device__ __forceinline__ void ln_stats(const float (&v)[10], float& mean, float& rstd) {
    float s = 0.f;
#pragma unroll
    for (int i = 0; i < 10; ++i) s += v[i];
    mean = wave_sum(s) * (1.f / 640.f);
    float q = 0.f;
#pragma unroll
    for (int i = 0; i < 10; ++i) { float d = v[i] - mean; q += d * d; }
    float var = wave_sum(q) * (1.f / 640.f);
    rstd = 1.f / sqrtf(var + 1e-5f);
}

#define CAS __attribute__((address_space(4)))
#define PIN1(a) asm volatile("" : "+v"(a))
template <int CTRL, int BANK> __device__ __forceinline__ float dppb(float old, float x) { return __int_as_float(__builtin_amdgcn_update_dpp(__float_as_int(old), __float_as_int(x), CTRL, 0xf, BANK, false)); }
__device__ __forceinline__ u32x2 ld2(rsrc_t r, u32 off) { return __builtin_amdgcn_raw_buffer_load_b64(r, off, 0, AUX_SC1); }
__device__ __forceinline__ u32 ld1(rsrc_t r, u32 off) { return __builtin_amdgcn_raw_buffer_load_b32(r, off, 0, AUX_SC1); }
__device__ __forceinline__ bool tagbad(u32x4 g, u32 tlo, u32 thi) { return (g.z != tlo) | (g.w != thi); }
__device__ __forceinline__ f32x2 up2(u32 v) { return f32x2{bflo(v), bfhi(v)}; }
__device__ __forceinline__ f32x2 splat(float v) { return f32x2{v, v}; }

__device__ __forceinline__ void dma16nt(const void* base, u32 voff, u32 lds) {
    asm volatile("s_mov_b32 m0, %2\n\ts_nop 0\n\tglobal_load_lds_dwordx4 %0, %1 nt" ::"v"(voff), "s"(base), "s"(lds) : "memory", "m0");
}
__device__ __forceinline__ u32 ldh(rsrc_t r, u32 off) { return __builtin_amdgcn_raw_buffer_load_b16(r, off, 0, 0); }
template <int N> __device__ __forceinline__ void vmwait() { if (N < 63) asm volatile("s_waitcnt vmcnt(%0)" ::"n"(N) : "memory"); }
__device__ __forceinline__ u32x4 ald4(u32x4 rs, u32 off) { u32x4 v; asm volatile("buffer_load_dwordx4 %0, %1, %2, 0 offen sc1" : "=v"(v) : "v"(off), "s"(rs) : "memory"); return v; }
template <int N> __device__ __forceinline__ void await(u32x4& v) { asm volatile("s_waitcnt vmcnt(%1)" : "+v"(v) : "n"(N) : "memory"); }
#define GETTAG() \
    const u64 epoch = ((u64)(u32)__builtin_amdgcn_readfirstlane(epv.y) << 32) | (u64)(u32)__builtin_amdgcn_readfirstlane(epv.x); \
    const u64 tag = (epoch + 1ull) * TAGMUL; \
    const u32 tlo = (u32)tag, thi = (u32)(tag >> 32);

__global__ __launch_bounds__(NT, 2) void adaln_mla_mlp_kernel(
    const u16* __restrict__ x, const u16* __restrict__ ckv, const u16* __restrict__ wq, const u16* __restrict__ wuk,
    const u16* __restrict__ wuv, const u16* __restrict__ wo, const u16* __restrict__ w1, const u16* __restrict__ w2,
    const u16* __restrict__ cc, const u16* __restrict__ wmod, u16* __restrict__ out, unsigned char* __restrict__ ws) {
    extern __shared__ __attribute__((aligned(16))) unsigned char smem[];
    LAS unsigned char* L = (LAS unsigned char*)smem;
    STAMP(0);
    const u64 tstart = __builtin_amdgcn_s_memrealtime();
    asm volatile("" ::"s"(x), "s"(ckv), "s"(wq), "s"(wuk), "s"(wuv), "s"(wo), "s"(w1), "s"(w2), "s"(cc), "s"(wmod), "s"(out), "s"(ws));
    const int t = threadIdx.x, lane = t & 63, w = __builtin_amdgcn_readfirstlane(t >> 6);
    const int bid = blockIdx.x;
    const rsrc_t rws = mkrs(ws, WS_BYTES);
    const u32x2 epv = ld2(rws, OFF_EPOCH);
    const u32x4 PZ = {POISON, POISON, POISON, POISON};
    const u32 lbase = (u32)(uintptr_t)L;
    const u32x4 rws4 = {(u32)(uintptr_t)ws, (u32)((uintptr_t)ws >> 32), WS_BYTES, 0x00020000u};

    if (bid < 128) {
        const int xi = bid, h = xi & 31, cq = xi >> 5;
        const rsrc_t rmod = mkrs(wmod, DM * 3840 * 2);
        const u32 e3off = OFF_E3 + (u32)((cq * 12 + (lane >> 2)) * 32 + h) * 64 + (lane & 3) * 16;
        st4(rws, (w == 7 && lane < 48) ? e3off : OOB, PZ);
        const rsrc_t rq = mkrs(wq, DM * 6144 * 2);
        const rsrc_t ruk = mkrs(wuk, DC * 6144 * 2);
        const u32 wbase = (u32)(((cq * 192 + w * 24 + (lane >> 5)) * 32 + h) * 192 + 6 * (lane & 31)) * 2;
        u32x3 qw[10], uw[12];
        wait_until(tstart, D_WQ);
        {
            const u32 base = (u32)(((cq * 160 + w * 20 + (lane >> 5)) * 32 + h) * 192 + 6 * (lane & 31)) * 2;
#pragma unroll
            for (int j = 0; j < 10; ++j) { qw[j] = ld3(rq, base + j * 2 * 12288, AUX_NT); if (j >= XW) { SB(); vmwait<XW>(); SB(); } }
            SB();
#pragma unroll
            for (int j = 0; j < UK1; ++j) { uw[j] = ld3(ruk, wbase + j * 2 * 12288, AUX_NT); if (10 + j >= XW) { SB(); vmwait<XW>(); SB(); } }
            SB();
        }
        GETTAG()
        f32x2 qf2[10][3];
#pragma unroll
        for (int j = 0; j < 10; ++j) { qf2[j][0] = up2(qw[j].x); qf2[j][1] = up2(qw[j].y); qf2[j][2] = up2(qw[j].z); PIN1(qf2[j][0]); PIN1(qf2[j][1]); PIN1(qf2[j][2]); }
        STAMP(1);
        {
            const bool act = (lane & 15) < 10;
            const u32 goff = act ? OFF_E1 + (u32)(((lane >> 4) * 4 + cq) * 8 + w) * 256 + (lane & 15) * 16 : OOB;
            wait_until(tstart, D_E1);
            u32x4 g0 = ld4(rws, goff, AUX_SC1), g1 = ld4(rws, goff + 32768, AUX_SC1);
            SB();
            asm volatile("s_barrier" ::: "memory");
            SB();
#pragma unroll
            for (int j = UK1; j < 12; ++j) uw[j] = ld3(ruk, wbase + j * 2 * 12288, AUX_NT);
            SB();
            if (anyl(act & (tagbad(g0, tlo, thi) | tagbad(g1, tlo, thi)))) {
                for (int it = 0; it < SPIN; ++it) { CFENCE();
                    g0 = ld4(rws, goff, AUX_SC1); g1 = ld4(rws, goff + 32768, AUX_SC1);
                    if (!anyl(act & (tagbad(g0, tlo, thi) | tagbad(g1, tlo, thi)))) break;
                }
            }
            STAMP(2);
            st4(rws, t == 448 ? OFF_PF + xi * 16 : OOB, u32x4{0u, 0u, tlo, thi});
            SB();
            float s = rs32(__uint_as_float(g0.x) + __uint_as_float(g1.x), __uint_as_float(g0.y) + __uint_as_float(g1.y));
            s = rs16(s, s);
            f32x2 a[3] = {{0.f, 0.f}, {0.f, 0.f}, {0.f, 0.f}};
#define QSTEP(j) { const f32x2 hv = splat(dpp<0x150 + j>(s)); a[0] += hv * qf2[j][0]; a[1] += hv * qf2[j][1]; a[2] += hv * qf2[j][2]; }
            QSTEP(0) QSTEP(1) QSTEP(2) QSTEP(3) QSTEP(4) QSTEP(5) QSTEP(6) QSTEP(7) QSTEP(8) QSTEP(9)
#undef QSTEP
            LAS float* red = (LAS float*)(L + L_RQ);
            {
                LAS float* dst = red + w * 192 + 6 * (lane & 31) + 3 * (lane >> 5);
                dst[0] = rs32(a[0].x, a[1].y); dst[1] = rs32(a[0].y, a[2].x); dst[2] = rs32(a[1].x, a[2].y);
            }
            BAR();
            f32x2 v = {0.f, 0.f};
            if (w < 2) {
                f32x2 p[8];
#pragma unroll
                for (int k = 0; k < 8; ++k) p[k] = *(LAS f32x2*)(red + k * 192 + t * 2);
                v = ((p[0] + p[1]) + (p[2] + p[3])) + ((p[4] + p[5]) + (p[6] + p[7]));
            }
            st4(rws, t < 96 ? OFF_E2 + (u32)((cq * 32 + h) * 96 + t) * 16 : OOB, u32x4{__float_as_uint(v.x), __float_as_uint(v.y), tlo, thi});
        }
        STAMP(3);
        f32x2 wf[12][3];
        {
            LAS float* qs = (LAS float*)(L + L_Q);
            const u32 goff = w < 6 ? OFF_E2 + (u32)(((lane >> 4) * 32 + h) * 96 + 16 * w + (lane & 15)) * 16 : OOB;
            { const u64 tn = __builtin_amdgcn_s_memrealtime(); wait_until(tn, D_E2); }
            u32x4 g = ld4(rws, goff, AUX_SC1);
            SB();
#pragma unroll
            for (int j = 0; j < 12; ++j) { wf[j][0] = up2(uw[j].x); wf[j][1] = up2(uw[j].y); wf[j][2] = up2(uw[j].z); PIN1(wf[j][0]); PIN1(wf[j][1]); PIN1(wf[j][2]); }
            if (w < 6) {
                if (anyl(tagbad(g, tlo, thi))) {
                    for (int it = 0; it < SPIN; ++it) { CFENCE();
                        g = ld4(rws, goff, AUX_SC1);
                        if (!anyl(tagbad(g, tlo, thi))) break;
                    }
                }
                float s = rs32(__uint_as_float(g.x), __uint_as_float(g.y));
                s = rs16(s, s);
                if (!(lane & 16)) qs[2 * (16 * w + (lane & 15)) + (lane >> 5)] = s;
            }
            STAMP(5);
            BAR();
            const f32x2 q0 = *(LAS f32x2*)(qs + 6 * (lane & 31)), q1 = *(LAS f32x2*)(qs + 6 * (lane & 31) + 2), q2 = *(LAS f32x2*)(qs + 6 * (lane & 31) + 4);
            float p[12];
#pragma unroll
            for (int j = 0; j < 12; ++j) { const f32x2 d = wf[j][0] * q0 + wf[j][1] * q1 + wf[j][2] * q2; p[j] = d.x + d.y; }
            float r[6], s[3];
#pragma unroll
            for (int j = 0; j < 6; ++j) r[j] = rs16(p[j], p[j + 6]);
#pragma unroll
            for (int j = 0; j < 3; ++j) {
                const float own = (lane & 8) ? r[j + 3] : r[j], oth = (lane & 8) ? r[j] : r[j + 3];
                float v = own + dpp<0x128>(oth);
                v += dpp<0x141>(v); v += dpp<0x1B>(v); v += dpp<0xB1>(v);
                s[j] = v;
            }
            LAS float* red = (LAS float*)(L + L_RA);
            {
                const int j = lane & 7;
                const float v = j == 0 ? s[0] : (j == 1 ? s[1] : s[2]);
                if (j < 3) red[w * 24 + 2 * (j + 3 * ((lane >> 3) & 1) + 6 * ((lane >> 4) & 1)) + (lane >> 5)] = v;
            }
            BAR();
            u32 o[4] = {0u, 0u, 0u, 0u};
            if (w == 0) {
                const float sc = 0.07216878364870322f * 1.4426950408889634f;
                const int c0 = ((lane >> 3) * 32 + (lane & 3) * 8) & 255;
                const f32x4 p0 = *(LAS f32x4*)(red + c0) * sc, p1 = *(LAS f32x4*)(red + c0 + 4) * sc;
                const float v8[8] = {p0.x, p0.y, p0.z, p0.w, p1.x, p1.y, p1.z, p1.w};
#pragma unroll
                for (int i = 0; i < 4; ++i) {
                    const u32 hi = cvtpk(v8[2 * i], v8[2 * i + 1]);
                    const u32 lo = cvtpk(v8[2 * i] - bflo(hi), v8[2 * i + 1] - bfhi(hi));
                    o[i] = canon((lane & 4) ? lo : hi);
                }
            }
            st4(rws, t < 48 ? e3off : OOB, u32x4{o[0], o[1], o[2], o[3]});
        }
        STAMP(6);
        const rsrc_t ruv = mkrs(wuv, DC * 6144 * 2);
        u32x3 vw[12];
        wait_until(tstart, D_PO);
        {
#pragma unroll
            for (int j = 0; j < 6; ++j) { const int idx = j * 512 + t, y = idx / 24, p = idx - y * 24; st4(rws, OFF_O + (u32)y * 49152 + (u32)(h * 4 + cq) * 384 + p * 16, PZ); }
            st4(rws, (w == 1 && lane < 16 && xi < 120) ? OFF_E6 + (u32)((xi % 12) * 640 + (xi / 12) * 64) * 4 + lane * 16 : OOB, PZ);
        }
        SB();
        wait_until(tstart, D_UV);
#pragma unroll
        for (int j = 0; j < 12; ++j) vw[j] = ld3(ruv, wbase + j * 2 * 12288, AUX_NT);
        SB();
        asm volatile("s_waitcnt vmcnt(12)\n\ts_barrier" ::: "memory");
        st4(rws, t == 448 ? OFF_PF2 + xi * 16 : OOB, u32x4{0u, 0u, tlo, thi});
        SB();
        STAMP(4);
        u32x4 gw[5]; u32 cv[5];
        const int rs = lane >> 2, ck = lane & 3;
        const int gi = xi - 48;
        const int g_s = 2 * ((gi >> 4) * 8 + (gi & 7)) + ((gi >> 3) & 1);
#define GLOADS() do { \
        const bool isg = xi >= 48; \
        const rsrc_t rg = mkrs(isg ? wmod : w1, isg ? DM * 3840 * 2 : DM * DFF * 2); \
        const rsrc_t rc = mkrs(isg ? cc : x, DM * 2); \
        const u32 goff0 = isg ? (u32)((t >> 2) * 3840 + 1280 + 32 * g_s + 8 * ck) * 2 : (u32)((w * 80 + rs) * DFF + 32 * xi + 8 * ck) * 2; \
        const u32 gstr = isg ? 128 * 7680 : 16 * DFF * 2; \
        const u32 coff0 = isg ? (u32)(t >> 2) * 2 : (u32)(w * 80 + rs) * 2; \
        const u32 cstr = isg ? 256 : 32; \
        _Pragma("unroll") for (int j = 0; j < 5; ++j) gw[j] = ld4(rg, goff0 + j * gstr, AUX_NT); \
        _Pragma("unroll") for (int j = 0; j < 5; ++j) cv[j] = ldh(rc, coff0 + j * cstr); \
        SB(); \
        } while (0)
#define GCOMP() do { \
        if (xi >= 48) { \
            float a[8]; \
_Pragma("unroll") \
            for (int i = 0; i < 8; ++i) a[i] = 0.f; \
_Pragma("unroll") \
            for (int j = 0; j < 5; ++j) { \
                float f[8]; unp8(gw[j], f); const float s = silu(bflo(cv[j])); \
_Pragma("unroll") \
                for (int i = 0; i < 8; ++i) a[i] += s * f[i]; \
            } \
            float v = rscat8(a, lane); \
            v += dpp<0x104>(v); \
            const int e = ((lane >> 5) & 1) * 4 + ((lane >> 4) & 1) * 2 + ((lane >> 3) & 1); \
            LAS float* redg = (LAS float*)(L + L_RG); \
            if (!(lane & 4)) redg[w * 32 + ck * 8 + e] = v; \
            BAR(); \
            if (w == 0 && lane < 16) { \
                f32x2 s = {0.f, 0.f}; \
_Pragma("unroll") \
                for (int i = 0; i < 8; ++i) s += *(LAS f32x2*)(redg + i * 32 + lane * 2); \
                st4(rws, OFF_G + (g_s * 16 + lane) * 16, u32x4{__float_as_uint(s.x), __float_as_uint(s.y), tlo, thi}); \
            } \
        } \
        } while (0)
        GLOADS();
        GCOMP();
        f32x2 vf[12][3];
#pragma unroll
        for (int j = 0; j < 12; ++j) { vf[j][0] = up2(vw[j].x); vf[j][1] = up2(vw[j].y); vf[j][2] = up2(vw[j].z); PIN1(vf[j][0]); PIN1(vf[j][1]); PIN1(vf[j][2]); }
        STAMP(7);
        {
            const u32 obase = OFF_O + (u32)(w * 16 + (lane >> 5)) * 49152 + (u32)(h * 4 + cq) * 384 + (lane & 31) * 12;
            constexpr u32 OJ = 2 * 49152;
            LAS float* red1 = (LAS float*)(L + L_RV1);
            LAS float* red2 = (LAS float*)(L + L_RV2);
            LAS f32x2* wl = (LAS f32x2*)(L + L_WL);
            if (w == 0) {
                const u32 mlA = OFF_ML + (u32)(h * 128 + lane) * 16;
                u32x4 ma, mb;
                for (int it = 0; it < SPIN; ++it) { CFENCE();
                    ma = ld4(rws, mlA, AUX_SC1); mb = ld4(rws, mlA + 1024, AUX_SC1);
                    if (!anyl(tagbad(ma, tlo, thi) | tagbad(mb, tlo, thi))) break; if (ML_SLEEP > 0) __builtin_amdgcn_s_sleep(ML_SLEEP);
                }
                wl[lane] = f32x2{__uint_as_float(ma.x), __uint_as_float(ma.y)};
                wl[lane + 64] = f32x2{__uint_as_float(mb.x), __uint_as_float(mb.y)};
            }
            BAR();
            STAMP(8);
            u32x3 oa[8];
#pragma unroll
            for (int j = 0; j < 8; ++j) oa[j] = ld3(rws, obase + j * OJ, AUX_SC1);
            SB();
            float wA;
            {
                const f32x2 a = wl[lane], b = wl[lane + 64], own = wl[16 * w + 2 * (lane & 7) + (lane >> 5)];
                const float M = wave_max(fmaxf(a.x, b.x));
                const float Lsum = wave_sum(__builtin_amdgcn_exp2f(a.x - M) * a.y + __builtin_amdgcn_exp2f(b.x - M) * b.y);
                wA = __builtin_amdgcn_exp2f(own.x - M) * __builtin_amdgcn_rcpf(Lsum);
            }
            f32x2 pr[8][3];
            u32 pend = 0;
#define OSTEP(j) { if (anyl(pois3(oa[j]))) pend |= 1u << j; else { const f32x2 ww = splat(dpp<0x150 + j>(wA)); pr[j][0] = up2(oa[j].x) * ww; pr[j][1] = up2(oa[j].y) * ww; pr[j][2] = up2(oa[j].z) * ww; } }
            OSTEP(0) OSTEP(1) OSTEP(2) OSTEP(3) OSTEP(4) OSTEP(5) OSTEP(6) OSTEP(7)
#undef OSTEP
            for (int it = 0; it < SPIN && pend; ++it) { CFENCE();
#pragma unroll
                for (int j = 0; j < 8; ++j) if (pend & (1u << j)) oa[j] = ld3(rws, obase + j * OJ, AUX_SC1);
#define OSTEP(j) if (pend & (1u << j)) { if (!anyl(pois3(oa[j]))) { pend &= ~(1u << j); const f32x2 ww = splat(dpp<0x150 + j>(wA)); pr[j][0] = up2(oa[j].x) * ww; pr[j][1] = up2(oa[j].y) * ww; pr[j][2] = up2(oa[j].z) * ww; } }
                OSTEP(0) OSTEP(1) OSTEP(2) OSTEP(3) OSTEP(4) OSTEP(5) OSTEP(6) OSTEP(7)
#undef OSTEP
            }
            STAMP(9);
            f32x2 a2[3];
#pragma unroll
            for (int k = 0; k < 3; ++k) a2[k] = ((pr[0][k] + pr[1][k]) + (pr[2][k] + pr[3][k])) + ((pr[4][k] + pr[5][k]) + (pr[6][k] + pr[7][k]));
            {
                LAS float* dst = red1 + w * 192 + 6 * (lane & 31) + 3 * (lane >> 5);
                dst[0] = rs32(a2[0].x, a2[1].y); dst[1] = rs32(a2[0].y, a2[2].x); dst[2] = rs32(a2[1].x, a2[2].y);
            }
            BAR();
            float cx = 0.f;
            {
                const int i = lane & 15, c = w * 24 + 2 * (i < 12 ? i : 11) + (lane >> 5), cidx = c ^ ((c % 96) >= 48 ? 4 : 0);
                float p[8];
#pragma unroll
                for (int k = 0; k < 8; ++k) p[k] = red1[k * 192 + cidx];
                cx = ((p[0] + p[1]) + (p[2] + p[3])) + ((p[4] + p[5]) + (p[6] + p[7]));
            }
            f32x2 c2[3] = {{0.f, 0.f}, {0.f, 0.f}, {0.f, 0.f}};
#define VSTEP(j) { const f32x2 cvv = splat(dpp<0x150 + j>(cx)); c2[0] += cvv * vf[j][0]; c2[1] += cvv * vf[j][1]; c2[2] += cvv * vf[j][2]; }
            VSTEP(0) VSTEP(1) VSTEP(2) VSTEP(3) VSTEP(4) VSTEP(5) VSTEP(6) VSTEP(7) VSTEP(8) VSTEP(9) VSTEP(10) VSTEP(11)
#undef VSTEP
            {
                LAS float* dst = red2 + w * 192 + 6 * (lane & 31) + 3 * (lane >> 5);
                dst[0] = rs32(c2[0].x, c2[1].y); dst[1] = rs32(c2[0].y, c2[2].x); dst[2] = rs32(c2[1].x, c2[2].y);
            }
            BAR();
            f32x2 s5 = {0.f, 0.f};
            if (w < 2) {
                f32x2 p[8];
#pragma unroll
                for (int k = 0; k < 8; ++k) p[k] = *(LAS f32x2*)(red2 + k * 192 + t * 2);
                s5 = ((p[0] + p[1]) + (p[2] + p[3])) + ((p[4] + p[5]) + (p[6] + p[7]));
            }
            st4(rws, t < 96 ? OFF_E5 + (u32)(cq * 3072 + h * 96 + t) * 16 : OOB, u32x4{__float_as_uint(s5.x), __float_as_uint(s5.y), tlo, thi});
        }
        BAR();
        STAMP(10);
        if (xi < 48) {
            LAS float* gs = (LAS float*)(L + L_GS);
            {
                u32x4 g0, g1;
                const bool two = t < 448;
                for (int it = 0; it < SPIN; ++it) { CFENCE();
                    g0 = ld4(rws, OFF_G + t * 16, AUX_SC1); bool bad = tagbad(g0, tlo, thi);
                    if (two) { g1 = ld4(rws, OFF_G + (t + 512) * 16, AUX_SC1); bad |= tagbad(g1, tlo, thi); }
                    if (!anyl(bad)) break; PSLEEP();
                }
                *(LAS u32x2*)(gs + 2 * t) = u32x2{g0.x, g0.y};
                if (two) *(LAS u32x2*)(gs + 2 * (t + 512)) = u32x2{g1.x, g1.y};
            }
            BAR();
            f32x2 wA[5][4], u[4], v1[4], v2[4];
            float xv[5], ga[5];
#pragma unroll
            for (int k = 0; k < 4; ++k) { u[k] = f32x2{0.f, 0.f}; v1[k] = f32x2{0.f, 0.f}; v2[k] = f32x2{0.f, 0.f}; }
#pragma unroll
            for (int j = 0; j < 5; ++j) {
                const int m = w * 80 + 16 * j + rs;
                const float gav = gs[m], sbv = gs[640 + m], cbp = 1.f + gs[1280 + m];
                xv[j] = bflo(cv[j]); ga[j] = gav;
                const f32x2 A = splat(gav * cbp), X0 = splat(xv[j] * cbp), C = splat(cbp), S = splat(sbv);
                const f32x2 f[4] = {up2(gw[j].x), up2(gw[j].y), up2(gw[j].z), up2(gw[j].w)};
#pragma unroll
                for (int k = 0; k < 4; ++k) { wA[j][k] = A * f[k]; u[k] += X0 * f[k]; v1[k] += C * f[k]; v2[k] += S * f[k]; PIN1(wA[j][k]); }
            }
            const int e = ((lane >> 5) & 1) * 4 + ((lane >> 4) & 1) * 2 + ((lane >> 3) & 1);
            LAS float* rp = (LAS float*)(L + L_RP);
            {
                float a[8] = {v1[0].x, v1[0].y, v1[1].x, v1[1].y, v1[2].x, v1[2].y, v1[3].x, v1[3].y};
                float v = rscat8(a, lane); v += dpp<0x104>(v);
                if (!(lane & 4)) rp[w * 32 + ck * 8 + e] = v;
                float b[8] = {v2[0].x, v2[0].y, v2[1].x, v2[1].y, v2[2].x, v2[2].y, v2[3].x, v2[3].y};
                v = rscat8(b, lane); v += dpp<0x104>(v);
                if (!(lane & 4)) rp[256 + w * 32 + ck * 8 + e] = v;
            }
            BAR();
            f32x2 V1 = {0.f, 0.f}, V2 = {0.f, 0.f};
            if (w == 0) {
#pragma unroll
                for (int k = 0; k < 8; ++k) { V1 += *(LAS f32x2*)(rp + k * 32 + (lane & 15) * 2); V2 += *(LAS f32x2*)(rp + 256 + k * 32 + (lane & 15) * 2); }
                PIN1(V1); PIN1(V2);
            }
#pragma unroll
            for (int k = 0; k < 4; ++k) PIN1(u[k]);
            STAMP(11);
            LAS float* r6 = (LAS float*)(L + L_R6);
            {
                const bool act = t < 480;
                const int pg = t / 160, q = t - pg * 160;
                const u32 goff = OFF_E6 + (u32)(4 * pg * 640 + 4 * q) * 4;
                u32x4 eg[4];
#pragma unroll
                for (int k = 0; k < 4; ++k) eg[k] = u32x4{0u, 0u, 0u, 0u};
                for (int it = 0; it < SPIN; ++it) { CFENCE();
                    if (act) {
#pragma unroll
                        for (int k = 0; k < 4; ++k) eg[k] = ld4(rws, goff + k * 2560, AUX_SC1);
                    }
                    if (!anyl(pois4(eg[0]) | pois4(eg[1]) | pois4(eg[2]) | pois4(eg[3]))) break; PSLEEP();
                }
                const f32x4 s = (__builtin_bit_cast(f32x4, eg[0]) + __builtin_bit_cast(f32x4, eg[1])) + (__builtin_bit_cast(f32x4, eg[2]) + __builtin_bit_cast(f32x4, eg[3]));
                if (act) *(LAS f32x4*)(r6 + pg * 640 + 4 * q) = s;
            }
            STAMP(12);
            BAR();
            float x1[5], sum = 0.f;
#pragma unroll
            for (int j = 0; j < 5; ++j) {
                const int m = w * 80 + 16 * j + rs;
                const float at = (r6[m] + r6[640 + m]) + r6[1280 + m];
                x1[j] = xv[j] + ga[j] * at; sum += x1[j];
                const f32x2 A = splat(at);
#pragma unroll
                for (int k = 0; k < 4; ++k) u[k] += A * wA[j][k];
            }
            sum += dpp<0x124>(sum); sum += dpp<0x128>(sum); sum = rs16(sum, sum); sum = rs32(sum, sum);
            const float mw_ = sum * (1.f / 80.f);
            float q = 0.f;
#pragma unroll
            for (int j = 0; j < 5; ++j) { const float d = x1[j] - mw_; q += d * d; }
            q += dpp<0x124>(q); q += dpp<0x128>(q); q = rs16(q, q); q = rs32(q, q);
            {
                float a[8] = {u[0].x, u[0].y, u[1].x, u[1].y, u[2].x, u[2].y, u[3].x, u[3].y};
                float v = rscat8(a, lane); v += dpp<0x104>(v);
                LAS float* rf = (LAS float*)(L + L_RF);
                if (!(lane & 4)) rf[w * 32 + ck * 8 + e] = v;
                LAS float* sst = (LAS float*)(L + L_ST);
                if (lane == 0) *(LAS f32x2*)(sst + 2 * w) = f32x2{mw_, q};
                BAR();
                if (w == 0 && lane < 16) {
                    f32x2 p[8], s8[8];
#pragma unroll
                    for (int k = 0; k < 8; ++k) { p[k] = *(LAS f32x2*)(rf + k * 32 + lane * 2); s8[k] = *(LAS f32x2*)(sst + 2 * k); }
                    const f32x2 U = ((p[0] + p[1]) + (p[2] + p[3])) + ((p[4] + p[5]) + (p[6] + p[7]));
                    const f32x2 SS = ((s8[0] + s8[1]) + (s8[2] + s8[3])) + ((s8[4] + s8[5]) + (s8[6] + s8[7]));
                    const float mean = SS.x * 0.125f;
                    float dv = 0.f;
#pragma unroll
                    for (int k = 0; k < 8; ++k) { const float d = s8[k].x - mean; dv += d * d; }
                    const float var = (SS.y + 80.f * dv) * (1.f / 640.f);
                    const float rstd = __builtin_amdgcn_rsqf(var + 1e-5f);
                    const float h0 = fmaxf(rstd * (U.x - mean * V1.x) + V2.x, 0.f), h1 = fmaxf(rstd * (U.y - mean * V1.y) + V2.y, 0.f);
                    st4(rws, OFF_HID + (u32)(xi * 16 + lane) * 16, u32x4{__float_as_uint(h0), __float_as_uint(h1), tlo, thi});
                }
            }

        } else if (xi < 88) {
            const int fi = xi - 48, s2 = 5 * (fi & 7) + (fi >> 3);
            const rsrc_t r2 = mkrs(w2, DFF * DM * 2);
            const int chunk = t & 1, q = t >> 1;
            u32x4 fw[6];
#pragma unroll
            for (int i = 0; i < 3; ++i)
#pragma unroll
                for (int e2 = 0; e2 < 2; ++e2) fw[2 * i + e2] = ld4(r2, (u32)((2 * (q + 256 * i) + e2) * DM + 16 * s2 + 8 * chunk) * 2, AUX_NT);
            SB();
            f32x4 x1 = {0.f, 0.f, 0.f, 0.f}, gb = {0.f, 0.f, 0.f, 0.f};
            if (w == 0) {
                const int c4 = lane & 3, p = lane >> 2;
                const u32x2 xx = *(const u32x2*)(x + 16 * s2 + 4 * c4);
                u32x4 ga0, ga1, gb0, gb1;
                for (int it = 0; it < SPIN; ++it) { CFENCE();
                    ga0 = ld4(rws, OFF_G + (u32)(8 * s2 + 2 * c4) * 16, AUX_SC1); ga1 = ld4(rws, OFF_G + (u32)(8 * s2 + 2 * c4 + 1) * 16, AUX_SC1);
                    gb0 = ld4(rws, OFF_G + (u32)(960 + 8 * s2 + 2 * c4) * 16, AUX_SC1); gb1 = ld4(rws, OFF_G + (u32)(960 + 8 * s2 + 2 * c4 + 1) * 16, AUX_SC1);
                    if (!anyl(tagbad(ga0, tlo, thi) | tagbad(ga1, tlo, thi) | tagbad(gb0, tlo, thi) | tagbad(gb1, tlo, thi))) break; PSLEEP();
                }
                u32x4 g = {0u, 0u, 0u, 0u};
                for (int it = 0; it < SPIN; ++it) { CFENCE();
                    if (p < 12) g = ld4(rws, OFF_E6 + (u32)(p * 640 + 16 * s2 + 4 * c4) * 4, AUX_SC1);
                    if (!anyl(pois4(g))) break; PSLEEP();
                }
                f32x4 at = __builtin_bit_cast(f32x4, g);
#pragma unroll
                for (int k = 0; k < 4; ++k) { float v = at[k]; v += dpp<0x124>(v); v += dpp<0x128>(v); v = rs16(v, v); v = rs32(v, v); at[k] = v; }
                x1 = f32x4{bflo(xx.x) + __uint_as_float(ga0.x) * at.x, bfhi(xx.x) + __uint_as_float(ga0.y) * at.y, bflo(xx.y) + __uint_as_float(ga1.x) * at.z, bfhi(xx.y) + __uint_as_float(ga1.y) * at.w};
                gb = f32x4{__uint_as_float(gb0.x), __uint_as_float(gb0.y), __uint_as_float(gb1.x), __uint_as_float(gb1.y)};
                asm volatile("" : "+v"(x1), "+v"(gb));
            }
            f32x2 ff[6][4];
#pragma unroll
            for (int j = 0; j < 6; ++j) { ff[j][0] = up2(fw[j].x); ff[j][1] = up2(fw[j].y); ff[j][2] = up2(fw[j].z); ff[j][3] = up2(fw[j].w); PIN1(ff[j][0]); PIN1(ff[j][1]); PIN1(ff[j][2]); PIN1(ff[j][3]); }
            STAMP(11);
            u32x4 hg[3];
            for (int it = 0; it < SPIN; ++it) { CFENCE();
#pragma unroll
                for (int i = 0; i < 3; ++i) hg[i] = ld4(rws, OFF_HID + (u32)(q + 256 * i) * 16, AUX_SC1);
                if (!anyl(tagbad(hg[0], tlo, thi) | tagbad(hg[1], tlo, thi) | tagbad(hg[2], tlo, thi))) break; PSLEEP();
            }
            STAMP(12);
            f32x2 acc[4] = {{0.f, 0.f}, {0.f, 0.f}, {0.f, 0.f}, {0.f, 0.f}};
#pragma unroll
            for (int i = 0; i < 3; ++i) {
                const f32x2 h0 = splat(__uint_as_float(hg[i].x)), h1 = splat(__uint_as_float(hg[i].y));
#pragma unroll
                for (int k = 0; k < 4; ++k) { acc[k] += h0 * ff[2 * i][k]; acc[k] += h1 * ff[2 * i + 1][k]; }
            }
            float a[8] = {acc[0].x, acc[0].y, acc[1].x, acc[1].y, acc[2].x, acc[2].y, acc[3].x, acc[3].y};
            float v = rscat8(a, lane);
            v += dpp<0x4E>(v); v += dpp<0x104>(v);
            const int e = ((lane >> 5) & 1) * 4 + ((lane >> 4) & 1) * 2 + ((lane >> 3) & 1);
            LAS float* r2l = (LAS float*)(L + L_R2);
            if (!(lane & 6)) r2l[w * 16 + chunk * 8 + e] = v;
            BAR();
            if (t < 4) {
                f32x4 p[8];
#pragma unroll
                for (int k = 0; k < 8; ++k) p[k] = *(LAS f32x4*)(r2l + k * 16 + t * 4);
                const f32x4 s = ((p[0] + p[1]) + (p[2] + p[3])) + ((p[4] + p[5]) + (p[6] + p[7]));
                const f32x4 o = x1 + gb * s;
                *(u32x2*)(out + 16 * s2 + 4 * t) = u32x2{cvtpk(o.x, o.y), cvtpk(o.z, o.w)};
            }
            if (xi == 48 && t == 0) __hip_atomic_store((GAS u64*)(ws + OFF_EPOCH), epoch + 1ull, __ATOMIC_RELAXED, __HIP_MEMORY_SCOPE_AGENT);

        }
    } else {
        const int yi = bid - 128;
        const bool isM = yi < 80;
        const unsigned char* src = (const unsigned char*)ckv + (size_t)yi * 64 * 1536;
        u32 koff[12];
#define KOFF() _Pragma("unroll") for (int j = 0; j < 12; ++j) { const int idx = j * 64 + lane, row = w * 8 + idx / 96, p = idx % 96; const int ch = (p & ~15) | ((p ^ row) & 15); koff[j] = (u32)(row * 1536 + ch * 16); PIN1(koff[j]); }
        u32x4 mw[3]; u32 mc[3]; u32 xr[5], xm = 0;
        const int m_rg = yi & 7, m_s = yi >> 3;
        if (isM) {
            const rsrc_t rmod = mkrs(wmod, DM * 3840 * 2);
            const int row0 = m_rg * 80 + w * 10 + (lane >> 4);
            const u32 off = (u32)(row0 * 3840 + ((lane >> 3) & 1) * 640 + m_s * 64 + (lane & 7) * 8) * 2;
            mw[0] = ld4(rmod, off, AUX_NT); mw[1] = ld4(rmod, off + 4 * 7680, AUX_NT); mw[2] = ld4(rmod, lane < 32 ? off + 8 * 7680 : OOB, AUX_NT);
            const rsrc_t rc = mkrs(cc, DM * 2);
            mc[0] = ldh(rc, row0 * 2); mc[1] = ldh(rc, row0 * 2 + 8); mc[2] = ldh(rc, row0 * 2 + (lane < 32 ? 16 : 0));
            if (w == 0) {
#pragma unroll
                for (int i = 0; i < 5; ++i) xr[i] = ((const u32*)x)[lane + 64 * i];
                xm = ((const u32*)x)[32 * m_s + (lane & 31)];
            }
        }
        SB();
        KOFF()
        if (CKV1 > 0) {
            wait_until(tstart, D_CKV);
#pragma unroll
            for (int j = 0; j < CKV1; ++j) dma16nt(src, koff[j], lbase + L_KV + (u32)(w * 12 + j) * 1024);
            SB();
        }
        GETTAG()
        if (isM) {
            f32x2 lnx = {0.f, 0.f};
            if (w == 0) {
                float xv[10];
#pragma unroll
                for (int i = 0; i < 5; ++i) { xv[2 * i] = bflo(xr[i]); xv[2 * i + 1] = bfhi(xr[i]); }
                float mean, rstd; ln_stats(xv, mean, rstd);
                lnx = f32x2{(bflo(xm) - mean) * rstd, (bfhi(xm) - mean) * rstd};
            }
            float a[8], f[8];
            const float s0 = silu(bflo(mc[0])), s1 = silu(bflo(mc[1])), s2 = silu(bflo(mc[2]));
            unp8(mw[0], f);
#pragma unroll
            for (int i = 0; i < 8; ++i) a[i] = s0 * f[i];
            unp8(mw[1], f);
#pragma unroll
            for (int i = 0; i < 8; ++i) a[i] += s1 * f[i];
            unp8(mw[2], f);
#pragma unroll
            for (int i = 0; i < 8; ++i) a[i] += s2 * f[i];
            const float r0 = rs32(a[0], a[4]), r1 = rs32(a[1], a[5]), r2 = rs32(a[2], a[6]), r3 = rs32(a[3], a[7]);
            const f32x2 sv = {rs16(r0, r2), rs16(r1, r3)};
            LAS float* red = (LAS float*)(L + L_RM);
            *(LAS f32x2*)(red + w * 128 + ((lane >> 3) & 1) * 64 + (lane & 7) * 8 + 4 * (lane >> 5) + 2 * ((lane >> 4) & 1)) = sv;
            BAR();
            if (w == 0) {
                f32x2 p[8];
#pragma unroll
                for (int k = 0; k < 8; ++k) p[k] = *(LAS f32x2*)(red + k * 128 + 2 * lane);
                const f32x2 s = ((p[0] + p[1]) + (p[2] + p[3])) + ((p[4] + p[5]) + (p[6] + p[7]));
                auto rx = __builtin_amdgcn_permlane32_swap(__float_as_uint(s.x), __float_as_uint(s.x), false, false);
                auto ry = __builtin_amdgcn_permlane32_swap(__float_as_uint(s.y), __float_as_uint(s.y), false, false);
                float hx = lnx.x * __uint_as_float(rx[1]) + __uint_as_float(rx[0]), hy = lnx.y * __uint_as_float(ry[1]) + __uint_as_float(ry[0]);
                if (m_rg == 0) { hx += lnx.x; hy += lnx.y; }
                const int g = 32 * m_s + lane, gm = g / 80, gr = g - gm * 80, gw_ = gr / 10;
                if (lane < 32) st4(rws, OFF_E1 + (u32)(((m_rg * 4 + gm) * 8 + gw_) * 16 + (gr - gw_ * 10)) * 16, u32x4{__float_as_uint(hx), __float_as_uint(hy), tlo, thi});
            }
            BAR();
        }
        STAMP(1);
        wait_until(tstart, isM ? D_CKV : D_CKV2);
        const int kq = w >> 1, ht = w & 1;
        const u32 qoff = OFF_E3 + (u32)(kq * 12 * 32 + ht * 16 + (lane & 15)) * 64 + (lane >> 4) * 16;
        const u32 pfoff = OFF_PF + (u32)(w * 16 + (lane & 15)) * 16;
        u32x4 pf, sent;
#pragma unroll
        for (int j = CKV1; j < 12; ++j) {
            dma16nt(src, koff[j], lbase + L_KV + (u32)(w * 12 + j) * 1024);
            if (j == JPF) pf = ald4(rws4, pfoff);
            if (j - CKV1 >= YW) vmwait<YW>();
        }
        SB();
        STAMP(2);
        await<11 - JPF>(pf);
        if (anyl(tagbad(pf, tlo, thi))) {
            for (int it = 0; it < SPIN; ++it) { CFENCE();
                pf = ald4(rws4, pfoff); await<0>(pf);
                if (!anyl(tagbad(pf, tlo, thi))) break;
            }
        }
        sent = ald4(rws4, qoff + 11 * 2048);
        await<0>(sent);
        BAR();
        STAMP(3);
        const u32 opz = OFF_O + (u32)yi * 49152 + t * 16;
        const int wrg = yi % 12, wcs = yi / 12;
        u32x4 ow[8];
        const rsrc_t ro = mkrs(wo, 6144 * DM * 2);
        const u32 wobase = yi < 120 ? (u32)((wrg * 512 + w * 64 + (lane >> 3) * 8) * DM + wcs * 64 + (lane & 7) * 8) * 2 : OOB;
        u32x4 pf2;
        u32x4 va[6][2];
        {
            u32x4 qf[12];
            if (anyl(pois4(sent))) {
                for (int i2 = 0; i2 < SPIN; ++i2) { CFENCE();
                    sent = ald4(rws4, qoff + 11 * 2048); await<0>(sent);
                    if (!anyl(pois4(sent))) break; PSLEEP();
                }
            }
            STAMP(5);
            qf[11] = sent;
#pragma unroll
            for (int j = 0; j < 11; ++j) qf[j] = ld4(rws, qoff + j * 2048, AUX_SC1);
            SB();
            const int r = lane & 15;
            const int keyr = 8 * (r >> 3) + 2 * (r & 3) + ((r >> 2) & 1);
            u32x4 af[6][4];
#pragma unroll
            for (int ks = 0; ks < 6; ++ks) {
                const int ch = kq * 24 + ks * 4 + (lane >> 4);
                const int p = (ch & ~15) | ((ch ^ keyr) & 15);
#pragma unroll
                for (int kt = 0; kt < 4; ++kt) { af[ks][kt] = *(LAS u32x4*)(L + L_KV + (kt * 16 + keyr) * 1536 + p * 16); }
            }
            {
                const int g = lane >> 4, e = (lane >> 2) & 3, pp = lane & 3;
#pragma unroll
                for (int q = 0; q < 6; ++q) {
                    const int ch = 12 * w + 3 * pp + (q >> 1), hf = (q ^ (pp >> 1)) & 1;
#pragma unroll
                    for (int k2 = 0; k2 < 2; ++k2) {
                        const int ka = k2 * 32 + 8 * g + 2 * e, kb = ka + 1;
                        const int pa = (ch & ~15) | ((ch ^ ka) & 15), pbx = (ch & ~15) | ((ch ^ kb) & 15);
                        const s16x4 t0 = __builtin_amdgcn_ds_read_tr16_b64_v4i16((LAS s16x4*)(L + L_KV + ka * 1536 + pa * 16 + hf * 8));
                        const s16x4 t1 = __builtin_amdgcn_ds_read_tr16_b64_v4i16((LAS s16x4*)(L + L_KV + kb * 1536 + pbx * 16 + hf * 8));
                        const u32x2 a0 = __builtin_bit_cast(u32x2, t0), a1 = __builtin_bit_cast(u32x2, t1);
                        va[q][k2] = u32x4{a0.x, a0.y, a1.x, a1.y};
                    }
                }
            }
#pragma unroll
            for (int ks = 0; ks < 6; ++ks)
#pragma unroll
                for (int kt = 0; kt < 4; ++kt) PIN1(af[ks][kt]);
#pragma unroll
            for (int q = 0; q < 6; ++q) { PIN1(va[q][0]); PIN1(va[q][1]); }
            f32x4 acc[4];
            for (int it = 0; it < SPIN; ++it) {
#pragma unroll
                for (int kt = 0; kt < 4; ++kt) acc[kt] = f32x4{0.f, 0.f, 0.f, 0.f};
                u32 pm = 0;
#pragma unroll
                for (int j = 0; j < 12; ++j) {
#pragma unroll
                    for (int kt = 0; kt < 4; ++kt) acc[kt] = mfma(af[j >> 1][kt], qf[j], acc[kt]);
                    pm = max(pm, max(max(qf[j].x, qf[j].y), max(qf[j].z, qf[j].w)));
                }
                if (!anyl(pm == POISON)) break;
                CFENCE();
#pragma unroll
                for (int j = 0; j < 12; ++j) qf[j] = ld4(rws, qoff + j * 2048, AUX_SC1);
                SB();
            }
            STAMP(6);
            pf2 = ald4(rws4, pfoff + (OFF_PF2 - OFF_PF));
#pragma unroll
            for (int j = 0; j < WO1; ++j) ow[j] = ld4(ro, wobase + j * 1280, AUX_NT);
            SB();
            LAS float* sp = (LAS float*)(L + L_SP);
#pragma unroll
            for (int kt = 0; kt < 4; ++kt) *(LAS f32x4*)(sp + (kq * 32 + ht * 16 + (lane & 15)) * 68 + kt * 16 + 4 * (lane >> 4)) = acc[kt];
        }
        BAR();
        {
            LAS float* sp = (LAS float*)(L + L_SP);
            const int hd = t >> 4, qd = t & 15;
            f32x4 s = *(LAS f32x4*)(sp + hd * 68 + qd * 4);
#pragma unroll
            for (int k = 1; k < 4; ++k) s += *(LAS f32x4*)(sp + (k * 32 + hd) * 68 + qd * 4);
            const float m = row_max(fmaxf(fmaxf(s.x, s.y), fmaxf(s.z, s.w)));
            const float p0 = __builtin_amdgcn_exp2f(s.x - m), p1 = __builtin_amdgcn_exp2f(s.y - m), p2 = __builtin_amdgcn_exp2f(s.z - m), p3 = __builtin_amdgcn_exp2f(s.w - m);
            const u32 b01 = cvtpk(p0, p1), b23 = cvtpk(p2, p3);
            *(LAS u32x2*)(L + L_P + hd * 144 + qd * 8) = u32x2{b01, b23};
            const float l = row_sum((bflo(b01) + bfhi(b01)) + (bflo(b23) + bfhi(b23)));
            st4(rws, qd == 0 ? OFF_ML + (u32)(hd * 128 + yi) * 16 : OOB, u32x4{__float_as_uint(m), __float_as_uint(l), tlo, thi});
        }
        BAR();
        STAMP(7);
        {
            u32x4 pb[2][2];
#pragma unroll
            for (int hh = 0; hh < 2; ++hh)
#pragma unroll
                for (int k2 = 0; k2 < 2; ++k2) pb[hh][k2] = *(LAS u32x4*)(L + L_P + (hh * 16 + (lane & 15)) * 144 + k2 * 64 + (lane >> 4) * 16);
            LAS unsigned char* sw = L + L_KV + (lane & 15) * OSTR + 192 * w + 48 * (lane >> 4);
#pragma unroll
            for (int hh = 0; hh < 2; ++hh) {
#pragma unroll
                for (int j = 0; j < 3; ++j) {
                    f32x4 a0 = {0.f, 0.f, 0.f, 0.f}, a1 = {0.f, 0.f, 0.f, 0.f};
                    a0 = mfma(va[2 * j][0], pb[hh][0], a0); a1 = mfma(va[2 * j + 1][0], pb[hh][0], a1);
                    a0 = mfma(va[2 * j][1], pb[hh][1], a0); a1 = mfma(va[2 * j + 1][1], pb[hh][1], a1);
                    *(LAS u32x4*)(sw + hh * 16 * OSTR + j * 16) = u32x4{canon(cvtpk(a0.x, a0.y)), canon(cvtpk(a0.z, a0.w)), canon(cvtpk(a1.x, a1.y)), canon(cvtpk(a1.z, a1.w))};
                }
            }
            STAMP(4);
            await<WO1 + 1>(pf2);
            if (anyl(tagbad(pf2, tlo, thi))) {
                for (int it = 0; it < SPIN; ++it) { CFENCE();
                    pf2 = ald4(rws4, pfoff + (OFF_PF2 - OFF_PF)); await<0>(pf2);
                    if (!anyl(tagbad(pf2, tlo, thi))) break;
                }
            }
            BAR();
            STAMP(8);
            u32x4 ov[6];
#pragma unroll
            for (int j = 0; j < 6; ++j) { const int p = j * 512 + t, hd = p / 96; ov[j] = *(LAS u32x4*)(L + L_KV + hd * (OSTR - 1536) + p * 16); }
#pragma unroll
            for (int j = 0; j < 6; ++j) st4(rws, opz + j * 8192, ov[j]);
            SB();
            asm volatile("s_barrier" ::: "memory");
            SB();
        }
        STAMP(9);
        if (yi < 120) {
#pragma unroll
            for (int j = WO1; j < 8; ++j) ow[j] = ld4(ro, wobase + j * 1280, AUX_NT);
            SB();
            f32x2 of[8][4];
#pragma unroll
            for (int j = 0; j < 8; ++j) { of[j][0] = up2(ow[j].x); of[j][1] = up2(ow[j].y); of[j][2] = up2(ow[j].z); of[j][3] = up2(ow[j].w); PIN1(of[j][0]); PIN1(of[j][1]); PIN1(of[j][2]); PIN1(of[j][3]); }
            STAMP(10);
            const int cc8 = lane & 7;
            const u32 goff = OFF_E5 + (u32)((cc8 < 4 ? 0 : 2) * 3072 + wrg * 256 + w * 32 + (lane >> 3) * 4 + (cc8 < 4 ? cc8 : 7 - cc8)) * 16;
            u32x4 g0, g1;
            for (int it = 0; it < SPIN; ++it) { CFENCE();
                g0 = ld4(rws, goff, AUX_SC1); g1 = ld4(rws, goff + 3072 * 16, AUX_SC1);
                if (!anyl(tagbad(g0, tlo, thi) | tagbad(g1, tlo, thi))) break; PSLEEP();
            }
            STAMP(11);
            float vx = __uint_as_float(g0.x) + __uint_as_float(g1.x), vy = __uint_as_float(g0.y) + __uint_as_float(g1.y);
            vx += dpp<0x141>(vx); vy += dpp<0x141>(vy);
            vx = dppb<0x1B, 0xA>(vx, vx); vy = dppb<0x1B, 0xA>(vy, vy);
            f32x2 a2[4] = {{0.f, 0.f}, {0.f, 0.f}, {0.f, 0.f}, {0.f, 0.f}};
#define WSTEP(k) { const f32x2 hx = splat(dpp<k * 0x55>(vx)), hy = splat(dpp<k * 0x55>(vy)); _Pragma("unroll") for (int i = 0; i < 4; ++i) { a2[i] += hx * of[2 * k][i]; a2[i] += hy * of[2 * k + 1][i]; } }
            WSTEP(0) WSTEP(1) WSTEP(2) WSTEP(3)
#undef WSTEP
            float a[8] = {a2[0].x, a2[0].y, a2[1].x, a2[1].y, a2[2].x, a2[2].y, a2[3].x, a2[3].y};
            const float v = rscat8(a, lane);
            const int e = ((lane >> 5) & 1) * 4 + ((lane >> 4) & 1) * 2 + ((lane >> 3) & 1);
            LAS float* red = (LAS float*)(L + L_RW);
            red[w * 64 + (lane & 7) * 8 + e] = v;
            BAR();
            if (w == 0 && lane < 16) {
                f32x4 p[8];
#pragma unroll
                for (int k = 0; k < 8; ++k) p[k] = *(LAS f32x4*)(red + k * 64 + lane * 4);
                const f32x4 s4 = ((p[0] + p[1]) + (p[2] + p[3])) + ((p[4] + p[5]) + (p[6] + p[7]));
                st4(rws, OFF_E6 + (u32)(wrg * 640 + wcs * 64) * 4 + lane * 16, u32x4{canonf(s4.x), canonf(s4.y), canonf(s4.z), canonf(s4.w)});
            }
            STAMP(12);
        }
    }
}

extern "C" void kernel_launch(void* const* d_in, const int* in_sizes, int n_in, void* d_out, int out_size, void* d_ws, size_t ws_size, hipStream_t stream) {
    (void)in_sizes; (void)n_in; (void)out_size; (void)ws_size;
    hipFuncSetAttribute((const void*)adaln_mla_mlp_kernel, hipFuncAttributeMaxDynamicSharedMemorySize, LDS_BYTES);
    adaln_mla_mlp_kernel<<<NWG, NT, LDS_BYTES, stream>>>(
        (const u16*)d_in[0], (const u16*)d_in[1], (const u16*)d_in[2], (const u16*)d_in[3], (const u16*)d_in[4], (const u16*)d_in[5],
        (const u16*)d_in[7], (const u16*)d_in[8], (const u16*)d_in[9], (const u16*)d_in[10], (u16*)d_out, (unsigned char*)d_ws);
}
```

```cpp
#include <hip/hip_runtime.h>
#include <stdint.h>

typedef unsigned u32;
typedef unsigned long long u64;
typedef unsigned short u16;
typedef u32 u32x2 __attribute__((ext_vector_type(2)));
typedef u32 u32x3 __attribute__((ext_vector_type(3)));
typedef u32 u32x4 __attribute__((ext_vector_type(4)));
typedef float f32x2 __attribute__((ext_vector_type(2)));
typedef float f32x4 __attribute__((ext_vector_type(4)));
typedef short bf16x8 __attribute__((ext_vector_type(8)));
typedef short s16x4 __attribute__((ext_vector_type(4)));
typedef __amdgpu_buffer_rsrc_t rsrc_t;
#define LAS __attribute__((address_space(3)))
#define GAS __attribute__((address_space(1)))

constexpr int NH = 32, SK = 8192, HD = 192, DM = 640, DC = 768, DFF = 1536;
constexpr int NWG = 256, NT = 512;

#pragma clang diagnostic ignored "-Winline-asm"
#define UK1 4
#define D_WQ 0
#define D_CKV 350
#define D_CKV2 350
#define CKV1 0
#define D_PO 560
#define D_UV 730
#define XW 4
#define YW 3
#define D_E1 260
#define D_E2 40
#define JPF 7
#define ML_SLEEP 4
#define WO1 0
#define FIX1 1
#define FIX2 0
#define GPOS 3
#define UV1 12
#define GY 0
#define D_S 670
#define FULLG 0
#define DIRO 2
#define VAPOS 1
#define PINAF 0
#define YPOIS 0
#define DUPKV 0
#define UVA 0
#define D_G 0
#define W1P 1
#define D_WO 1330
#define FLAGO 1
#define WOE 0
constexpr u32 OFF_EPOCH = 0;
constexpr u32 OFF_PF = 256;
constexpr u32 OFF_PF2 = OFF_PF + 128 * 16;
constexpr u32 OFF_E1 = OFF_PF2 + 128 * 16;
constexpr u32 OFF_E2 = OFF_E1 + 8 * 4 * 8 * 256;
constexpr u32 OFF_E3 = OFF_E2 + 4 * 32 * 96 * 16;
constexpr u32 OFF_ML = OFF_E3 + 32 * 4 * 768;
constexpr u32 OFF_E5 = OFF_ML + 32 * 128 * 16;
constexpr u32 OFF_E6 = OFF_E5 + 4 * 3072 * 16;
constexpr u32 OFF_G = OFF_E6 + 12 * 640 * 4;
constexpr u32 OFF_HID = OFF_G + 1280 * 16;
constexpr u32 OFF_O = OFF_HID + 768 * 16;
constexpr u32 WS_BYTES = OFF_O + 32 * 4 * 128 * 384;
constexpr u32 OFF_DIAG = WS_BYTES;
static_assert(OFF_O % 128 == 0 && OFF_E3 % 128 == 0 && OFF_E5 % 128 == 0 && OFF_E2 % 256 == 0 && OFF_E6 % 128 == 0 && OFF_E1 % 256 == 0, "alignment");

constexpr int L_KV = 0;
constexpr int OSTR = 1552;
constexpr int L_SP = 98304;
constexpr int L_P = L_SP + 4 * 32 * 68 * 4;
constexpr int L_RM = L_P + 32 * 144;
constexpr int L_RW = L_RM + 8 * 128 * 4;
constexpr int L_ML = L_RW + 8 * 64 * 4;
constexpr int LDS_BYTES = L_ML + 256;
constexpr int L_RQ = 0;
constexpr int L_Q = L_RQ + 6144;
constexpr int L_RA = L_Q + 768;
constexpr int L_WL = L_RA + 768;
constexpr int L_RV1 = L_WL + 1024;
constexpr int L_RV2 = L_RV1 + 6144;
constexpr int L_RG = L_RV2 + 6144;
constexpr int L_GS = L_RG + 1024;
constexpr int L_RP = L_GS + 7680;
constexpr int L_RF = L_RP + 2048;
constexpr int L_ST = L_RF + 1024;
constexpr int L_R2 = L_ST + 64;
constexpr int L_R6 = L_R2 + 512;
constexpr int SPIN = 1 << 16;
constexpr u32 POISON = 0xFFFFFFFFu;
constexpr u32 OOB = 0x80000000u;
constexpr int AUX_SC1 = 16, AUX_NT = 2;
constexpr u64 TAGMUL = 0x9E3779B97F4A7C15ull;

__device__ __forceinline__ float bflo(u32 v) { return __uint_as_float(v << 16); }
__device__ __forceinline__ float bfhi(u32 v) { return __uint_as_float(v & 0xFFFF0000u); }
__device__ __forceinline__ float bf1(u16 v) { return __uint_as_float((u32)v << 16); }
typedef __bf16 bf16x2_t __attribute__((ext_vector_type(2)));
__device__ __forceinline__ u32 cvtpk(float lo, float hi) { f32x2 v = {lo, hi}; return __builtin_bit_cast(u32, __builtin_convertvector(v, bf16x2_t)); }
__device__ __forceinline__ u32 canon(u32 v) { return v < 0xFFFFFFFEu ? v : 0xFFFFFFFEu; }
__device__ __forceinline__ u32 canonf(float v) { return canon(__float_as_uint(v)); }
__device__ __forceinline__ rsrc_t mkrs(const void* p, u32 bytes) { return __builtin_amdgcn_make_buffer_rsrc((void*)p, 0, bytes, 0x00020000); }
__device__ __forceinline__ u32x4 ld4(rsrc_t r, u32 off, int aux) { return aux == AUX_SC1 ? __builtin_amdgcn_raw_buffer_load_b128(r, off, 0, AUX_SC1) : __builtin_amdgcn_raw_buffer_load_b128(r, off, 0, AUX_NT); }
__device__ __forceinline__ u32x3 ld3(rsrc_t r, u32 off, int aux) { return aux == AUX_SC1 ? __builtin_amdgcn_raw_buffer_load_b96(r, off, 0, AUX_SC1) : __builtin_amdgcn_raw_buffer_load_b96(r, off, 0, AUX_NT); }
__device__ __forceinline__ void st4(rsrc_t r, u32 off, u32x4 v) { __builtin_amdgcn_raw_buffer_store_b128(v, r, off, 0, AUX_SC1); }
__device__ __forceinline__ void st2(rsrc_t r, u32 off, u32x2 v) { __builtin_amdgcn_raw_buffer_store_b64(v, r, off, 0, AUX_SC1); }
#define CFENCE() asm volatile("" ::: "memory")
#define PSL 0
#define PSLEEP() do { if (PSL > 0) __builtin_amdgcn_s_sleep(PSL); } while (0)
#define SB() __builtin_amdgcn_sched_barrier(0)
#define BAR() asm volatile("s_waitcnt lgkmcnt(0)\n\ts_barrier" ::: "memory")
#define LGKM0() asm volatile("s_waitcnt lgkmcnt(0)" ::: "memory")
#define DRAIN() asm volatile("s_waitcnt vmcnt(0)" ::: "memory")

template <int CTRL> __device__ __forceinline__ float dpp(float x) { return __int_as_float(__builtin_amdgcn_update_dpp(0, __float_as_int(x), CTRL, 0xf, 0xf, true)); }
__device__ __forceinline__ float rs32(float a, float b) { auto r = __builtin_amdgcn_permlane32_swap(__float_as_uint(a), __float_as_uint(b), false, false); return __uint_as_float(r[0]) + __uint_as_float(r[1]); }
__device__ __forceinline__ float rs16(float a, float b) { auto r = __builtin_amdgcn_permlane16_swap(__float_as_uint(a), __float_as_uint(b), false, false); return __uint_as_float(r[0]) + __uint_as_float(r[1]); }
__device__ __forceinline__ float row_sum(float x) { x += dpp<0x128>(x); x += dpp<0x124>(x); x += dpp<0x122>(x); x += dpp<0x121>(x); return x; }
__device__ __forceinline__ float row_max(float x) { x = fmaxf(x, dpp<0x128>(x)); x = fmaxf(x, dpp<0x124>(x)); x = fmaxf(x, dpp<0x122>(x)); x = fmaxf(x, dpp<0x121>(x)); return x; }
__device__ __forceinline__ float wave_sum(float x) { x = row_sum(x); x = rs16(x, x); x = rs32(x, x); return x; }
__device__ __forceinline__ float wave_max(float x) {
    x = row_max(x);
    { auto r = __builtin_amdgcn_permlane16_swap(__float_as_uint(x), __float_as_uint(x), false, false); x = fmaxf(__uint_as_float(r[0]), __uint_as_float(r[1])); }
    { auto r = __builtin_amdgcn_permlane32_swap(__float_as_uint(x), __float_as_uint(x), false, false); x = fmaxf(__uint_as_float(r[0]), __uint_as_float(r[1])); }
    return x;
}
__device__ __forceinline__ float rscat8(const float (&a)[8], int lane) {
    float r0 = rs32(a[0], a[4]), r1 = rs32(a[1], a[5]), r2 = rs32(a[2], a[6]), r3 = rs32(a[3], a[7]);
    float s0 = rs16(r0, r2), s1 = rs16(r1, r3);
    float t0 = s0 + dpp<0x128>(s0), t1 = s1 + dpp<0x128>(s1);
    return (lane & 8) ? t1 : t0;
}
__device__ __forceinline__ bool pois4(u32x4 v) { u32 m = max(max(v.x, v.y), max(v.z, v.w)); return m == POISON; }
__device__ __forceinline__ bool pois3(u32x3 v) { u32 m = max(max(v.x, v.y), v.z); return m == POISON; }
__device__ __forceinline__ bool anyl(bool b) { return __builtin_amdgcn_ballot_w64(b) != 0ull; }
__device__ __forceinline__ f32x4 mfma(u32x4 a, u32x4 b, f32x4 c) { return __builtin_amdgcn_mfma_f32_16x16x32_bf16(__builtin_bit_cast(bf16x8, a), __builtin_bit_cast(bf16x8, b), c, 0, 0, 0); }
__device__ __forceinline__ float silu(float v) { return v / (1.f + __expf(-v)); }
__device__ __forceinline__ void unp8(u32x4 v, float (&f)[8]) { f[0] = bflo(v.x); f[1] = bfhi(v.x); f[2] = bflo(v.y); f[3] = bfhi(v.y); f[4] = bflo(v.z); f[5] = bfhi(v.z); f[6] = bflo(v.w); f[7] = bfhi(v.w); }
__device__ __forceinline__ void unp6(u32x3 v, float (&f)[6]) { f[0] = bflo(v.x); f[1] = bfhi(v.x); f[2] = bflo(v.y); f[3] = bfhi(v.y); f[4] = bflo(v.z); f[5] = bfhi(v.z); }

__device__ __forceinline__ void wait_until(u64 t0, int ticks) {
    if (ticks > 0) { for (int i = 0; i < 4096; ++i) { if (__builtin_amdgcn_s_memrealtime() - t0 >= (u64)ticks) break; __builtin_amdgcn_s_sleep(1); } }
}
#define STAMP(i) do { } while (0)

#define STAMP2(i) do { } while (0)
__device__ __forceinline__ void ln_stats(const float (&v)[10], float& mean, float& rstd) {
    float s = 0.f;
#pragma unroll
    for (int i = 0; i < 10; ++i) s += v[i];
    mean = wave_sum(s) * (1.f / 640.f);
    float q = 0.f;
#pragma unroll
    for (int i = 0; i < 10; ++i) { float d = v[i] - mean; q += d * d; }
    float var = wave_sum(q) * (1.f / 640.f);
    rstd = 1.f / sqrtf(var + 1e-5f);
}

#define CAS __attribute__((address_space(4)))
#define PIN1(a) asm volatile("" : "+v"(a))
template <int CTRL, int BANK> __device__ __forceinline__ float dppb(float old, float x) { return __int_as_float(__builtin_amdgcn_update_dpp(__float_as_int(old), __float_as_int(x), CTRL, 0xf, BANK, false)); }
__device__ __forceinline__ u32x2 ld2(rsrc_t r, u32 off) { return __builtin_amdgcn_raw_buffer_load_b64(r, off, 0, AUX_SC1); }
__device__ __forceinline__ u32 ld1(rsrc_t r, u32 off) { return __builtin_amdgcn_raw_buffer_load_b32(r, off, 0, AUX_SC1); }
__device__ __forceinline__ bool tagbad(u32x4 g, u32 tlo, u32 thi) { return (g.z != tlo) | (g.w != thi); }
__device__ __forceinline__ f32x2 up2(u32 v) { return f32x2{bflo(v), bfhi(v)}; }
__device__ __forceinline__ f32x2 splat(float v) { return f32x2{v, v}; }

__device__ __forceinline__ void dma16nt(const void* base, u32 voff, u32 lds) {
    asm volatile("s_mov_b32 m0, %2\n\ts_nop 0\n\tglobal_load_lds_dwordx4 %0, %1 nt" ::"v"(voff), "s"(base), "s"(lds) : "memory", "m0");
}
__device__ __forceinline__ u32 ldh(rsrc_t r, u32 off) { return __builtin_amdgcn_raw_buffer_load_b16(r, off, 0, 0); }
template <int N> __device__ __forceinline__ void vmwait() { if (N < 63) asm volatile("s_waitcnt vmcnt(%0)" ::"n"(N) : "memory"); }
__device__ __forceinline__ u32x4 ald4(u32x4 rs, u32 off) { u32x4 v; asm volatile("buffer_load_dwordx4 %0, %1, %2, 0 offen sc1" : "=v"(v) : "v"(off), "s"(rs) : "memory"); return v; }
template <int N> __device__ __forceinline__ void await(u32x4& v) { asm volatile("s_waitcnt vmcnt(%1)" : "+v"(v) : "n"(N) : "memory"); }
#define GETTAG() \
    const u64 epoch = ((u64)(u32)__builtin_amdgcn_readfirstlane(epv.y) << 32) | (u64)(u32)__builtin_amdgcn_readfirstlane(epv.x); \
    const u64 tag = (epoch + 1ull) * TAGMUL; \
    const u32 tlo = (u32)tag, thi = (u32)(tag >> 32);

__global__ __launch_bounds__(NT, 2) void adaln_mla_mlp_kernel(
    const u16* __restrict__ x, const u16* __restrict__ ckv, const u16* __restrict__ wq, const u16* __restrict__ wuk,
    const u16* __restrict__ wuv, const u16* __restrict__ wo, const u16* __restrict__ w1, const u16* __restrict__ w2,
    const u16* __restrict__ cc, const u16* __restrict__ wmod, u16* __restrict__ out, unsigned char* __restrict__ ws) {
    extern __shared__ __attribute__((aligned(16))) unsigned char smem[];
    LAS unsigned char* L = (LAS unsigned char*)smem;
    STAMP(0);
    const u64 tstart = __builtin_amdgcn_s_memrealtime();
    asm volatile("" ::"s"(x), "s"(ckv), "s"(wq), "s"(wuk), "s"(wuv), "s"(wo), "s"(w1), "s"(w2), "s"(cc), "s"(wmod), "s"(out), "s"(ws));
    const int t = threadIdx.x, lane = t & 63, w = __builtin_amdgcn_readfirstlane(t >> 6);
    const int bid = blockIdx.x;
    const rsrc_t rws = mkrs(ws, WS_BYTES);
    const u32x2 epv = ld2(rws, OFF_EPOCH);
    const u32x4 PZ = {POISON, POISON, POISON, POISON};
    const u32 lbase = (u32)(uintptr_t)L;
    const u32x4 rws4 = {(u32)(uintptr_t)ws, (u32)((uintptr_t)ws >> 32), WS_BYTES, 0x00020000u};

    if (bid < 128) {
        const int xi = bid, h = xi & 31, cq = xi >> 5;
        const rsrc_t rmod = mkrs(wmod, DM * 3840 * 2);
        const u32 e3off = OFF_E3 + (u32)((cq * 12 + (lane >> 2)) * 32 + h) * 64 + (lane & 3) * 16;
        st4(rws, (w == 7 && lane < 48) ? e3off : OOB, PZ);
        const rsrc_t rq = mkrs(wq, DM * 6144 * 2);
        const rsrc_t ruk = mkrs(wuk, DC * 6144 * 2);
        const u32 wbase = (u32)(((cq * 192 + w * 24 + (lane >> 5)) * 32 + h) * 192 + 6 * (lane & 31)) * 2;
        u32x3 qw[10], uw[12];
        wait_until(tstart, D_WQ);
        {
            const u32 base = (u32)(((cq * 160 + w * 20 + (lane >> 5)) * 32 + h) * 192 + 6 * (lane & 31)) * 2;
#pragma unroll
            for (int j = 0; j < 10; ++j) { qw[j] = ld3(rq, base + j * 2 * 12288, AUX_NT); if (j >= XW) { SB(); vmwait<XW>(); SB(); } }
            SB();
#pragma unroll
            for (int j = 0; j < UK1; ++j) { uw[j] = ld3(ruk, wbase + j * 2 * 12288, AUX_NT); if (10 + j >= XW) { SB(); vmwait<XW>(); SB(); } }
            SB();
        }
        GETTAG()
        f32x2 qf2[10][3];
#pragma unroll
        for (int j = 0; j < 10; ++j) { qf2[j][0] = up2(qw[j].x); qf2[j][1] = up2(qw[j].y); qf2[j][2] = up2(qw[j].z); PIN1(qf2[j][0]); PIN1(qf2[j][1]); PIN1(qf2[j][2]); }
        STAMP(1);
        {
            const bool act = (lane & 15) < 10;
            const u32 goff = act ? OFF_E1 + (u32)(((lane >> 4) * 4 + cq) * 8 + w) * 256 + (lane & 15) * 16 : OOB;
            wait_until(tstart, D_E1);
            u32x4 g0 = ld4(rws, goff, AUX_SC1), g1 = ld4(rws, goff + 32768, AUX_SC1);
            SB();
            asm volatile("s_barrier" ::: "memory");
            SB();
#pragma unroll
            for (int j = UK1; j < 12; ++j) uw[j] = ld3(ruk, wbase + j * 2 * 12288, AUX_NT);
            SB();
            if (anyl(act & (tagbad(g0, tlo, thi) | tagbad(g1, tlo, thi)))) {
                for (int it = 0; it < SPIN; ++it) { CFENCE();
                    g0 = ld4(rws, goff, AUX_SC1); g1 = ld4(rws, goff + 32768, AUX_SC1);
                    if (!anyl(act & (tagbad(g0, tlo, thi) | tagbad(g1, tlo, thi)))) break;
                }
            }
            STAMP(2);
            st4(rws, t == 448 ? OFF_PF + xi * 16 : OOB, u32x4{0u, 0u, tlo, thi});
            SB();
            float s = rs32(__uint_as_float(g0.x) + __uint_as_float(g1.x), __uint_as_float(g0.y) + __uint_as_float(g1.y));
            s = rs16(s, s);
            f32x2 a[3] = {{0.f, 0.f}, {0.f, 0.f}, {0.f, 0.f}};
#define QSTEP(j) { const f32x2 hv = splat(dpp<0x150 + j>(s)); a[0] += hv * qf2[j][0]; a[1] += hv * qf2[j][1]; a[2] += hv * qf2[j][2]; }
            QSTEP(0) QSTEP(1) QSTEP(2) QSTEP(3) QSTEP(4) QSTEP(5) QSTEP(6) QSTEP(7) QSTEP(8) QSTEP(9)
#undef QSTEP
            LAS float* red = (LAS float*)(L + L_RQ);
            {
                LAS float* dst = red + w * 192 + 6 * (lane & 31) + 3 * (lane >> 5);
                dst[0] = rs32(a[0].x, a[1].y); dst[1] = rs32(a[0].y, a[2].x); dst[2] = rs32(a[1].x, a[2].y);
            }
            BAR();
            f32x2 v = {0.f, 0.f};
            if (w < 2) {
                f32x2 p[8];
#pragma unroll
                for (int k = 0; k < 8; ++k) p[k] = *(LAS f32x2*)(red + k * 192 + t * 2);
                v = ((p[0] + p[1]) + (p[2] + p[3])) + ((p[4] + p[5]) + (p[6] + p[7]));
            }
            st4(rws, t < 96 ? OFF_E2 + (u32)((cq * 32 + h) * 96 + t) * 16 : OOB, u32x4{__float_as_uint(v.x), __float_as_uint(v.y), tlo, thi});
        }
        STAMP(3);
        f32x2 wf[12][3];
        {
            LAS float* qs = (LAS float*)(L + L_Q);
            const u32 goff = w < 6 ? OFF_E2 + (u32)(((lane >> 4) * 32 + h) * 96 + 16 * w + (lane & 15)) * 16 : OOB;
            { const u64 tn = __builtin_amdgcn_s_memrealtime(); wait_until(tn, D_E2); }
            u32x4 g = ld4(rws, goff, AUX_SC1);
            SB();
#pragma unroll
            for (int j = 0; j < 12; ++j) { wf[j][0] = up2(uw[j].x); wf[j][1] = up2(uw[j].y); wf[j][2] = up2(uw[j].z); PIN1(wf[j][0]); PIN1(wf[j][1]); PIN1(wf[j][2]); }
            if (w < 6) {
                if (anyl(tagbad(g, tlo, thi))) {
                    for (int it = 0; it < SPIN; ++it) { CFENCE();
                        g = ld4(rws, goff, AUX_SC1);
                        if (!anyl(tagbad(g, tlo, thi))) break;
                    }
                }
                float s = rs32(__uint_as_float(g.x), __uint_as_float(g.y));
                s = rs16(s, s);
                if (!(lane & 16)) qs[2 * (16 * w + (lane & 15)) + (lane >> 5)] = s;
            }
            STAMP(5);
            BAR();
            const f32x2 q0 = *(LAS f32x2*)(qs + 6 * (lane & 31)), q1 = *(LAS f32x2*)(qs + 6 * (lane & 31) + 2), q2 = *(LAS f32x2*)(qs + 6 * (lane & 31) + 4);
            float p[12];
#pragma unroll
            for (int j = 0; j < 12; ++j) { const f32x2 d = wf[j][0] * q0 + wf[j][1] * q1 + wf[j][2] * q2; p[j] = d.x + d.y; }
            float r[6], s[3];
#pragma unroll
            for (int j = 0; j < 6; ++j) r[j] = rs16(p[j], p[j + 6]);
#pragma unroll
            for (int j = 0; j < 3; ++j) {
                const float own = (lane & 8) ? r[j + 3] : r[j], oth = (lane & 8) ? r[j] : r[j + 3];
                float v = own + dpp<0x128>(oth);
                v += dpp<0x141>(v); v += dpp<0x1B>(v); v += dpp<0xB1>(v);
                s[j] = v;
            }
            LAS float* red = (LAS float*)(L + L_RA);
            {
                const int j = lane & 7;
                const float v = j == 0 ? s[0] : (j == 1 ? s[1] : s[2]);
                if (j < 3) red[w * 24 + 2 * (j + 3 * ((lane >> 3) & 1) + 6 * ((lane >> 4) & 1)) + (lane >> 5)] = v;
            }
            BAR();
            u32 o[4] = {0u, 0u, 0u, 0u};
            if (w == 0) {
                const float sc = 0.07216878364870322f * 1.4426950408889634f;
                const int c0 = ((lane >> 3) * 32 + (lane & 3) * 8) & 255;
                const f32x4 p0 = *(LAS f32x4*)(red + c0) * sc, p1 = *(LAS f32x4*)(red + c0 + 4) * sc;
                const float v8[8] = {p0.x, p0.y, p0.z, p0.w, p1.x, p1.y, p1.z, p1.w};
#pragma unroll
                for (int i = 0; i < 4; ++i) {
                    const u32 hi = cvtpk(v8[2 * i], v8[2 * i + 1]);
                    const u32 lo = cvtpk(v8[2 * i] - bflo(hi), v8[2 * i + 1] - bfhi(hi));
                    o[i] = canon((lane & 4) ? lo : hi);
                }
            }
            st4(rws, t < 48 ? e3off : OOB, u32x4{o[0], o[1], o[2], o[3]});
        }
        STAMP(6);
        const rsrc_t ruv = mkrs(wuv, DC * 6144 * 2);
        u32x3 vw[12];
        SB();
#pragma unroll
        for (int j = 0; j < UVA; ++j) vw[j] = ld3(ruv, wbase + j * 2 * 12288, AUX_NT);
        SB();
        u32x4 gw[5]; u32 cv[5];
        const int rs = lane >> 2, ck = lane & 3;
        const int gi = xi - 48;
        const int g_s = 2 * ((gi >> 4) * 8 + (gi & 7)) + ((gi >> 3) & 1);
        const int f_s = W1P ? 2 * ((xi >> 4) * 8 + (xi & 7)) + ((xi >> 3) & 1) : xi;
#define GLOADS() do { \
        const bool isg = xi >= 48; \
        const rsrc_t rg = mkrs(isg ? wmod : w1, isg ? DM * 3840 * 2 : DM * DFF * 2); \
        const rsrc_t rc = mkrs(isg ? cc : x, DM * 2); \
        const u32 goff0 = isg ? (GY ? OOB : (u32)((t >> 2) * 3840 + 1280 + 32 * g_s + 8 * ck) * 2) : (u32)((w * 80 + rs) * DFF + 32 * f_s + 8 * ck) * 2; \
        const u32 gstr = isg ? (GY ? 0 : 128 * 7680) : 16 * DFF * 2; \
        const u32 coff0 = isg ? (u32)(t >> 2) * 2 : (u32)(w * 80 + rs) * 2; \
        const u32 cstr = isg ? 256 : 32; \
        _Pragma("unroll") for (int j = 0; j < 5; ++j) gw[j] = ld4(rg, goff0 + j * gstr, AUX_NT); \
        _Pragma("unroll") for (int j = 0; j < 5; ++j) cv[j] = ldh(rc, coff0 + j * cstr); \
        SB(); \
        } while (0)
#define GCOMP() do { \
        if (!GY && xi >= 48) { \
            float a[8]; \
_Pragma("unroll") \
            for (int i = 0; i < 8; ++i) a[i] = 0.f; \
_Pragma("unroll") \
            for (int j = 0; j < 5; ++j) { \
                float f[8]; unp8(gw[j], f); const float s = silu(bflo(cv[j])); \
_Pragma("unroll") \
                for (int i = 0; i < 8; ++i) a[i] += s * f[i]; \
            } \
            float v = rscat8(a, lane); \
            v += dpp<0x104>(v); \
            const int e = ((lane >> 5) & 1) * 4 + ((lane >> 4) & 1) * 2 + ((lane >> 3) & 1); \
            LAS float* redg = (LAS float*)(L + L_RG); \
            if (!(lane & 4)) redg[w * 32 + ck * 8 + e] = v; \
            BAR(); \
            if (w == 0 && lane < 16) { \
                f32x2 s = {0.f, 0.f}; \
_Pragma("unroll") \
                for (int i = 0; i < 8; ++i) s += *(LAS f32x2*)(redg + i * 32 + lane * 2); \
                st4(rws, OFF_G + (g_s * 16 + lane) * 16, u32x4{__float_as_uint(s.x), __float_as_uint(s.y), tlo, thi}); \
            } \
        } \
        } while (0)
        wait_until(tstart, D_G);
        GLOADS();
        wait_until(tstart, D_UV);
#pragma unroll
        for (int j = UVA; j < 12; ++j) vw[j] = ld3(ruv, wbase + j * 2 * 12288, AUX_NT);
        SB();
        STAMP(4);
        GCOMP();
        f32x2 vf[12][3];
#pragma unroll
        for (int j = 0; j < 12; ++j) { vf[j][0] = up2(vw[j].x); vf[j][1] = up2(vw[j].y); vf[j][2] = up2(vw[j].z); PIN1(vf[j][0]); PIN1(vf[j][1]); PIN1(vf[j][2]); }
        STAMP(7);
        {
            const u32 obase = OFF_O + (u32)(w * 16 + (lane >> 5)) * 49152 + (u32)(h * 4 + cq) * 384 + (lane & 31) * 12;
            constexpr u32 OJ = 2 * 49152;
            LAS float* red1 = (LAS float*)(L + L_RV1);
            LAS float* red2 = (LAS float*)(L + L_RV2);
            LAS f32x2* wl = (LAS f32x2*)(L + L_WL);
            u32x3 oa[8];
            {
                const u32 mlA = lane < 16 ? OFF_ML + (u32)(h * 128 + w * 16 + lane) * 16 : OOB;
                u32x4 ma = ld4(rws, mlA, AUX_SC1);
                if (anyl((lane < 16) & tagbad(ma, tlo, thi))) {
                    for (int it = 0; it < SPIN; ++it) { CFENCE();
                        if (ML_SLEEP > 0) __builtin_amdgcn_s_sleep(ML_SLEEP);
                        ma = ld4(rws, mlA, AUX_SC1);
                        if (!anyl((lane < 16) & tagbad(ma, tlo, thi))) break;
                    }
                }
                CFENCE();
                STAMP(8);
#pragma unroll
                for (int j = 0; j < 8; ++j) oa[j] = ld3(rws, obase + j * OJ, AUX_SC1);
                SB();
                if (lane < 16) wl[w * 16 + lane] = f32x2{__uint_as_float(ma.x), __uint_as_float(ma.y)};
            }
            BAR();
            float wA;
            {
                const f32x2 a = wl[lane], b = wl[lane + 64], own = wl[16 * w + 2 * (lane & 7) + (lane >> 5)];
                const float M = wave_max(fmaxf(a.x, b.x));
                const float Lsum = wave_sum(__builtin_amdgcn_exp2f(a.x - M) * a.y + __builtin_amdgcn_exp2f(b.x - M) * b.y);
                wA = __builtin_amdgcn_exp2f(own.x - M) * __builtin_amdgcn_rcpf(Lsum);
            }
            f32x2 pr[8][3];
#define OSTEP(j) { const f32x2 ww = splat(dpp<0x150 + j>(wA)); pr[j][0] = up2(oa[j].x) * ww; pr[j][1] = up2(oa[j].y) * ww; pr[j][2] = up2(oa[j].z) * ww; }
            OSTEP(0) OSTEP(1) OSTEP(2) OSTEP(3) OSTEP(4) OSTEP(5) OSTEP(6) OSTEP(7)
#undef OSTEP
            STAMP(9);
            f32x2 a2[3];
#pragma unroll
            for (int k = 0; k < 3; ++k) a2[k] = ((pr[0][k] + pr[1][k]) + (pr[2][k] + pr[3][k])) + ((pr[4][k] + pr[5][k]) + (pr[6][k] + pr[7][k]));
            {
                LAS float* dst = red1 + w * 192 + 6 * (lane & 31) + 3 * (lane >> 5);
                dst[0] = rs32(a2[0].x, a2[1].y); dst[1] = rs32(a2[0].y, a2[2].x); dst[2] = rs32(a2[1].x, a2[2].y);
            }
            STAMP2(1);
            BAR();
            STAMP2(2);
            float cx = 0.f;
            {
                const int i = lane & 15, r = 2 * (i < 12 ? i : 11) + (lane >> 5), cidx = (w >> 2) * 96 + (4 * (w & 3) + (r & 3)) * 6 + 2 * (r >> 3) + (((r >> 2) & 1) ^ ((w >> 1) & 1));
                float p[8];
#pragma unroll
                for (int k = 0; k < 8; ++k) p[k] = red1[k * 192 + cidx];
                cx = ((p[0] + p[1]) + (p[2] + p[3])) + ((p[4] + p[5]) + (p[6] + p[7]));
            }
            f32x2 c2[3] = {{0.f, 0.f}, {0.f, 0.f}, {0.f, 0.f}};
#define VSTEP(j) { const f32x2 cvv = splat(dpp<0x150 + j>(cx)); c2[0] += cvv * vf[j][0]; c2[1] += cvv * vf[j][1]; c2[2] += cvv * vf[j][2]; }
            VSTEP(0) VSTEP(1) VSTEP(2) VSTEP(3) VSTEP(4) VSTEP(5) VSTEP(6) VSTEP(7) VSTEP(8) VSTEP(9) VSTEP(10) VSTEP(11)
#undef VSTEP
            {
                LAS float* dst = red2 + w * 192 + 6 * (lane & 31) + 3 * (lane >> 5);
                dst[0] = rs32(c2[0].x, c2[1].y); dst[1] = rs32(c2[0].y, c2[2].x); dst[2] = rs32(c2[1].x, c2[2].y);
            }
            STAMP2(3);
            BAR();
            f32x2 s5 = {0.f, 0.f};
            if (w < 2) {
                f32x2 p[8];
#pragma unroll
                for (int k = 0; k < 8; ++k) p[k] = *(LAS f32x2*)(red2 + k * 192 + t * 2);
                s5 = ((p[0] + p[1]) + (p[2] + p[3])) + ((p[4] + p[5]) + (p[6] + p[7]));
            }
            st4(rws, t < 96 ? OFF_E5 + (u32)(cq * 3072 + h * 96 + t) * 16 : OOB, u32x4{__float_as_uint(s5.x), __float_as_uint(s5.y), tlo, thi});
        }
        BAR();
        STAMP(10);
        if (xi < 48) {
            LAS float* gs = (LAS float*)(L + L_GS);
            {
                u32x4 g0, g1;
                const bool two = t < 448;
                for (int it = 0; it < SPIN; ++it) { CFENCE();
                    g0 = ld4(rws, OFF_G + t * 16, AUX_SC1); bool bad = tagbad(g0, tlo, thi);
                    if (two) { g1 = ld4(rws, OFF_G + (t + 512) * 16, AUX_SC1); bad |= tagbad(g1, tlo, thi); }
                    if (!anyl(bad)) break; PSLEEP();
                }
                *(LAS u32x2*)(gs + 2 * t) = u32x2{g0.x, g0.y};
                if (two) *(LAS u32x2*)(gs + 2 * (t + 512)) = u32x2{g1.x, g1.y};
            }
            BAR();
            f32x2 wA[5][4], u[4], v1[4], v2[4];
            float xv[5], ga[5];
#pragma unroll
            for (int k = 0; k < 4; ++k) { u[k] = f32x2{0.f, 0.f}; v1[k] = f32x2{0.f, 0.f}; v2[k] = f32x2{0.f, 0.f}; }
#pragma unroll
            for (int j = 0; j < 5; ++j) {
                const int m = w * 80 + 16 * j + rs;
                const float gav = gs[m], sbv = gs[640 + m], cbp = 1.f + gs[1280 + m];
                xv[j] = bflo(cv[j]); ga[j] = gav;
                const f32x2 A = splat(gav * cbp), X0 = splat(xv[j] * cbp), C = splat(cbp), S = splat(sbv);
                const f32x2 f[4] = {up2(gw[j].x), up2(gw[j].y), up2(gw[j].z), up2(gw[j].w)};
#pragma unroll
                for (int k = 0; k < 4; ++k) { wA[j][k] = A * f[k]; u[k] += X0 * f[k]; v1[k] += C * f[k]; v2[k] += S * f[k]; PIN1(wA[j][k]); }
            }
            const int e = ((lane >> 5) & 1) * 4 + ((lane >> 4) & 1) * 2 + ((lane >> 3) & 1);
            LAS float* rp = (LAS float*)(L + L_RP);
            {
                float a[8] = {v1[0].x, v1[0].y, v1[1].x, v1[1].y, v1[2].x, v1[2].y, v1[3].x, v1[3].y};
                float v = rscat8(a, lane); v += dpp<0x104>(v);
                if (!(lane & 4)) rp[w * 32 + ck * 8 + e] = v;
                float b[8] = {v2[0].x, v2[0].y, v2[1].x, v2[1].y, v2[2].x, v2[2].y, v2[3].x, v2[3].y};
                v = rscat8(b, lane); v += dpp<0x104>(v);
                if (!(lane & 4)) rp[256 + w * 32 + ck * 8 + e] = v;
            }
            BAR();
            f32x2 V1 = {0.f, 0.f}, V2 = {0.f, 0.f};
            if (w == 0) {
#pragma unroll
                for (int k = 0; k < 8; ++k) { V1 += *(LAS f32x2*)(rp + k * 32 + (lane & 15) * 2); V2 += *(LAS f32x2*)(rp + 256 + k * 32 + (lane & 15) * 2); }
                PIN1(V1); PIN1(V2);
            }
#pragma unroll
            for (int k = 0; k < 4; ++k) PIN1(u[k]);
            STAMP(11);
            LAS float* r6 = (LAS float*)(L + L_R6);
            {
                const bool act = t < 480;
                const int pg = t / 160, q = t - pg * 160;
                const u32 goff = OFF_E6 + (u32)(4 * pg * 640 + 4 * q) * 4;
                u32x4 eg[4];
#pragma unroll
                for (int k = 0; k < 4; ++k) eg[k] = u32x4{0u, 0u, 0u, 0u};
                for (int it = 0; it < SPIN; ++it) { CFENCE();
                    if (act) {
#pragma unroll
                        for (int k = 0; k < 4; ++k) eg[k] = ld4(rws, goff + k * 2560, AUX_SC1);
                    }
                    if (!anyl(pois4(eg[0]) | pois4(eg[1]) | pois4(eg[2]) | pois4(eg[3]))) break; PSLEEP();
                }
                const f32x4 s = (__builtin_bit_cast(f32x4, eg[0]) + __builtin_bit_cast(f32x4, eg[1])) + (__builtin_bit_cast(f32x4, eg[2]) + __builtin_bit_cast(f32x4, eg[3]));
                if (act) *(LAS f32x4*)(r6 + pg * 640 + 4 * q) = s;
            }
            STAMP(12);
            BAR();
            STAMP2(5);
            float x1[5], sum = 0.f;
#pragma unroll
            for (int j = 0; j < 5; ++j) {
                const int m = w * 80 + 16 * j + rs;
                const float at = (r6[m] + r6[640 + m]) + r6[1280 + m];
                x1[j] = xv[j] + ga[j] * at; sum += x1[j];
                const f32x2 A = splat(at);
#pragma unroll
                for (int k = 0; k < 4; ++k) u[k] += A * wA[j][k];
            }
            sum += dpp<0x124>(sum); sum += dpp<0x128>(sum); sum = rs16(sum, sum); sum = rs32(sum, sum);
            const float mw_ = sum * (1.f / 80.f);
            float q = 0.f;
#pragma unroll
            for (int j = 0; j < 5; ++j) { const float d = x1[j] - mw_; q += d * d; }
            q += dpp<0x124>(q); q += dpp<0x128>(q); q = rs16(q, q); q = rs32(q, q);
            {
                float a[8] = {u[0].x, u[0].y, u[1].x, u[1].y, u[2].x, u[2].y, u[3].x, u[3].y};
                float v = rscat8(a, lane); v += dpp<0x104>(v);
                LAS float* rf = (LAS float*)(L + L_RF);
                if (!(lane & 4)) rf[w * 32 + ck * 8 + e] = v;
                LAS float* sst = (LAS float*)(L + L_ST);
                if (lane == 0) *(LAS f32x2*)(sst + 2 * w) = f32x2{mw_, q};
                STAMP2(4);
                BAR();
                if (w == 0 && lane < 16) {
                    f32x2 p[8], s8[8];
#pragma unroll
                    for (int k = 0; k < 8; ++k) { p[k] = *(LAS f32x2*)(rf + k * 32 + lane * 2); s8[k] = *(LAS f32x2*)(sst + 2 * k); }
                    const f32x2 U = ((p[0] + p[1]) + (p[2] + p[3])) + ((p[4] + p[5]) + (p[6] + p[7]));
                    const f32x2 SS = ((s8[0] + s8[1]) + (s8[2] + s8[3])) + ((s8[4] + s8[5]) + (s8[6] + s8[7]));
                    const float mean = SS.x * 0.125f;
                    float dv = 0.f;
#pragma unroll
                    for (int k = 0; k < 8; ++k) { const float d = s8[k].x - mean; dv += d * d; }
                    const float var = (SS.y + 80.f * dv) * (1.f / 640.f);
                    const float rstd = __builtin_amdgcn_rsqf(var + 1e-5f);
                    const float h0 = fmaxf(rstd * (U.x - mean * V1.x) + V2.x, 0.f), h1 = fmaxf(rstd * (U.y - mean * V1.y) + V2.y, 0.f);
                    st4(rws, OFF_HID + (u32)(f_s * 16 + lane) * 16, u32x4{__float_as_uint(h0), __float_as_uint(h1), tlo, thi});
                }
                STAMP2(6);
            }

        } else if (xi < 88) {
            const int fi = xi - 48, s2 = 5 * (fi & 7) + (fi >> 3);
            const rsrc_t r2 = mkrs(w2, DFF * DM * 2);
            const int chunk = t & 1, q = t >> 1;
            u32x4 fw[6];
#pragma unroll
            for (int i = 0; i < 3; ++i)
#pragma unroll
                for (int e2 = 0; e2 < 2; ++e2) fw[2 * i + e2] = ld4(r2, (u32)((2 * (q + 256 * i) + e2) * DM + 16 * s2 + 8 * chunk) * 2, AUX_NT);
            SB();
            f32x4 x1 = {0.f, 0.f, 0.f, 0.f}, gb = {0.f, 0.f, 0.f, 0.f};
            if (w == 0) {
                const int c4 = lane & 3, p = lane >> 2;
                const u32x2 xx = *(const u32x2*)(x + 16 * s2 + 4 * c4);
                u32x4 ga0, ga1, gb0, gb1;
                for (int it = 0; it < SPIN; ++it) { CFENCE();
                    ga0 = ld4(rws, OFF_G + (u32)(8 * s2 + 2 * c4) * 16, AUX_SC1); ga1 = ld4(rws, OFF_G + (u32)(8 * s2 + 2 * c4 + 1) * 16, AUX_SC1);
                    gb0 = ld4(rws, OFF_G + (u32)(960 + 8 * s2 + 2 * c4) * 16, AUX_SC1); gb1 = ld4(rws, OFF_G + (u32)(960 + 8 * s2 + 2 * c4 + 1) * 16, AUX_SC1);
                    if (!anyl(tagbad(ga0, tlo, thi) | tagbad(ga1, tlo, thi) | tagbad(gb0, tlo, thi) | tagbad(gb1, tlo, thi))) break; PSLEEP();
                }
                u32x4 g = {0u, 0u, 0u, 0u};
                for (int it = 0; it < SPIN; ++it) { CFENCE();
                    if (p < 12) g = ld4(rws, OFF_E6 + (u32)(p * 640 + 16 * s2 + 4 * c4) * 4, AUX_SC1);
                    if (!anyl(pois4(g))) break; PSLEEP();
                }
                f32x4 at = __builtin_bit_cast(f32x4, g);
#pragma unroll
                for (int k = 0; k < 4; ++k) { float v = at[k]; v += dpp<0x124>(v); v += dpp<0x128>(v); v = rs16(v, v); v = rs32(v, v); at[k] = v; }
                x1 = f32x4{bflo(xx.x) + __uint_as_float(ga0.x) * at.x, bfhi(xx.x) + __uint_as_float(ga0.y) * at.y, bflo(xx.y) + __uint_as_float(ga1.x) * at.z, bfhi(xx.y) + __uint_as_float(ga1.y) * at.w};
                gb = f32x4{__uint_as_float(gb0.x), __uint_as_float(gb0.y), __uint_as_float(gb1.x), __uint_as_float(gb1.y)};
                asm volatile("" : "+v"(x1), "+v"(gb));
            }
            f32x2 ff[6][4];
#pragma unroll
            for (int j = 0; j < 6; ++j) { ff[j][0] = up2(fw[j].x); ff[j][1] = up2(fw[j].y); ff[j][2] = up2(fw[j].z); ff[j][3] = up2(fw[j].w); PIN1(ff[j][0]); PIN1(ff[j][1]); PIN1(ff[j][2]); PIN1(ff[j][3]); }
            STAMP(11);
            u32x4 hg[3];
            for (int it = 0; it < SPIN; ++it) { CFENCE();
#pragma unroll
                for (int i = 0; i < 3; ++i) hg[i] = ld4(rws, OFF_HID + (u32)(q + 256 * i) * 16, AUX_SC1);
                if (!anyl(tagbad(hg[0], tlo, thi) | tagbad(hg[1], tlo, thi) | tagbad(hg[2], tlo, thi))) break; PSLEEP();
            }
            STAMP(12);
            f32x2 acc[4] = {{0.f, 0.f}, {0.f, 0.f}, {0.f, 0.f}, {0.f, 0.f}};
#pragma unroll
            for (int i = 0; i < 3; ++i) {
                const f32x2 h0 = splat(__uint_as_float(hg[i].x)), h1 = splat(__uint_as_float(hg[i].y));
#pragma unroll
                for (int k = 0; k < 4; ++k) { acc[k] += h0 * ff[2 * i][k]; acc[k] += h1 * ff[2 * i + 1][k]; }
            }
            float a[8] = {acc[0].x, acc[0].y, acc[1].x, acc[1].y, acc[2].x, acc[2].y, acc[3].x, acc[3].y};
            float v = rscat8(a, lane);
            v += dpp<0x4E>(v); v += dpp<0x104>(v);
            const int e = ((lane >> 5) & 1) * 4 + ((lane >> 4) & 1) * 2 + ((lane >> 3) & 1);
            LAS float* r2l = (LAS float*)(L + L_R2);
            if (!(lane & 6)) r2l[w * 16 + chunk * 8 + e] = v;
            STAMP2(5);
            BAR();
            STAMP2(4);
            if (t < 4) {
                f32x4 p[8];
#pragma unroll
                for (int k = 0; k < 8; ++k) p[k] = *(LAS f32x4*)(r2l + k * 16 + t * 4);
                const f32x4 s = ((p[0] + p[1]) + (p[2] + p[3])) + ((p[4] + p[5]) + (p[6] + p[7]));
                const f32x4 o = x1 + gb * s;
                *(u32x2*)(out + 16 * s2 + 4 * t) = u32x2{cvtpk(o.x, o.y), cvtpk(o.z, o.w)};
            }
            if (xi == 48 && t == 0) __hip_atomic_store((GAS u64*)(ws + OFF_EPOCH), epoch + 1ull, __ATOMIC_RELAXED, __HIP_MEMORY_SCOPE_AGENT);

        }
    } else {
        const int yi = bid - 128;
        const bool isM = yi < 80;
        const unsigned char* src = (const unsigned char*)ckv + (size_t)yi * 64 * 1536;
        u32 koff[12];
#define KOFF() _Pragma("unroll") for (int j = 0; j < 12; ++j) { const int idx = j * 64 + lane, row = w * 8 + idx / 96, p = idx % 96; const int ch = (p & ~15) | ((p ^ row) & 15); koff[j] = (u32)(row * 1536 + ch * 16); PIN1(koff[j]); }
        u32x4 mw[3]; u32 mc[3]; u32 xr[5], xm = 0;
        const int m_rg = yi & 7, m_s = yi >> 3;
        if (isM) {
            const rsrc_t rmod = mkrs(wmod, DM * 3840 * 2);
            const int row0 = m_rg * 80 + w * 10 + (lane >> 4);
            const u32 off = (u32)(row0 * 3840 + ((lane >> 3) & 1) * 640 + m_s * 64 + (lane & 7) * 8) * 2;
            mw[0] = ld4(rmod, off, AUX_NT); mw[1] = ld4(rmod, off + 4 * 7680, AUX_NT); mw[2] = ld4(rmod, lane < 32 ? off + 8 * 7680 : OOB, AUX_NT);
            const rsrc_t rc = mkrs(cc, DM * 2);
            mc[0] = ldh(rc, row0 * 2); mc[1] = ldh(rc, row0 * 2 + 8); mc[2] = ldh(rc, row0 * 2 + (lane < 32 ? 16 : 0));
            if (w == 0) {
#pragma unroll
                for (int i = 0; i < 5; ++i) xr[i] = ((const u32*)x)[lane + 64 * i];
                xm = ((const u32*)x)[32 * m_s + (lane & 31)];
            }
        }
        SB();
        KOFF()
        if (CKV1 > 0) {
            wait_until(tstart, D_CKV);
#pragma unroll
            for (int j = 0; j < CKV1; ++j) dma16nt(src, koff[j], lbase + L_KV + (u32)(w * 12 + j) * 1024);
            SB();
        }
        GETTAG()
        if (isM) {
            f32x2 lnx = {0.f, 0.f};
            if (w == 0) {
                float xv[10];
#pragma unroll
                for (int i = 0; i < 5; ++i) { xv[2 * i] = bflo(xr[i]); xv[2 * i + 1] = bfhi(xr[i]); }
                float mean, rstd; ln_stats(xv, mean, rstd);
                lnx = f32x2{(bflo(xm) - mean) * rstd, (bfhi(xm) - mean) * rstd};
            }
            float a[8], f[8];
            const float s0 = silu(bflo(mc[0])), s1 = silu(bflo(mc[1])), s2 = silu(bflo(mc[2]));
            unp8(mw[0], f);
#pragma unroll
            for (int i = 0; i < 8; ++i) a[i] = s0 * f[i];
            unp8(mw[1], f);
#pragma unroll
            for (int i = 0; i < 8; ++i) a[i] += s1 * f[i];
            unp8(mw[2], f);
#pragma unroll
            for (int i = 0; i < 8; ++i) a[i] += s2 * f[i];
            const float r0 = rs32(a[0], a[4]), r1 = rs32(a[1], a[5]), r2 = rs32(a[2], a[6]), r3 = rs32(a[3], a[7]);
            const f32x2 sv = {rs16(r0, r2), rs16(r1, r3)};
            LAS float* red = (LAS float*)(L + L_RM);
            *(LAS f32x2*)(red + w * 128 + ((lane >> 3) & 1) * 64 + (lane & 7) * 8 + 4 * (lane >> 5) + 2 * ((lane >> 4) & 1)) = sv;
            BAR();
            if (w == 0) {
                f32x2 p[8];
#pragma unroll
                for (int k = 0; k < 8; ++k) p[k] = *(LAS f32x2*)(red + k * 128 + 2 * lane);
                const f32x2 s = ((p[0] + p[1]) + (p[2] + p[3])) + ((p[4] + p[5]) + (p[6] + p[7]));
                auto rx = __builtin_amdgcn_permlane32_swap(__float_as_uint(s.x), __float_as_uint(s.x), false, false);
                auto ry = __builtin_amdgcn_permlane32_swap(__float_as_uint(s.y), __float_as_uint(s.y), false, false);
                float hx = lnx.x * __uint_as_float(rx[1]) + __uint_as_float(rx[0]), hy = lnx.y * __uint_as_float(ry[1]) + __uint_as_float(ry[0]);
                if (m_rg == 0) { hx += lnx.x; hy += lnx.y; }
                const int g = 32 * m_s + lane, gm = g / 80, gr = g - gm * 80, gw_ = gr / 10;
                if (lane < 32) st4(rws, OFF_E1 + (u32)(((m_rg * 4 + gm) * 8 + gw_) * 16 + (gr - gw_ * 10)) * 16, u32x4{__float_as_uint(hx), __float_as_uint(hy), tlo, thi});
            }
            BAR();
        }
        STAMP(1);
        wait_until(tstart, isM ? D_CKV : D_CKV2);
        const int kq = w >> 1, ht = w & 1;
        const u32 qoff = OFF_E3 + (u32)(kq * 12 * 32 + ht * 16 + (lane & 15)) * 64 + (lane >> 4) * 16;
        const u32 pfoff = OFF_PF + (u32)(w * 16 + (lane & 15)) * 16;
        u32x4 pf, sent;
#pragma unroll
        for (int j = CKV1; j < 12; ++j) {
            dma16nt(src, koff[j], lbase + L_KV + (u32)(w * 12 + j) * 1024);
            if (j == JPF) pf = ald4(rws4, pfoff);
            if (j - CKV1 >= YW) vmwait<YW>();
        }
        SB();
        STAMP(2);
        await<11 - JPF>(pf);
        if (anyl(tagbad(pf, tlo, thi))) {
            for (int it = 0; it < SPIN; ++it) { CFENCE();
                pf = ald4(rws4, pfoff); await<0>(pf);
                if (!anyl(tagbad(pf, tlo, thi))) break;
            }
        }
        const u32 opz = OFF_O + (u32)yi * 49152 + t * 16;
        const int wrg = yi % 12, wcs = yi / 12;
#define YPOISON() do { \
            st4(rws, (t < 16 && yi < 120) ? OFF_E6 + (u32)(wrg * 640 + wcs * 64) * 4 + lane * 16 : OOB, PZ); \
            SB(); } while (0)
        wait_until(tstart, D_S);
        sent = ald4(rws4, qoff + 11 * 2048);
        await<0>(sent);
        BAR();
        STAMP(3);
        u32x4 ow[8];
        const rsrc_t ro = mkrs(wo, 6144 * DM * 2);
        const u32 wobase = yi < 120 ? (u32)((wrg * 512 + w * 64 + (lane >> 3) * 8) * DM + wcs * 64 + (lane & 7) * 8) * 2 : OOB;
        u32x4 pf2;
        const u32 pf2off = OFF_PF2 + (u32)(kq * 32 + (lane & 31)) * 16;
        u32x4 va[6][2];
        {
            u32x4 qf[12];
            if (anyl(pois4(sent))) {
                for (int i2 = 0; i2 < SPIN; ++i2) { CFENCE();
                    sent = ald4(rws4, qoff + 11 * 2048); await<0>(sent);
                    if (!anyl(pois4(sent))) break; PSLEEP();
                }
            }
            STAMP(5);
            qf[11] = sent;
#pragma unroll
            for (int j = 0; j < 11; ++j) qf[j] = ld4(rws, qoff + j * 2048, AUX_SC1);
            SB();
            YPOISON();
            const int r = lane & 15;
            const int keyr = 8 * (r >> 3) + 2 * (r & 3) + ((r >> 2) & 1);
            u32x4 af[6][4];
#pragma unroll
            for (int ks = 0; ks < 6; ++ks) {
                const int ch = kq * 24 + ks * 4 + (lane >> 4);
                const int p = (ch & ~15) | ((ch ^ keyr) & 15);
#pragma unroll
                for (int kt = 0; kt < 4; ++kt) { af[ks][kt] = *(LAS u32x4*)(L + L_KV + (kt * 16 + keyr) * 1536 + p * 16); }
            }
            SB();
#define VAREADS() do { \
            { \
                const int g = lane >> 4, e = (lane >> 2) & 3, pp = lane & 3; \
_Pragma("unroll") \
                for (int q = 0; q < 6; ++q) { \
                    const int ch = 12 * w + 3 * pp + (q >> 1), hf = (q ^ (pp >> 1)) & 1; \
_Pragma("unroll") \
                    for (int k2 = 0; k2 < 2; ++k2) { \
                        const int ka = k2 * 32 + 8 * g + 2 * e, kb = ka + 1; \
                        const int pa = (ch & ~15) | ((ch ^ ka) & 15), pbx = (ch & ~15) | ((ch ^ kb) & 15); \
                        const s16x4 t0 = __builtin_amdgcn_ds_read_tr16_b64_v4i16((LAS s16x4*)(L + L_KV + ka * 1536 + pa * 16 + hf * 8)); \
                        const s16x4 t1 = __builtin_amdgcn_ds_read_tr16_b64_v4i16((LAS s16x4*)(L + L_KV + kb * 1536 + pbx * 16 + hf * 8)); \
                        const u32x2 a0 = __builtin_bit_cast(u32x2, t0), a1 = __builtin_bit_cast(u32x2, t1); \
                        va[q][k2] = u32x4{a0.x, a0.y, a1.x, a1.y}; \
                    } \
                } \
            } \
            } while (0)
            f32x4 acc[4];
            u32 pm;
#define QKPASS() do { \
            _Pragma("unroll") for (int kt = 0; kt < 4; ++kt) acc[kt] = f32x4{0.f, 0.f, 0.f, 0.f}; \
            pm = 0; \
            _Pragma("unroll") for (int j = 0; j < 12; ++j) { \
                _Pragma("unroll") for (int kt = 0; kt < 4; ++kt) acc[kt] = mfma(af[j >> 1][kt], qf[j], acc[kt]); \
                pm = max(pm, max(max(qf[j].x, qf[j].y), max(qf[j].z, qf[j].w))); \
            } } while (0)
            QKPASS();
            if (anyl(pm == POISON)) {
                for (int it = 0; it < SPIN; ++it) { CFENCE();
#pragma unroll
                    for (int j = 0; j < 12; ++j) qf[j] = ld4(rws, qoff + j * 2048, AUX_SC1);
                    SB();
                    QKPASS();
                    if (!anyl(pm == POISON)) break;
                }
            }
            STAMP(6);
#pragma unroll
            for (int j = 0; j < WO1; ++j) ow[j] = ld4(ro, wobase + j * 1280, AUX_NT);
            SB();
            LAS float* sp = (LAS float*)(L + L_SP);
#pragma unroll
            for (int kt = 0; kt < 4; ++kt) *(LAS f32x4*)(sp + (kq * 32 + ht * 16 + (lane & 15)) * 68 + kt * 16 + 4 * (lane >> 4)) = acc[kt];
            SB();
            VAREADS();
            SB();
        }
        BAR();
        float sm_m, sm_l;
        {
            LAS float* sp = (LAS float*)(L + L_SP);
            const int hd = t >> 4, qd = t & 15;
            f32x4 s = *(LAS f32x4*)(sp + hd * 68 + qd * 4);
#pragma unroll
            for (int k = 1; k < 4; ++k) s += *(LAS f32x4*)(sp + (k * 32 + hd) * 68 + qd * 4);
            const float m = row_max(fmaxf(fmaxf(s.x, s.y), fmaxf(s.z, s.w)));
            const float p0 = __builtin_amdgcn_exp2f(s.x - m), p1 = __builtin_amdgcn_exp2f(s.y - m), p2 = __builtin_amdgcn_exp2f(s.z - m), p3 = __builtin_amdgcn_exp2f(s.w - m);
            const u32 b01 = cvtpk(p0, p1), b23 = cvtpk(p2, p3);
            *(LAS u32x2*)(L + L_P + hd * 144 + qd * 8) = u32x2{b01, b23};
            const float l = row_sum((bflo(b01) + bfhi(b01)) + (bflo(b23) + bfhi(b23)));
            sm_m = m; sm_l = l;
        }
        BAR();
        STAMP(7);
        {
            u32x4 pb[2][2];
#pragma unroll
            for (int hh = 0; hh < 2; ++hh)
#pragma unroll
                for (int k2 = 0; k2 < 2; ++k2) pb[hh][k2] = *(LAS u32x4*)(L + L_P + (hh * 16 + (lane & 15)) * 144 + k2 * 64 + (lane >> 4) * 16);
            STAMP(4);
            {
                const u32 ob2 = OFF_O + (u32)yi * 49152 + (u32)(lane >> 4) * 4 * 1536 + (u32)w * 192 + (lane & 15) * 12;
#pragma unroll
                for (int hh = 0; hh < 2; ++hh) {
                    f32x4 d[6];
#pragma unroll
                    for (int q = 0; q < 6; ++q) { d[q] = mfma(pb[hh][0], va[q][0], f32x4{0.f, 0.f, 0.f, 0.f}); }
#pragma unroll
                    for (int q = 0; q < 6; ++q) { d[q] = mfma(pb[hh][1], va[q][1], d[q]); }
#pragma unroll
                    for (int e = 0; e < 4; ++e)
                        __builtin_amdgcn_raw_buffer_store_b96(u32x3{cvtpk(d[0][e], d[1][e]), cvtpk(d[2][e], d[3][e]), cvtpk(d[4][e], d[5][e])}, rws, ob2 + (hh * 16 + e) * 1536, 0, AUX_SC1);
                }
            }
            SB();
#pragma unroll
            for (int j = 0; j < WOE; ++j) ow[WO1 + j] = ld4(ro, wobase + (WO1 + j) * 1280, AUX_NT);
            SB();
            vmwait<WOE>();
            STAMP(8);
            asm volatile("s_barrier" ::: "memory");
            st4(rws, (t & 15) == 0 ? OFF_ML + (u32)((t >> 4) * 128 + yi) * 16 : OOB, u32x4{__float_as_uint(sm_m), __float_as_uint(sm_l), tlo, thi});
            SB();
        }
        STAMP(9);
        if (yi < 120) {
#pragma unroll
            for (int j = WO1 + FLAGO * WOE; j < 8; ++j) { if (j == WO1 + FLAGO * WOE) wait_until(tstart, D_WO); ow[j] = ld4(ro, wobase + j * 1280, AUX_NT); }
            SB();
            f32x2 of[8][4];
#pragma unroll
            for (int j = 0; j < 8; ++j) { of[j][0] = up2(ow[j].x); of[j][1] = up2(ow[j].y); of[j][2] = up2(ow[j].z); of[j][3] = up2(ow[j].w); PIN1(of[j][0]); PIN1(of[j][1]); PIN1(of[j][2]); PIN1(of[j][3]); }
            STAMP(10);
            const int cc8 = lane & 7;
            const u32 goff = OFF_E5 + (u32)((cc8 < 4 ? 0 : 2) * 3072 + wrg * 256 + w * 32 + (lane >> 3) * 4 + (cc8 < 4 ? cc8 : 7 - cc8)) * 16;
            u32x4 g0, g1;
            for (int it = 0; it < SPIN; ++it) { CFENCE();
                g0 = ld4(rws, goff, AUX_SC1); g1 = ld4(rws, goff + 3072 * 16, AUX_SC1);
                if (!anyl(tagbad(g0, tlo, thi) | tagbad(g1, tlo, thi))) break; PSLEEP();
            }
            STAMP(11);
            float vx = __uint_as_float(g0.x) + __uint_as_float(g1.x), vy = __uint_as_float(g0.y) + __uint_as_float(g1.y);
            vx += dpp<0x141>(vx); vy += dpp<0x141>(vy);
            vx = dppb<0x1B, 0xA>(vx, vx); vy = dppb<0x1B, 0xA>(vy, vy);
            f32x2 a2[4] = {{0.f, 0.f}, {0.f, 0.f}, {0.f, 0.f}, {0.f, 0.f}};
#define WSTEP(k) { const f32x2 hx = splat(dpp<k * 0x55>(vx)), hy = splat(dpp<k * 0x55>(vy)); _Pragma("unroll") for (int i = 0; i < 4; ++i) { a2[i] += hx * of[2 * k][i]; a2[i] += hy * of[2 * k + 1][i]; } }
            WSTEP(0) WSTEP(1) WSTEP(2) WSTEP(3)
#undef WSTEP
            float a[8] = {a2[0].x, a2[0].y, a2[1].x, a2[1].y, a2[2].x, a2[2].y, a2[3].x, a2[3].y};
            const float v = rscat8(a, lane);
            const int e = ((lane >> 5) & 1) * 4 + ((lane >> 4) & 1) * 2 + ((lane >> 3) & 1);
            LAS float* red = (LAS float*)(L + L_RW);
            red[w * 64 + (lane & 7) * 8 + e] = v;
            BAR();
            if (w == 0 && lane < 16) {
                f32x4 p[8];
#pragma unroll
                for (int k = 0; k < 8; ++k) p[k] = *(LAS f32x4*)(red + k * 64 + lane * 4);
                const f32x4 s4 = ((p[0] + p[1]) + (p[2] + p[3])) + ((p[4] + p[5]) + (p[6] + p[7]));
                st4(rws, OFF_E6 + (u32)(wrg * 640 + wcs * 64) * 4 + lane * 16, u32x4{canonf(s4.x), canonf(s4.y), canonf(s4.z), canonf(s4.w)});
            }
            STAMP(12);
        }
    }
}

extern "C" void kernel_launch(void* const* d_in, const int* in_sizes, int n_in, void* d_out, int out_size, void* d_ws, size_t ws_size, hipStream_t stream) {
    (void)in_sizes; (void)n_in; (void)out_size; (void)ws_size;
    hipFuncSetAttribute((const void*)adaln_mla_mlp_kernel, hipFuncAttributeMaxDynamicSharedMemorySize, LDS_BYTES);
    adaln_mla_mlp_kernel<<<NWG, NT, LDS_BYTES, stream>>>(
        (const u16*)d_in[0], (const u16*)d_in[1], (const u16*)d_in[2], (const u16*)d_in[3], (const u16*)d_in[4], (const u16*)d_in[5],
        (const u16*)d_in[7], (const u16*)d_in[8], (const u16*)d_in[9], (const u16*)d_in[10], (u16*)d_out, (unsigned char*)d_ws);
}
```

```cpp
#include <hip/hip_runtime.h>
#include <stdint.h>

typedef unsigned u32;
typedef unsigned long long u64;
typedef unsigned short u16;
typedef u32 u32x2 __attribute__((ext_vector_type(2)));
typedef u32 u32x3 __attribute__((ext_vector_type(3)));
typedef u32 u32x4 __attribute__((ext_vector_type(4)));
typedef float f32x2 __attribute__((ext_vector_type(2)));
typedef float f32x4 __attribute__((ext_vector_type(4)));
typedef short bf16x8 __attribute__((ext_vector_type(8)));
typedef short s16x4 __attribute__((ext_vector_type(4)));
typedef __amdgpu_buffer_rsrc_t rsrc_t;
#define LAS __attribute__((address_space(3)))
#define GAS __attribute__((address_space(1)))

constexpr int NH = 32, SK = 8192, HD = 192, DM = 640, DC = 768, DFF = 1536;
constexpr int NWG = 256, NT = 512;

#pragma clang diagnostic ignored "-Winline-asm"
#define UK1 4
#define D_WQ 0
#define D_CKV 350
#define D_CKV2 350
#define CKV1 0
#define D_PO 560
#define D_UV 730
#define XW 4
#define YW 3
#define D_E1 260
#define D_E2 40
#define JPF 7
#define ML_SLEEP 4
#define WO1 0
#define FIX1 1
#define FIX2 0
#define GPOS 3
#define UV1 12
#define GY 0
#define D_S 670
#define FULLG 0
#define DIRO 2
#define VAPOS 1
#define PINAF 0
#define YPOIS 0
#define DUPKV 0
#define UVA 0
#define D_G 0
#define W1P 1
#define D_WO 1290
#define PFM 0
#define D_PW 980
#define WOA 0
#define D_WA 0
#define QLIN 0
#define D_ML 1180
#define D_E5 1450
#define D_E6 1600
#define D_H 1780
#define W2P 0
#define D_W2 0
#define W2A 2
#define W1A 2
#define F2MAP 0
#define TLBW 0
#define E6D 2
#define LEAN3 1
#define F2N 4
#define FLAGO 1
#define WOE 0
constexpr u32 OFF_EPOCH = 0;
constexpr u32 OFF_PF = 256;
constexpr u32 OFF_PF2 = OFF_PF + 128 * 16;
constexpr u32 OFF_E1 = OFF_PF2 + 128 * 16;
constexpr u32 OFF_E2 = OFF_E1 + 8 * 4 * 8 * 256;
constexpr u32 OFF_E3 = OFF_E2 + 4 * 32 * 96 * 16;
constexpr u32 OFF_ML = OFF_E3 + 32 * 4 * 768;
constexpr u32 OFF_E5 = OFF_ML + 32 * 128 * 16;
constexpr u32 OFF_E6 = OFF_E5 + 4 * 3072 * 16;
constexpr u32 OFF_G = OFF_E6 + 12 * 640 * 4;
constexpr u32 OFF_HID = OFF_G + 1280 * 16;
constexpr u32 OFF_O = OFF_HID + 768 * 16;
constexpr u32 OFF_PP = OFF_O + 32 * 4 * 128 * 384;
constexpr u32 WS_BYTES = OFF_PP + 48 * 320 * 16;
constexpr u32 OFF_DIAG = WS_BYTES;
static_assert(OFF_O % 128 == 0 && OFF_E3 % 128 == 0 && OFF_E5 % 128 == 0 && OFF_E2 % 256 == 0 && OFF_E6 % 128 == 0 && OFF_E1 % 256 == 0, "alignment");

constexpr int L_KV = 0;
constexpr int OSTR = 1552;
constexpr int L_SP = 98304;
constexpr int L_P = L_SP + 4 * 32 * 68 * 4;
constexpr int L_RM = L_P + 32 * 144;
constexpr int L_RW = L_RM + 8 * 128 * 4;
constexpr int L_ML = L_RW + 8 * 64 * 4;
constexpr int LDS_BYTES = L_ML + 256;
constexpr int L_RQ = 0;
constexpr int L_Q = L_RQ + 6144;
constexpr int L_RA = L_Q + 768;
constexpr int L_WL = L_RA + 768;
constexpr int L_RV1 = L_WL + 1024;
constexpr int L_RV2 = L_RV1 + 6144;
constexpr int L_RG = L_RV2 + 6144;
constexpr int L_GS = L_RG + 1024;
constexpr int L_RP = L_GS + 7680;
constexpr int L_RF = L_RP + 2048;
constexpr int L_ST = L_RF + 1024;
constexpr int L_R2 = L_ST + 64;
constexpr int L_R6 = L_R2 + 512;
constexpr int SPIN = 1 << 16;
constexpr u32 POISON = 0xFFFFFFFFu;
constexpr u32 OOB = 0x80000000u;
constexpr int AUX_SC1 = 16, AUX_NT = 2;
constexpr u64 TAGMUL = 0x9E3779B97F4A7C15ull;

__device__ __forceinline__ float bflo(u32 v) { return __uint_as_float(v << 16); }
__device__ __forceinline__ float bfhi(u32 v) { return __uint_as_float(v & 0xFFFF0000u); }
__device__ __forceinline__ float bf1(u16 v) { return __uint_as_float((u32)v << 16); }
typedef __bf16 bf16x2_t __attribute__((ext_vector_type(2)));
__device__ __forceinline__ u32 cvtpk(float lo, float hi) { f32x2 v = {lo, hi}; return __builtin_bit_cast(u32, __builtin_convertvector(v, bf16x2_t)); }
__device__ __forceinline__ u32 canon(u32 v) { return v < 0xFFFFFFFEu ? v : 0xFFFFFFFEu; }
__device__ __forceinline__ u32 canonf(float v) { return canon(__float_as_uint(v)); }
__device__ __forceinline__ rsrc_t mkrs(const void* p, u32 bytes) { return __builtin_amdgcn_make_buffer_rsrc((void*)p, 0, bytes, 0x00020000); }
__device__ __forceinline__ u32x4 ld4(rsrc_t r, u32 off, int aux) { return aux == AUX_SC1 ? __builtin_amdgcn_raw_buffer_load_b128(r, off, 0, AUX_SC1) : __builtin_amdgcn_raw_buffer_load_b128(r, off, 0, AUX_NT); }
__device__ __forceinline__ u32x3 ld3(rsrc_t r, u32 off, int aux) { return aux == AUX_SC1 ? __builtin_amdgcn_raw_buffer_load_b96(r, off, 0, AUX_SC1) : __builtin_amdgcn_raw_buffer_load_b96(r, off, 0, AUX_NT); }
__device__ __forceinline__ void st4(rsrc_t r, u32 off, u32x4 v) { __builtin_amdgcn_raw_buffer_store_b128(v, r, off, 0, AUX_SC1); }
__device__ __forceinline__ void st2(rsrc_t r, u32 off, u32x2 v) { __builtin_amdgcn_raw_buffer_store_b64(v, r, off, 0, AUX_SC1); }
#define CFENCE() asm volatile("" ::: "memory")
#define PSL 0
#define PSLEEP() do { if (PSL > 0) __builtin_amdgcn_s_sleep(PSL); } while (0)
#define SB() __builtin_amdgcn_sched_barrier(0)
#define BAR() asm volatile("s_waitcnt lgkmcnt(0)\n\ts_barrier" ::: "memory")
#define LGKM0() asm volatile("s_waitcnt lgkmcnt(0)" ::: "memory")
#define DRAIN() asm volatile("s_waitcnt vmcnt(0)" ::: "memory")

template <int CTRL> __device__ __forceinline__ float dpp(float x) { return __int_as_float(__builtin_amdgcn_update_dpp(0, __float_as_int(x), CTRL, 0xf, 0xf, true)); }
__device__ __forceinline__ float rs32(float a, float b) { auto r = __builtin_amdgcn_permlane32_swap(__float_as_uint(a), __float_as_uint(b), false, false); return __uint_as_float(r[0]) + __uint_as_float(r[1]); }
__device__ __forceinline__ float rs16(float a, float b) { auto r = __builtin_amdgcn_permlane16_swap(__float_as_uint(a), __float_as_uint(b), false, false); return __uint_as_float(r[0]) + __uint_as_float(r[1]); }
__device__ __forceinline__ float row_sum(float x) { x += dpp<0x128>(x); x += dpp<0x124>(x); x += dpp<0x122>(x); x += dpp<0x121>(x); return x; }
__device__ __forceinline__ float row_max(float x) { x = fmaxf(x, dpp<0x128>(x)); x = fmaxf(x, dpp<0x124>(x)); x = fmaxf(x, dpp<0x122>(x)); x = fmaxf(x, dpp<0x121>(x)); return x; }
__device__ __forceinline__ float wave_sum(float x) { x = row_sum(x); x = rs16(x, x); x = rs32(x, x); return x; }
__device__ __forceinline__ float wave_max(float x) {
    x = row_max(x);
    { auto r = __builtin_amdgcn_permlane16_swap(__float_as_uint(x), __float_as_uint(x), false, false); x = fmaxf(__uint_as_float(r[0]), __uint_as_float(r[1])); }
    { auto r = __builtin_amdgcn_permlane32_swap(__float_as_uint(x), __float_as_uint(x), false, false); x = fmaxf(__uint_as_float(r[0]), __uint_as_float(r[1])); }
    return x;
}
__device__ __forceinline__ float rscat8(const float (&a)[8], int lane) {
    float r0 = rs32(a[0], a[4]), r1 = rs32(a[1], a[5]), r2 = rs32(a[2], a[6]), r3 = rs32(a[3], a[7]);
    float s0 = rs16(r0, r2), s1 = rs16(r1, r3);
    float t0 = s0 + dpp<0x128>(s0), t1 = s1 + dpp<0x128>(s1);
    return (lane & 8) ? t1 : t0;
}
__device__ __forceinline__ bool pois4(u32x4 v) { u32 m = max(max(v.x, v.y), max(v.z, v.w)); return m == POISON; }
__device__ __forceinline__ bool pois3(u32x3 v) { u32 m = max(max(v.x, v.y), v.z); return m == POISON; }
__device__ __forceinline__ bool anyl(bool b) { return __builtin_amdgcn_ballot_w64(b) != 0ull; }
__device__ __forceinline__ f32x4 mfma(u32x4 a, u32x4 b, f32x4 c) { return __builtin_amdgcn_mfma_f32_16x16x32_bf16(__builtin_bit_cast(bf16x8, a), __builtin_bit_cast(bf16x8, b), c, 0, 0, 0); }
__device__ __forceinline__ float silu(float v) { return v / (1.f + __expf(-v)); }
__device__ __forceinline__ void unp8(u32x4 v, float (&f)[8]) { f[0] = bflo(v.x); f[1] = bfhi(v.x); f[2] = bflo(v.y); f[3] = bfhi(v.y); f[4] = bflo(v.z); f[5] = bfhi(v.z); f[6] = bflo(v.w); f[7] = bfhi(v.w); }
__device__ __forceinline__ void unp6(u32x3 v, float (&f)[6]) { f[0] = bflo(v.x); f[1] = bfhi(v.x); f[2] = bflo(v.y); f[3] = bfhi(v.y); f[4] = bflo(v.z); f[5] = bfhi(v.z); }

__device__ __forceinline__ void wait_until(u64 t0, int ticks) {
    if (ticks > 0) { for (int i = 0; i < 4096; ++i) { if (__builtin_amdgcn_s_memrealtime() - t0 >= (u64)ticks) break; __builtin_amdgcn_s_sleep(1); } }
}
#define STAMP(i) do { } while (0)

#define STAMP2(i) do { } while (0)
__device__ __forceinline__ void ln_stats(const float (&v)[10], float& mean, float& rstd) {
    float s = 0.f;
#pragma unroll
    for (int i = 0; i < 10; ++i) s += v[i];
    mean = wave_sum(s) * (1.f / 640.f);
    float q = 0.f;
#pragma unroll
    for (int i = 0; i < 10; ++i) { float d = v[i] - mean; q += d * d; }
    float var = wave_sum(q) * (1.f / 640.f);
    rstd = 1.f / sqrtf(var + 1e-5f);
}

#define CAS __attribute__((address_space(4)))
#define PIN1(a) asm volatile("" : "+v"(a))
template <int CTRL, int BANK> __device__ __forceinline__ float dppb(float old, float x) { return __int_as_float(__builtin_amdgcn_update_dpp(__float_as_int(old), __float_as_int(x), CTRL, 0xf, BANK, false)); }
__device__ __forceinline__ u32x2 ld2(rsrc_t r, u32 off) { return __builtin_amdgcn_raw_buffer_load_b64(r, off, 0, AUX_SC1); }
__device__ __forceinline__ u32 ld1(rsrc_t r, u32 off) { return __builtin_amdgcn_raw_buffer_load_b32(r, off, 0, AUX_SC1); }
__device__ __forceinline__ bool tagbad(u32x4 g, u32 tlo, u32 thi) { return (g.z != tlo) | (g.w != thi); }
__device__ __forceinline__ f32x2 up2(u32 v) { return f32x2{bflo(v), bfhi(v)}; }
__device__ __forceinline__ f32x2 splat(float v) { return f32x2{v, v}; }

__device__ __forceinline__ void dma16nt(const void* base, u32 voff, u32 lds) {
    asm volatile("s_mov_b32 m0, %2\n\ts_nop 0\n\tglobal_load_lds_dwordx4 %0, %1 nt" ::"v"(voff), "s"(base), "s"(lds) : "memory", "m0");
}
__device__ __forceinline__ u32 ldh(rsrc_t r, u32 off) { return __builtin_amdgcn_raw_buffer_load_b16(r, off, 0, 0); }
template <int N> __device__ __forceinline__ void vmwait() { if (N < 63) asm volatile("s_waitcnt vmcnt(%0)" ::"n"(N) : "memory"); }
__device__ __forceinline__ u32x4 ald4(u32x4 rs, u32 off) { u32x4 v; asm volatile("buffer_load_dwordx4 %0, %1, %2, 0 offen sc1" : "=v"(v) : "v"(off), "s"(rs) : "memory"); return v; }
template <int N> __device__ __forceinline__ void await(u32x4& v) { asm volatile("s_waitcnt vmcnt(%1)" : "+v"(v) : "n"(N) : "memory"); }
#define GETTAG() \
    const u64 epoch = ((u64)(u32)__builtin_amdgcn_readfirstlane(epv.y) << 32) | (u64)(u32)__builtin_amdgcn_readfirstlane(epv.x); \
    const u64 tag = (epoch + 1ull) * TAGMUL; \
    const u32 tlo = (u32)tag, thi = (u32)(tag >> 32);

__global__ __launch_bounds__(NT, 2) void adaln_mla_mlp_kernel(
    const u16* __restrict__ x, const u16* __restrict__ ckv, const u16* __restrict__ wq, const u16* __restrict__ wuk,
    const u16* __restrict__ wuv, const u16* __restrict__ wo, const u16* __restrict__ w1, const u16* __restrict__ w2,
    const u16* __restrict__ cc, const u16* __restrict__ wmod, u16* __restrict__ out, unsigned char* __restrict__ ws) {
    extern __shared__ __attribute__((aligned(16))) unsigned char smem[];
    LAS unsigned char* L = (LAS unsigned char*)smem;
    STAMP(0);
    const u64 tstart = __builtin_amdgcn_s_memrealtime();
    asm volatile("" ::"s"(x), "s"(ckv), "s"(wq), "s"(wuk), "s"(wuv), "s"(wo), "s"(w1), "s"(w2), "s"(cc), "s"(wmod), "s"(out), "s"(ws));
    const int t = threadIdx.x, lane = t & 63, w = __builtin_amdgcn_readfirstlane(t >> 6);
    const int bid = blockIdx.x;
    const rsrc_t rws = mkrs(ws, WS_BYTES);
    const u32x2 epv = ld2(rws, OFF_EPOCH);
    const u32x4 PZ = {POISON, POISON, POISON, POISON};
    const u32 lbase = (u32)(uintptr_t)L;
    const u32x4 rws4 = {(u32)(uintptr_t)ws, (u32)((uintptr_t)ws >> 32), WS_BYTES, 0x00020000u};

    if (bid < 128) {
        const int xi = bid, h = xi & 31, cq = xi >> 5;
        const rsrc_t rmod = mkrs(wmod, DM * 3840 * 2);
        const u32 e3off = OFF_E3 + (u32)((cq * 12 + (lane >> 2)) * 32 + h) * 64 + (lane & 3) * 16;
        st4(rws, (w == 7 && lane < 48) ? e3off : OOB, PZ);
        const rsrc_t rq = mkrs(wq, DM * 6144 * 2);
        const rsrc_t ruk = mkrs(wuk, DC * 6144 * 2);
        const u32 wbase = (u32)(((cq * 192 + w * 24 + (lane >> 5)) * 32 + h) * 192 + 6 * (lane & 31)) * 2;
        u32x3 qw[10], uw[12];
        wait_until(tstart, D_WQ);
        {
            const u32 base = (u32)(((cq * 160 + w * 20 + (lane >> 5)) * 32 + h) * 192 + 6 * (lane & 31)) * 2;
#pragma unroll
            for (int j = 0; j < 10; ++j) { qw[j] = ld3(rq, base + j * 2 * 12288, AUX_NT); if (j >= XW) { SB(); vmwait<XW>(); SB(); } }
            SB();
#pragma unroll
            for (int j = 0; j < UK1; ++j) { uw[j] = ld3(ruk, wbase + j * 2 * 12288, AUX_NT); if (10 + j >= XW) { SB(); vmwait<XW>(); SB(); } }
            SB();
        }
        GETTAG()
        f32x2 qf2[10][3];
#pragma unroll
        for (int j = 0; j < 10; ++j) { qf2[j][0] = up2(qw[j].x); qf2[j][1] = up2(qw[j].y); qf2[j][2] = up2(qw[j].z); PIN1(qf2[j][0]); PIN1(qf2[j][1]); PIN1(qf2[j][2]); }
        STAMP(1);
        {
            const bool act = (lane & 15) < 10;
            const u32 goff = act ? OFF_E1 + (u32)(((lane >> 4) * 4 + cq) * 8 + w) * 256 + (lane & 15) * 16 : OOB;
            wait_until(tstart, D_E1);
            u32x4 g0 = ld4(rws, goff, AUX_SC1), g1 = ld4(rws, goff + 32768, AUX_SC1);
            SB();
            asm volatile("s_barrier" ::: "memory");
            SB();
#pragma unroll
            for (int j = UK1; j < 12; ++j) uw[j] = ld3(ruk, wbase + j * 2 * 12288, AUX_NT);
            SB();
            if (anyl(act & (tagbad(g0, tlo, thi) | tagbad(g1, tlo, thi)))) {
                for (int it = 0; it < SPIN; ++it) { CFENCE();
                    g0 = ld4(rws, goff, AUX_SC1); g1 = ld4(rws, goff + 32768, AUX_SC1);
                    if (!anyl(act & (tagbad(g0, tlo, thi) | tagbad(g1, tlo, thi)))) break;
                }
            }
            STAMP(2);
            st4(rws, t == 448 ? OFF_PF + xi * 16 : OOB, u32x4{0u, 0u, tlo, thi});
            SB();
            float s = rs32(__uint_as_float(g0.x) + __uint_as_float(g1.x), __uint_as_float(g0.y) + __uint_as_float(g1.y));
            s = rs16(s, s);
            f32x2 a[3] = {{0.f, 0.f}, {0.f, 0.f}, {0.f, 0.f}};
#define QSTEP(j) { const f32x2 hv = splat(dpp<0x150 + j>(s)); a[0] += hv * qf2[j][0]; a[1] += hv * qf2[j][1]; a[2] += hv * qf2[j][2]; }
            QSTEP(0) QSTEP(1) QSTEP(2) QSTEP(3) QSTEP(4) QSTEP(5) QSTEP(6) QSTEP(7) QSTEP(8) QSTEP(9)
#undef QSTEP
            LAS float* red = (LAS float*)(L + L_RQ);
            {
                LAS float* dst = red + w * 192 + 6 * (lane & 31) + 3 * (lane >> 5);
                dst[0] = rs32(a[0].x, a[1].y); dst[1] = rs32(a[0].y, a[2].x); dst[2] = rs32(a[1].x, a[2].y);
            }
            BAR();
            f32x2 v = {0.f, 0.f};
            if (w < 2) {
                f32x2 p[8];
#pragma unroll
                for (int k = 0; k < 8; ++k) p[k] = *(LAS f32x2*)(red + k * 192 + t * 2);
                v = ((p[0] + p[1]) + (p[2] + p[3])) + ((p[4] + p[5]) + (p[6] + p[7]));
            }
            st4(rws, t < 96 ? OFF_E2 + (u32)((cq * 32 + h) * 96 + t) * 16 : OOB, u32x4{__float_as_uint(v.x), __float_as_uint(v.y), tlo, thi});
        }
        STAMP(3);
        f32x2 wf[12][3];
        {
            LAS float* qs = (LAS float*)(L + L_Q);
            const u32 goff = w < 6 ? OFF_E2 + (u32)(((lane >> 4) * 32 + h) * 96 + 16 * w + (lane & 15)) * 16 : OOB;
            { const u64 tn = __builtin_amdgcn_s_memrealtime(); wait_until(tn, D_E2); }
            u32x4 g = ld4(rws, goff, AUX_SC1);
            SB();
#pragma unroll
            for (int j = 0; j < 12; ++j) { wf[j][0] = up2(uw[j].x); wf[j][1] = up2(uw[j].y); wf[j][2] = up2(uw[j].z); PIN1(wf[j][0]); PIN1(wf[j][1]); PIN1(wf[j][2]); }
            if (w < 6) {
                if (anyl(tagbad(g, tlo, thi))) {
                    for (int it = 0; it < SPIN; ++it) { CFENCE();
                        g = ld4(rws, goff, AUX_SC1);
                        if (!anyl(tagbad(g, tlo, thi))) break;
                    }
                }
                float s = rs32(__uint_as_float(g.x), __uint_as_float(g.y));
                s = rs16(s, s);
                if (!(lane & 16)) qs[2 * (16 * w + (lane & 15)) + (lane >> 5)] = s;
            }
            STAMP(5);
            BAR();
            const f32x2 q0 = *(LAS f32x2*)(qs + 6 * (lane & 31)), q1 = *(LAS f32x2*)(qs + 6 * (lane & 31) + 2), q2 = *(LAS f32x2*)(qs + 6 * (lane & 31) + 4);
            float p[12];
#pragma unroll
            for (int j = 0; j < 12; ++j) { const f32x2 d = wf[j][0] * q0 + wf[j][1] * q1 + wf[j][2] * q2; p[j] = d.x + d.y; }
            float r[6], s[3];
#pragma unroll
            for (int j = 0; j < 6; ++j) r[j] = rs16(p[j], p[j + 6]);
#pragma unroll
            for (int j = 0; j < 3; ++j) {
                const float own = (lane & 8) ? r[j + 3] : r[j], oth = (lane & 8) ? r[j] : r[j + 3];
                float v = own + dpp<0x128>(oth);
                v += dpp<0x141>(v); v += dpp<0x1B>(v); v += dpp<0xB1>(v);
                s[j] = v;
            }
            LAS float* red = (LAS float*)(L + L_RA);
            {
                const int j = lane & 7;
                const float v = j == 0 ? s[0] : (j == 1 ? s[1] : s[2]);
                if (j < 3) red[w * 24 + 2 * (j + 3 * ((lane >> 3) & 1) + 6 * ((lane >> 4) & 1)) + (lane >> 5)] = v;
            }
            BAR();
            u32 o[4] = {0u, 0u, 0u, 0u};
            if (w == 0) {
                const float sc = 0.07216878364870322f * 1.4426950408889634f;
                const int c0 = ((lane >> 3) * 32 + (lane & 3) * 8) & 255;
                const f32x4 p0 = *(LAS f32x4*)(red + c0) * sc, p1 = *(LAS f32x4*)(red + c0 + 4) * sc;
                const float v8[8] = {p0.x, p0.y, p0.z, p0.w, p1.x, p1.y, p1.z, p1.w};
#pragma unroll
                for (int i = 0; i < 4; ++i) {
                    const u32 hi = cvtpk(v8[2 * i], v8[2 * i + 1]);
                    const u32 lo = cvtpk(v8[2 * i] - bflo(hi), v8[2 * i + 1] - bfhi(hi));
                    o[i] = canon((lane & 4) ? lo : hi);
                }
            }
            st4(rws, t < 48 ? e3off : OOB, u32x4{o[0], o[1], o[2], o[3]});
        }
        STAMP(6);
        const rsrc_t ruv = mkrs(wuv, DC * 6144 * 2);
        u32x3 vw[12];
        SB();
#pragma unroll
        for (int j = 0; j < UVA; ++j) vw[j] = ld3(ruv, wbase + j * 2 * 12288, AUX_NT);
        SB();
        u32x4 gw[5]; u32 cv[5];
        const int rs = lane >> 2, ck = lane & 3;
        const int gi = xi - 48;
        const int g_s = 2 * ((gi >> 4) * 8 + (gi & 7)) + ((gi >> 3) & 1);
        const int f_s = W1P ? 2 * ((xi >> 4) * 8 + (xi & 7)) + ((xi >> 3) & 1) : xi;
        const int fi = xi - 48;
        const bool isf2 = xi >= 48 && xi < 88;
        const int s2 = 5 * (fi & 7) + (fi >> 3);
        const rsrc_t r2 = mkrs(w2, DFF * DM * 2);
        u32x4 fw[6];
#define W2LOADS() do { \
        const u32 w2b = isf2 ? (u32)(2 * (t >> 1) * DM + 16 * s2 + 8 * (t & 1)) * 2 : OOB; \
        _Pragma("unroll") for (int i = 0; i < 3; ++i) { fw[2 * i] = __builtin_amdgcn_raw_buffer_load_b128(r2, w2b + (u32)(512 * i * DM) * 2, 0, W2A); fw[2 * i + 1] = __builtin_amdgcn_raw_buffer_load_b128(r2, w2b + (u32)((512 * i + 1) * DM) * 2, 0, W2A); } \
        SB(); \
        } while (0)
#define W2RLOADS() do { \
        const u32 o2 = (xi < 48 && (lane & 15) < 10) ? (u32)((32 * f_s + 8 * (lane >> 4)) * DM + 80 * w + 8 * (lane & 15)) * 2 : OOB; \
        _Pragma("unroll") for (int j = 0; j < 8; ++j) w2r[j] = __builtin_amdgcn_raw_buffer_load_b128(r2, o2 + j * DM * 2, 0, W2A); \
        SB(); \
        } while (0)
#define GLOADS() do { \
        const bool isg = xi >= 48; \
        const rsrc_t rg = mkrs(isg ? wmod : w1, isg ? DM * 3840 * 2 : DM * DFF * 2); \
        const rsrc_t rc = mkrs(isg ? cc : x, DM * 2); \
        const u32 goff0 = isg ? (GY ? OOB : (u32)((t >> 2) * 3840 + 1280 + 32 * g_s + 8 * ck) * 2) : (u32)((w * 80 + (E6D ? 4 : 1) * rs) * DFF + 32 * f_s + 8 * ck) * 2; \
        const u32 gstr = isg ? (GY ? 0 : 128 * 7680) : (E6D ? 1 : 16) * DFF * 2; \
        const u32 coff0 = isg ? (u32)(t >> 2) * 2 : (u32)(w * 80 + (E6D ? 4 : 1) * rs) * 2; \
        const u32 cstr = isg ? 256 : (E6D ? 2 : 32); \
        const u32 gx4 = (E6D && !isg) ? (u32)(60 - 3 * rs) * DFF * 2 : 0u, cx4 = (E6D && !isg) ? (u32)(60 - 3 * rs) * 2 : 0u; \
        _Pragma("unroll") for (int j = 0; j < 5; ++j) gw[j] = __builtin_amdgcn_raw_buffer_load_b128(rg, goff0 + j * gstr + (j == 4 ? gx4 : 0u), 0, W1A); \
        _Pragma("unroll") for (int j = 0; j < 5; ++j) cv[j] = ldh(rc, coff0 + j * cstr + (j == 4 ? cx4 : 0u)); \
        SB(); \
        } while (0)
#define GCOMP() do { \
        if (!GY && xi >= 48) { \
            float a[8]; \
_Pragma("unroll") \
            for (int i = 0; i < 8; ++i) a[i] = 0.f; \
_Pragma("unroll") \
            for (int j = 0; j < 5; ++j) { \
                float f[8]; unp8(gw[j], f); const float s = silu(bflo(cv[j])); \
_Pragma("unroll") \
                for (int i = 0; i < 8; ++i) a[i] += s * f[i]; \
            } \
            float v = rscat8(a, lane); \
            v += dpp<0x104>(v); \
            const int e = ((lane >> 5) & 1) * 4 + ((lane >> 4) & 1) * 2 + ((lane >> 3) & 1); \
            LAS float* redg = (LAS float*)(L + L_RG); \
            if (!(lane & 4)) redg[w * 32 + ck * 8 + e] = v; \
            BAR(); \
            if (w == 0 && lane < 16) { \
                f32x2 s = {0.f, 0.f}; \
_Pragma("unroll") \
                for (int i = 0; i < 8; ++i) s += *(LAS f32x2*)(redg + i * 32 + lane * 2); \
                st4(rws, OFF_G + (g_s * 16 + lane) * 16, u32x4{__float_as_uint(s.x), __float_as_uint(s.y), tlo, thi}); \
            } \
        } \
        } while (0)
        wait_until(tstart, D_G);
        GLOADS();
        wait_until(tstart, D_UV);
#pragma unroll
        for (int j = UVA; j < 12; ++j) vw[j] = ld3(ruv, wbase + j * 2 * 12288, AUX_NT);
        SB();
        STAMP(4);
        GCOMP();
        f32x2 vf[12][3];
#pragma unroll
        for (int j = 0; j < 12; ++j) { vf[j][0] = up2(vw[j].x); vf[j][1] = up2(vw[j].y); vf[j][2] = up2(vw[j].z); PIN1(vf[j][0]); PIN1(vf[j][1]); PIN1(vf[j][2]); }
        STAMP(7);
        u32 pfd = 0;
        {
            const u32 obase = OFF_O + (u32)(w * 16 + (lane >> 5)) * 49152 + (u32)(h * 4 + cq) * 384 + (lane & 31) * 12;
            constexpr u32 OJ = 2 * 49152;
            LAS float* red1 = (LAS float*)(L + L_RV1);
            LAS float* red2 = (LAS float*)(L + L_RV2);
            LAS f32x2* wl = (LAS f32x2*)(L + L_WL);
            u32x3 oa[8];
            {
                const u32 mlA = lane < 16 ? OFF_ML + (u32)(h * 128 + w * 16 + lane) * 16 : OOB;
                wait_until(tstart, D_ML);
                u32x4 ma = ld4(rws, mlA, AUX_SC1);
                if (anyl((lane < 16) & tagbad(ma, tlo, thi))) {
                    for (int it = 0; it < SPIN; ++it) { CFENCE();
                        if (ML_SLEEP > 0) __builtin_amdgcn_s_sleep(ML_SLEEP);
                        ma = ld4(rws, mlA, AUX_SC1);
                        if (!anyl((lane < 16) & tagbad(ma, tlo, thi))) break;
                    }
                }
                CFENCE();
                STAMP(8);
#pragma unroll
                for (int j = 0; j < 8; ++j) oa[j] = ld3(rws, obase + j * OJ, AUX_SC1);
                SB();
                if (lane < 16) wl[w * 16 + lane] = f32x2{__uint_as_float(ma.x), __uint_as_float(ma.y)};
            }
            BAR();
            float wA;
            {
                const f32x2 a = wl[lane], b = wl[lane + 64], own = wl[16 * w + 2 * (lane & 7) + (lane >> 5)];
                const float M = wave_max(fmaxf(a.x, b.x));
                const float Lsum = wave_sum(__builtin_amdgcn_exp2f(a.x - M) * a.y + __builtin_amdgcn_exp2f(b.x - M) * b.y);
                wA = __builtin_amdgcn_exp2f(own.x - M) * __builtin_amdgcn_rcpf(Lsum);
            }
            f32x2 pr[8][3];
#define OSTEP(j) { const f32x2 ww = splat(dpp<0x150 + j>(wA)); pr[j][0] = up2(oa[j].x) * ww; pr[j][1] = up2(oa[j].y) * ww; pr[j][2] = up2(oa[j].z) * ww; }
            OSTEP(0) OSTEP(1) OSTEP(2) OSTEP(3) OSTEP(4) OSTEP(5) OSTEP(6) OSTEP(7)
#undef OSTEP
            STAMP(9);
            f32x2 a2[3];
#pragma unroll
            for (int k = 0; k < 3; ++k) a2[k] = ((pr[0][k] + pr[1][k]) + (pr[2][k] + pr[3][k])) + ((pr[4][k] + pr[5][k]) + (pr[6][k] + pr[7][k]));
            {
                LAS float* dst = red1 + w * 192 + 6 * (lane & 31) + 3 * (lane >> 5);
                dst[0] = rs32(a2[0].x, a2[1].y); dst[1] = rs32(a2[0].y, a2[2].x); dst[2] = rs32(a2[1].x, a2[2].y);
            }
            STAMP2(1);
            BAR();
            STAMP2(2);
            float cx = 0.f;
            {
                const int i = lane & 15, r = 2 * (i < 12 ? i : 11) + (lane >> 5), cidx = (w >> 2) * 96 + (4 * (w & 3) + (r & 3)) * 6 + 2 * (r >> 3) + (((r >> 2) & 1) ^ ((w >> 1) & 1));
                float p[8];
#pragma unroll
                for (int k = 0; k < 8; ++k) p[k] = red1[k * 192 + cidx];
                cx = ((p[0] + p[1]) + (p[2] + p[3])) + ((p[4] + p[5]) + (p[6] + p[7]));
            }
            f32x2 c2[3] = {{0.f, 0.f}, {0.f, 0.f}, {0.f, 0.f}};
#define VSTEP(j) { const f32x2 cvv = splat(dpp<0x150 + j>(cx)); c2[0] += cvv * vf[j][0]; c2[1] += cvv * vf[j][1]; c2[2] += cvv * vf[j][2]; }
            VSTEP(0) VSTEP(1) VSTEP(2) VSTEP(3) VSTEP(4) VSTEP(5) VSTEP(6) VSTEP(7) VSTEP(8) VSTEP(9) VSTEP(10) VSTEP(11)
#undef VSTEP
            {
                LAS float* dst = red2 + w * 192 + 6 * (lane & 31) + 3 * (lane >> 5);
                dst[0] = rs32(c2[0].x, c2[1].y); dst[1] = rs32(c2[0].y, c2[2].x); dst[2] = rs32(c2[1].x, c2[2].y);
            }
            STAMP2(3);
            BAR();
            f32x2 s5 = {0.f, 0.f};
            if (w < 2) {
                f32x2 p[8];
#pragma unroll
                for (int k = 0; k < 8; ++k) p[k] = *(LAS f32x2*)(red2 + k * 192 + t * 2);
                s5 = ((p[0] + p[1]) + (p[2] + p[3])) + ((p[4] + p[5]) + (p[6] + p[7]));
            }
            st4(rws, t < 96 ? OFF_E5 + (u32)(cq * 3072 + h * 96 + t) * 16 : OOB, u32x4{__float_as_uint(s5.x), __float_as_uint(s5.y), tlo, thi});
        }
        BAR();
        STAMP(10);
        if (xi < 48) {
            LAS float* gs = (LAS float*)(L + L_GS);
            u32x4 w2r[8];
            {
                u32x4 g0, g1;
                const bool two = t < 448;
                g0 = ld4(rws, OFF_G + t * 16, AUX_SC1); g1 = ld4(rws, two ? OFF_G + (t + 512) * 16 : OFF_G + t * 16, AUX_SC1);
                SB();
                wait_until(tstart, D_W2);
                W2RLOADS();
                if (anyl(tagbad(g0, tlo, thi) | tagbad(g1, tlo, thi)))
                for (int it = 0; it < SPIN; ++it) { CFENCE();
                    g0 = ld4(rws, OFF_G + t * 16, AUX_SC1); bool bad = tagbad(g0, tlo, thi);
                    if (two) { g1 = ld4(rws, OFF_G + (t + 512) * 16, AUX_SC1); bad |= tagbad(g1, tlo, thi); }
                    if (!anyl(bad)) break; PSLEEP();
                }
                *(LAS u32x2*)(gs + 2 * t) = u32x2{g0.x, g0.y};
                if (two) *(LAS u32x2*)(gs + 2 * (t + 512)) = u32x2{g1.x, g1.y};
            }
            BAR();
            f32x2 wA[5][4], u[4], v1[4], v2[4];
            float xv[5], ga[5];
#pragma unroll
            for (int k = 0; k < 4; ++k) { u[k] = f32x2{0.f, 0.f}; v1[k] = f32x2{0.f, 0.f}; v2[k] = f32x2{0.f, 0.f}; }
#pragma unroll
            for (int j = 0; j < 5; ++j) {
                const int m = E6D ? w * 80 + (j < 4 ? 4 * rs + j : 64 + rs) : w * 80 + 16 * j + rs;
                const float gav = gs[m], sbv = gs[640 + m], cbp = 1.f + gs[1280 + m];
                xv[j] = bflo(cv[j]); ga[j] = gav;
                const f32x2 A = splat(gav * cbp), X0 = splat(xv[j] * cbp), C = splat(cbp), S = splat(sbv);
                const f32x2 f[4] = {up2(gw[j].x), up2(gw[j].y), up2(gw[j].z), up2(gw[j].w)};
#pragma unroll
                for (int k = 0; k < 4; ++k) { wA[j][k] = A * f[k]; u[k] += X0 * f[k]; v1[k] += C * f[k]; v2[k] += S * f[k]; PIN1(wA[j][k]); }
            }
            const int e = ((lane >> 5) & 1) * 4 + ((lane >> 4) & 1) * 2 + ((lane >> 3) & 1);
            LAS float* rp = (LAS float*)(L + L_RP);
            {
                float a[8] = {v1[0].x, v1[0].y, v1[1].x, v1[1].y, v1[2].x, v1[2].y, v1[3].x, v1[3].y};
                float v = rscat8(a, lane); v += dpp<0x104>(v);
                if (!(lane & 4)) rp[w * 32 + ck * 8 + e] = v;
                float b[8] = {v2[0].x, v2[0].y, v2[1].x, v2[1].y, v2[2].x, v2[2].y, v2[3].x, v2[3].y};
                v = rscat8(b, lane); v += dpp<0x104>(v);
                if (!(lane & 4)) rp[256 + w * 32 + ck * 8 + e] = v;
            }
            BAR();
            f32x2 V1 = {0.f, 0.f}, V2 = {0.f, 0.f};
            const int hp = 2 * (4 * (lane >> 4) + (lane & 3));
#pragma unroll
            for (int k = 0; k < 8; ++k) { V1 += *(LAS f32x2*)(rp + k * 32 + hp); V2 += *(LAS f32x2*)(rp + 256 + k * 32 + hp); }
            PIN1(V1); PIN1(V2);
            f32x2 w2f[8][4];
#pragma unroll
            for (int j = 0; j < 8; ++j) { w2f[j][0] = up2(w2r[j].x); w2f[j][1] = up2(w2r[j].y); w2f[j][2] = up2(w2r[j].z); w2f[j][3] = up2(w2r[j].w); PIN1(w2f[j][0]); PIN1(w2f[j][1]); PIN1(w2f[j][2]); PIN1(w2f[j][3]); }
#pragma unroll
            for (int k = 0; k < 4; ++k) PIN1(u[k]);
            STAMP(11);
            LAS float* r6 = (LAS float*)(L + L_R6);
            float atv[5];
            {
                const int pg = lane / 20, qq = lane - pg * 20;
                const u32 goff = lane < 60 ? OFF_E6 + (u32)(4 * pg * 640 + w * 80 + 4 * qq) * 4 : OOB;
                u32x4 eg[4];
                wait_until(tstart, D_E6);
                for (int it = 0; it < SPIN; ++it) { CFENCE();
#pragma unroll
                    for (int k = 0; k < 4; ++k) eg[k] = ld4(rws, goff + k * 2560, AUX_SC1);
                    if (!anyl(pois4(eg[0]) | pois4(eg[1]) | pois4(eg[2]) | pois4(eg[3]))) break; PSLEEP();
                }
                STAMP(12);
                const f32x4 s = (__builtin_bit_cast(f32x4, eg[0]) + __builtin_bit_cast(f32x4, eg[1])) + (__builtin_bit_cast(f32x4, eg[2]) + __builtin_bit_cast(f32x4, eg[3]));
                LAS float* rw = r6 + w * 256;
                *(LAS f32x4*)(rw + 4 * lane) = s;
                CFENCE(); __builtin_amdgcn_wave_barrier(); CFENCE();
                const f32x4 a0 = *(LAS f32x4*)(rw + 4 * rs), a1 = *(LAS f32x4*)(rw + 80 + 4 * rs), a2 = *(LAS f32x4*)(rw + 160 + 4 * rs);
                const float b0 = rw[64 + rs], b1 = rw[144 + rs], b2 = rw[224 + rs];
                const f32x4 as = (a0 + a1) + a2;
                atv[0] = as.x; atv[1] = as.y; atv[2] = as.z; atv[3] = as.w; atv[4] = (b0 + b1) + b2;
            }
            (void)r6;
            float x1[5], sum = 0.f;
#pragma unroll
            for (int j = 0; j < 5; ++j) {
                const float at = atv[j];
                x1[j] = xv[j] + ga[j] * at; sum += x1[j];
                const f32x2 A = splat(at);
#pragma unroll
                for (int k = 0; k < 4; ++k) u[k] += A * wA[j][k];
            }
            sum += dpp<0x124>(sum); sum += dpp<0x128>(sum); sum = rs16(sum, sum); sum = rs32(sum, sum);
            const float mw_ = sum * (1.f / 80.f);
            float q = 0.f;
#pragma unroll
            for (int j = 0; j < 5; ++j) { const float d = x1[j] - mw_; q += d * d; }
            q += dpp<0x124>(q); q += dpp<0x128>(q); q = rs16(q, q); q = rs32(q, q);
            {
                float a[8] = {u[0].x, u[0].y, u[1].x, u[1].y, u[2].x, u[2].y, u[3].x, u[3].y};
                float v = rscat8(a, lane); v += dpp<0x104>(v);
                LAS float* rf = (LAS float*)(L + L_RF);
                if (!(lane & 4)) rf[w * 32 + ck * 8 + e] = v;
                LAS float* sst = (LAS float*)(L + L_ST);
                if (lane == 0) *(LAS f32x2*)(sst + 2 * w) = f32x2{mw_, q};
                STAMP2(4);
                BAR();
                {
                    const int kk = (lane >> 2) & 3;
                    const f32x2 pa = *(LAS f32x2*)(rf + kk * 32 + hp), pb = *(LAS f32x2*)(rf + 128 + kk * 32 + hp), s1 = *(LAS f32x2*)(sst + 2 * (lane & 7));
                    f32x2 U = pa + pb;
                    float ms = s1.x;
                    ms += dpp<0xB1>(ms); U.x += dpp<0x124>(U.x); U.y += dpp<0x124>(U.y);
                    ms += dpp<0x4E>(ms); U.x += dpp<0x128>(U.x); U.y += dpp<0x128>(U.y);
                    ms += dpp<0x141>(ms);
                    const float mean = ms * 0.125f, d = s1.x - mean;
                    float vs = s1.y + 80.f * d * d;
                    vs += dpp<0xB1>(vs); vs += dpp<0x4E>(vs); vs += dpp<0x141>(vs);
                    const float var = vs * (1.f / 640.f);
                    const float rstd = __builtin_amdgcn_rsqf(var + 1e-5f);
                    const float h0 = fmaxf(rstd * (U.x - mean * V1.x) + V2.x, 0.f), h1 = fmaxf(rstd * (U.y - mean * V1.y) + V2.y, 0.f);
                    f32x2 o2[4] = {{0.f, 0.f}, {0.f, 0.f}, {0.f, 0.f}, {0.f, 0.f}};
#define HSTEP(k) { const f32x2 ha = splat(dpp<0x150 + k>(h0)), hb = splat(dpp<0x150 + k>(h1)); _Pragma("unroll") for (int i = 0; i < 4; ++i) { o2[i] += ha * w2f[2 * k][i]; o2[i] += hb * w2f[2 * k + 1][i]; } }
                    HSTEP(0) HSTEP(1) HSTEP(2) HSTEP(3)
#undef HSTEP
                    const float r0 = rs32(o2[0].x, o2[2].x), r1 = rs32(o2[0].y, o2[2].y), r2_ = rs32(o2[1].x, o2[3].x), r3 = rs32(o2[1].y, o2[3].y);
                    const float q0 = rs16(r0, r2_), q1 = rs16(r1, r3);
                    st4(rws, (lane & 15) < 10 ? OFF_PP + (u32)(f_s * 320 + 40 * w + 4 * (lane & 15) + (lane >> 4)) * 16 : OOB, u32x4{__float_as_uint(q0), __float_as_uint(q1), tlo, thi});
                }
                if (false) {
                    f32x2 p[8], s8[8];
#pragma unroll
                    for (int k = 0; k < 8; ++k) { p[k] = *(LAS f32x2*)(rf + k * 32 + lane * 2); s8[k] = *(LAS f32x2*)(sst + 2 * k); }
                    const f32x2 U = ((p[0] + p[1]) + (p[2] + p[3])) + ((p[4] + p[5]) + (p[6] + p[7]));
                    const f32x2 SS = ((s8[0] + s8[1]) + (s8[2] + s8[3])) + ((s8[4] + s8[5]) + (s8[6] + s8[7]));
                    const float mean = SS.x * 0.125f;
                    float dv = 0.f;
#pragma unroll
                    for (int k = 0; k < 8; ++k) { const float d = s8[k].x - mean; dv += d * d; }
                    const float var = (SS.y + 80.f * dv) * (1.f / 640.f);
                    const float rstd = __builtin_amdgcn_rsqf(var + 1e-5f);
                    const float h0 = fmaxf(rstd * (U.x - mean * V1.x) + V2.x, 0.f), h1 = fmaxf(rstd * (U.y - mean * V1.y) + V2.y, 0.f);
                    st4(rws, OFF_HID + (u32)(f_s * 16 + lane) * 16, u32x4{__float_as_uint(h0), __float_as_uint(h1), tlo, thi});
                }
                STAMP2(6);
            }

        } else if (isf2) {
            if (w == 0) {
                const int r = lane & 7, pq = lane >> 3;
                const u32 xx = *(const u32*)(x + 16 * s2 + 2 * r);
                u32x4 ga0, gb0;
                for (int it = 0; it < SPIN; ++it) { CFENCE();
                    ga0 = ld4(rws, OFF_G + (u32)(8 * s2 + r) * 16, AUX_SC1); gb0 = ld4(rws, OFF_G + (u32)(960 + 8 * s2 + r) * 16, AUX_SC1);
                    if (!anyl(tagbad(ga0, tlo, thi) | tagbad(gb0, tlo, thi))) break; PSLEEP();
                }
                u32x2 e0, e1;
                const u32 eo = OFF_E6 + (u32)(pq * 640 + 16 * s2 + 2 * r) * 4;
                wait_until(tstart, D_E6);
                for (int it = 0; it < SPIN; ++it) { CFENCE();
                    e0 = ld2(rws, eo); e1 = ld2(rws, pq < 4 ? eo + 8 * 2560 : OOB);
                    if (!anyl((max(e0.x, e0.y) == POISON) | (max(e1.x, e1.y) == POISON))) break; PSLEEP();
                }
                float ax = __uint_as_float(e0.x) + __uint_as_float(e1.x), ay = __uint_as_float(e0.y) + __uint_as_float(e1.y);
                ax += dpp<0x128>(ax); ay += dpp<0x128>(ay); ax = rs16(ax, ax); ay = rs16(ay, ay); ax = rs32(ax, ax); ay = rs32(ay, ay);
                f32x2 x1 = {bflo(xx) + __uint_as_float(ga0.x) * ax, bfhi(xx) + __uint_as_float(ga0.y) * ay};
                f32x2 gb = {__uint_as_float(gb0.x), __uint_as_float(gb0.y)};
                PIN1(x1); PIN1(gb);
                STAMP(11);
                u32x4 pg[6];
                const u32 po = OFF_PP + (u32)(pq * 320 + 8 * s2 + r) * 16;
                wait_until(tstart, D_H);
                for (int it = 0; it < SPIN; ++it) { CFENCE();
                    bool bad = false;
#pragma unroll
                    for (int j = 0; j < 6; ++j) pg[j] = ld4(rws, po + j * 8 * 5120, AUX_SC1);
#pragma unroll
                    for (int j = 0; j < 6; ++j) bad |= tagbad(pg[j], tlo, thi);
                    if (!anyl(bad)) break; PSLEEP();
                }
                STAMP(12);
                float sx = ((__uint_as_float(pg[0].x) + __uint_as_float(pg[1].x)) + (__uint_as_float(pg[2].x) + __uint_as_float(pg[3].x))) + (__uint_as_float(pg[4].x) + __uint_as_float(pg[5].x));
                float sy = ((__uint_as_float(pg[0].y) + __uint_as_float(pg[1].y)) + (__uint_as_float(pg[2].y) + __uint_as_float(pg[3].y))) + (__uint_as_float(pg[4].y) + __uint_as_float(pg[5].y));
                sx += dpp<0x128>(sx); sy += dpp<0x128>(sy); sx = rs16(sx, sx); sy = rs16(sy, sy); sx = rs32(sx, sx); sy = rs32(sy, sy);
                if (lane < 8) *(u32*)(out + 16 * s2 + 2 * r) = cvtpk(x1.x + gb.x * sx, x1.y + gb.y * sy);
                if (xi == 48 && lane == 0) __hip_atomic_store((GAS u64*)(ws + OFF_EPOCH), epoch + 1ull, __ATOMIC_RELAXED, __HIP_MEMORY_SCOPE_AGENT);
            }
        } else if (false) {
            const int chunk = t & 1, q = t >> 1;
            W2LOADS();
            f32x4 x1 = {0.f, 0.f, 0.f, 0.f}, gb = {0.f, 0.f, 0.f, 0.f};
            if (w == 0) {
                const int c4 = lane & 3, p = lane >> 2;
                const u32x2 xx = *(const u32x2*)(x + 16 * s2 + 4 * c4);
                u32x4 ga0, ga1, gb0, gb1;
                for (int it = 0; it < SPIN; ++it) { CFENCE();
                    ga0 = ld4(rws, OFF_G + (u32)(8 * s2 + 2 * c4) * 16, AUX_SC1); ga1 = ld4(rws, OFF_G + (u32)(8 * s2 + 2 * c4 + 1) * 16, AUX_SC1);
                    gb0 = ld4(rws, OFF_G + (u32)(960 + 8 * s2 + 2 * c4) * 16, AUX_SC1); gb1 = ld4(rws, OFF_G + (u32)(960 + 8 * s2 + 2 * c4 + 1) * 16, AUX_SC1);
                    if (!anyl(tagbad(ga0, tlo, thi) | tagbad(ga1, tlo, thi) | tagbad(gb0, tlo, thi) | tagbad(gb1, tlo, thi))) break; PSLEEP();
                }
                u32x4 g = {0u, 0u, 0u, 0u};
                wait_until(tstart, D_E6);
                for (int it = 0; it < SPIN; ++it) { CFENCE();
                    if (p < 12) g = ld4(rws, OFF_E6 + (u32)(p * 640 + 16 * s2 + 4 * c4) * 4, AUX_SC1);
                    if (!anyl(pois4(g))) break; PSLEEP();
                }
                f32x4 at = __builtin_bit_cast(f32x4, g);
#pragma unroll
                for (int k = 0; k < 4; ++k) { float v = at[k]; v += dpp<0x124>(v); v += dpp<0x128>(v); v = rs16(v, v); v = rs32(v, v); at[k] = v; }
                x1 = f32x4{bflo(xx.x) + __uint_as_float(ga0.x) * at.x, bfhi(xx.x) + __uint_as_float(ga0.y) * at.y, bflo(xx.y) + __uint_as_float(ga1.x) * at.z, bfhi(xx.y) + __uint_as_float(ga1.y) * at.w};
                gb = f32x4{__uint_as_float(gb0.x), __uint_as_float(gb0.y), __uint_as_float(gb1.x), __uint_as_float(gb1.y)};
                asm volatile("" : "+v"(x1), "+v"(gb));
            }
            f32x2 ff[6][4];
#pragma unroll
            for (int j = 0; j < 6; ++j) { ff[j][0] = up2(fw[j].x); ff[j][1] = up2(fw[j].y); ff[j][2] = up2(fw[j].z); ff[j][3] = up2(fw[j].w); PIN1(ff[j][0]); PIN1(ff[j][1]); PIN1(ff[j][2]); PIN1(ff[j][3]); }
            STAMP(11);
            u32x4 hg[3];
            wait_until(tstart, D_H);
            for (int it = 0; it < SPIN; ++it) { CFENCE();
#pragma unroll
                for (int i = 0; i < 3; ++i) hg[i] = ld4(rws, OFF_HID + (u32)(q + 256 * i) * 16, AUX_SC1);
                if (!anyl(tagbad(hg[0], tlo, thi) | tagbad(hg[1], tlo, thi) | tagbad(hg[2], tlo, thi))) break; PSLEEP();
            }
            STAMP(12);
            f32x2 acc[4] = {{0.f, 0.f}, {0.f, 0.f}, {0.f, 0.f}, {0.f, 0.f}};
#pragma unroll
            for (int i = 0; i < 3; ++i) {
                const f32x2 h0 = splat(__uint_as_float(hg[i].x)), h1 = splat(__uint_as_float(hg[i].y));
#pragma unroll
                for (int k = 0; k < 4; ++k) { acc[k] += h0 * ff[2 * i][k]; acc[k] += h1 * ff[2 * i + 1][k]; }
            }
            float a[8] = {acc[0].x, acc[0].y, acc[1].x, acc[1].y, acc[2].x, acc[2].y, acc[3].x, acc[3].y};
            float v = rscat8(a, lane);
            v += dpp<0x4E>(v); v += dpp<0x104>(v);
            const int e = ((lane >> 5) & 1) * 4 + ((lane >> 4) & 1) * 2 + ((lane >> 3) & 1);
            LAS float* r2l = (LAS float*)(L + L_R2);
            if (!(lane & 6)) r2l[w * 16 + chunk * 8 + e] = v;
            STAMP2(5);
            BAR();
            STAMP2(4);
            if (t < 4) {
                f32x4 p[8];
#pragma unroll
                for (int k = 0; k < 8; ++k) p[k] = *(LAS f32x4*)(r2l + k * 16 + t * 4);
                const f32x4 s = ((p[0] + p[1]) + (p[2] + p[3])) + ((p[4] + p[5]) + (p[6] + p[7]));
                const f32x4 o = x1 + gb * s;
                *(u32x2*)(out + 16 * s2 + 4 * t) = u32x2{cvtpk(o.x, o.y), cvtpk(o.z, o.w)};
            }
            if (xi == 48 && t == 0) __hip_atomic_store((GAS u64*)(ws + OFF_EPOCH), epoch + 1ull, __ATOMIC_RELAXED, __HIP_MEMORY_SCOPE_AGENT);

        }
    } else {
        const int yi = bid - 128;
        const bool isM = yi < 80;
        const unsigned char* src = (const unsigned char*)ckv + (size_t)yi * 64 * 1536;
        u32 koff[12];
#define KOFF() _Pragma("unroll") for (int j = 0; j < 12; ++j) { const int idx = j * 64 + lane, row = w * 8 + idx / 96, p = idx % 96; const int ch = (p & ~15) | ((p ^ row) & 15); koff[j] = (u32)(row * 1536 + ch * 16); PIN1(koff[j]); }
        u32x4 mw[3]; u32 mc[3]; u32 xr[5], xm = 0;
        const int m_rg = yi & 7, m_s = yi >> 3;
        if (isM) {
            const rsrc_t rmod = mkrs(wmod, DM * 3840 * 2);
            const int row0 = m_rg * 80 + w * 10 + (lane >> 4);
            const u32 off = (u32)(row0 * 3840 + ((lane >> 3) & 1) * 640 + m_s * 64 + (lane & 7) * 8) * 2;
            mw[0] = ld4(rmod, off, AUX_NT); mw[1] = ld4(rmod, off + 4 * 7680, AUX_NT); mw[2] = ld4(rmod, lane < 32 ? off + 8 * 7680 : OOB, AUX_NT);
            const rsrc_t rc = mkrs(cc, DM * 2);
            mc[0] = ldh(rc, row0 * 2); mc[1] = ldh(rc, row0 * 2 + 8); mc[2] = ldh(rc, row0 * 2 + (lane < 32 ? 16 : 0));
            if (w == 0) {
#pragma unroll
                for (int i = 0; i < 5; ++i) xr[i] = ((const u32*)x)[lane + 64 * i];
                xm = ((const u32*)x)[32 * m_s + (lane & 31)];
            }
        }
        SB();
        KOFF()
        if (CKV1 > 0) {
            wait_until(tstart, D_CKV);
#pragma unroll
            for (int j = 0; j < CKV1; ++j) dma16nt(src, koff[j], lbase + L_KV + (u32)(w * 12 + j) * 1024);
            SB();
        }
        GETTAG()
        if (isM) {
            f32x2 lnx = {0.f, 0.f};
            if (w == 0) {
                float xv[10];
#pragma unroll
                for (int i = 0; i < 5; ++i) { xv[2 * i] = bflo(xr[i]); xv[2 * i + 1] = bfhi(xr[i]); }
                float mean, rstd; ln_stats(xv, mean, rstd);
                lnx = f32x2{(bflo(xm) - mean) * rstd, (bfhi(xm) - mean) * rstd};
            }
            float a[8], f[8];
            const float s0 = silu(bflo(mc[0])), s1 = silu(bflo(mc[1])), s2 = silu(bflo(mc[2]));
            unp8(mw[0], f);
#pragma unroll
            for (int i = 0; i < 8; ++i) a[i] = s0 * f[i];
            unp8(mw[1], f);
#pragma unroll
            for (int i = 0; i < 8; ++i) a[i] += s1 * f[i];
            unp8(mw[2], f);
#pragma unroll
            for (int i = 0; i < 8; ++i) a[i] += s2 * f[i];
            const float r0 = rs32(a[0], a[4]), r1 = rs32(a[1], a[5]), r2 = rs32(a[2], a[6]), r3 = rs32(a[3], a[7]);
            const f32x2 sv = {rs16(r0, r2), rs16(r1, r3)};
            LAS float* red = (LAS float*)(L + L_RM);
            *(LAS f32x2*)(red + w * 128 + ((lane >> 3) & 1) * 64 + (lane & 7) * 8 + 4 * (lane >> 5) + 2 * ((lane >> 4) & 1)) = sv;
            BAR();
            if (w == 0) {
                f32x2 p[8];
#pragma unroll
                for (int k = 0; k < 8; ++k) p[k] = *(LAS f32x2*)(red + k * 128 + 2 * lane);
                const f32x2 s = ((p[0] + p[1]) + (p[2] + p[3])) + ((p[4] + p[5]) + (p[6] + p[7]));
                auto rx = __builtin_amdgcn_permlane32_swap(__float_as_uint(s.x), __float_as_uint(s.x), false, false);
                auto ry = __builtin_amdgcn_permlane32_swap(__float_as_uint(s.y), __float_as_uint(s.y), false, false);
                float hx = lnx.x * __uint_as_float(rx[1]) + __uint_as_float(rx[0]), hy = lnx.y * __uint_as_float(ry[1]) + __uint_as_float(ry[0]);
                if (m_rg == 0) { hx += lnx.x; hy += lnx.y; }
                const int g = 32 * m_s + lane, gm = g / 80, gr = g - gm * 80, gw_ = gr / 10;
                if (lane < 32) st4(rws, OFF_E1 + (u32)(((m_rg * 4 + gm) * 8 + gw_) * 16 + (gr - gw_ * 10)) * 16, u32x4{__float_as_uint(hx), __float_as_uint(hy), tlo, thi});
            }
            BAR();
        }
        STAMP(1);
        wait_until(tstart, isM ? D_CKV : D_CKV2);
        const int kq = w >> 1, ht = w & 1;
        const u32 qoff = OFF_E3 + (u32)(kq * 12 * 32 + ht * 16 + (lane & 15)) * 64 + (lane >> 4) * 16;
        const u32 pfoff = OFF_PF + (u32)(w * 16 + (lane & 15)) * 16;
        u32x4 pf, sent;
#pragma unroll
        for (int j = CKV1; j < 12; ++j) {
            dma16nt(src, koff[j], lbase + L_KV + (u32)(w * 12 + j) * 1024);
            if (j == JPF) pf = ald4(rws4, pfoff);
            if (j - CKV1 >= YW) vmwait<YW>();
        }
        SB();
        STAMP(2);
        await<11 - JPF>(pf);
        if (anyl(tagbad(pf, tlo, thi))) {
            for (int it = 0; it < SPIN; ++it) { CFENCE();
                pf = ald4(rws4, pfoff); await<0>(pf);
                if (!anyl(tagbad(pf, tlo, thi))) break;
            }
        }
        const u32 opz = OFF_O + (u32)yi * 49152 + t * 16;
        const int wrg = yi % 12, wcs = yi / 12;
#define YPOISON() do { \
            st4(rws, (t < 16 && yi < 120) ? OFF_E6 + (u32)(wrg * 640 + wcs * 64) * 4 + lane * 16 : OOB, PZ); \
            SB(); } while (0)
        wait_until(tstart, D_S);
        sent = ald4(rws4, qoff + 11 * 2048);
        await<0>(sent);
        BAR();
        STAMP(3);
        u32x4 ow[8];
        const rsrc_t ro = mkrs(wo, 6144 * DM * 2);
        const u32 wobase = yi < 120 ? (u32)((wrg * 512 + w * 64 + (lane >> 3) * 8) * DM + wcs * 64 + (lane & 7) * 8) * 2 : OOB;
        u32x4 pf2;
        const u32 pf2off = OFF_PF2 + (u32)(kq * 32 + (lane & 31)) * 16;
        u32x4 va[6][2];
        {
            u32x4 qf[12];
            if (anyl(pois4(sent))) {
                for (int i2 = 0; i2 < SPIN; ++i2) { CFENCE();
                    sent = ald4(rws4, qoff + 11 * 2048); await<0>(sent);
                    if (!anyl(pois4(sent))) break; PSLEEP();
                }
            }
            STAMP(5);
            qf[11] = sent;
#pragma unroll
            for (int j = 0; j < 11; ++j) qf[j] = ld4(rws, qoff + j * 2048, AUX_SC1);
            SB();
            YPOISON();
            const int r = lane & 15;
            const int keyr = 8 * (r >> 3) + 2 * (r & 3) + ((r >> 2) & 1);
            u32x4 af[6][4];
#pragma unroll
            for (int ks = 0; ks < 6; ++ks) {
                const int ch = kq * 24 + ks * 4 + (lane >> 4);
                const int p = (ch & ~15) | ((ch ^ keyr) & 15);
#pragma unroll
                for (int kt = 0; kt < 4; ++kt) { af[ks][kt] = *(LAS u32x4*)(L + L_KV + (kt * 16 + keyr) * 1536 + p * 16); }
            }
            SB();
#define VAREADS() do { \
            { \
                const int g = lane >> 4, e = (lane >> 2) & 3, pp = lane & 3; \
_Pragma("unroll") \
                for (int q = 0; q < 6; ++q) { \
                    const int ch = 12 * w + 3 * pp + (q >> 1), hf = (q ^ (pp >> 1)) & 1; \
_Pragma("unroll") \
                    for (int k2 = 0; k2 < 2; ++k2) { \
                        const int ka = k2 * 32 + 8 * g + 2 * e, kb = ka + 1; \
                        const int pa = (ch & ~15) | ((ch ^ ka) & 15), pbx = (ch & ~15) | ((ch ^ kb) & 15); \
                        const s16x4 t0 = __builtin_amdgcn_ds_read_tr16_b64_v4i16((LAS s16x4*)(L + L_KV + ka * 1536 + pa * 16 + hf * 8)); \
                        const s16x4 t1 = __builtin_amdgcn_ds_read_tr16_b64_v4i16((LAS s16x4*)(L + L_KV + kb * 1536 + pbx * 16 + hf * 8)); \
                        const u32x2 a0 = __builtin_bit_cast(u32x2, t0), a1 = __builtin_bit_cast(u32x2, t1); \
                        va[q][k2] = u32x4{a0.x, a0.y, a1.x, a1.y}; \
                    } \
                } \
            } \
            } while (0)
            f32x4 acc[4];
            u32 pm;
#define QKPASS() do { \
            _Pragma("unroll") for (int kt = 0; kt < 4; ++kt) acc[kt] = f32x4{0.f, 0.f, 0.f, 0.f}; \
            pm = 0; \
            _Pragma("unroll") for (int j = 0; j < 12; ++j) { \
                _Pragma("unroll") for (int kt = 0; kt < 4; ++kt) acc[kt] = mfma(af[j >> 1][kt], qf[j], acc[kt]); \
                pm = max(pm, max(max(qf[j].x, qf[j].y), max(qf[j].z, qf[j].w))); \
            } } while (0)
            QKPASS();
            if (anyl(pm == POISON)) {
                for (int it = 0; it < SPIN; ++it) { CFENCE();
#pragma unroll
                    for (int j = 0; j < 12; ++j) qf[j] = ld4(rws, qoff + j * 2048, AUX_SC1);
                    SB();
                    QKPASS();
                    if (!anyl(pm == POISON)) break;
                }
            }
            STAMP(6);
#pragma unroll
            for (int j = 0; j < WO1; ++j) ow[j] = ld4(ro, wobase + j * 1280, AUX_NT);
            SB();
            LAS float* sp = (LAS float*)(L + L_SP);
#pragma unroll
            for (int kt = 0; kt < 4; ++kt) *(LAS f32x4*)(sp + (kq * 32 + ht * 16 + (lane & 15)) * 68 + kt * 16 + 4 * (lane >> 4)) = acc[kt];
            SB();
            VAREADS();
            SB();
        }
        BAR();
        float sm_m, sm_l;
        {
            LAS float* sp = (LAS float*)(L + L_SP);
            const int hd = t >> 4, qd = t & 15;
            f32x4 s = *(LAS f32x4*)(sp + hd * 68 + qd * 4);
#pragma unroll
            for (int k = 1; k < 4; ++k) s += *(LAS f32x4*)(sp + (k * 32 + hd) * 68 + qd * 4);
            const float m = row_max(fmaxf(fmaxf(s.x, s.y), fmaxf(s.z, s.w)));
            const float p0 = __builtin_amdgcn_exp2f(s.x - m), p1 = __builtin_amdgcn_exp2f(s.y - m), p2 = __builtin_amdgcn_exp2f(s.z - m), p3 = __builtin_amdgcn_exp2f(s.w - m);
            const u32 b01 = cvtpk(p0, p1), b23 = cvtpk(p2, p3);
            *(LAS u32x2*)(L + L_P + hd * 144 + qd * 8) = u32x2{b01, b23};
            const float l = row_sum((bflo(b01) + bfhi(b01)) + (bflo(b23) + bfhi(b23)));
            sm_m = m; sm_l = l;
        }
        BAR();
        STAMP(7);
        {
            u32x4 pb[2][2];
#pragma unroll
            for (int hh = 0; hh < 2; ++hh)
#pragma unroll
                for (int k2 = 0; k2 < 2; ++k2) pb[hh][k2] = *(LAS u32x4*)(L + L_P + (hh * 16 + (lane & 15)) * 144 + k2 * 64 + (lane >> 4) * 16);
            STAMP(4);
            {
                const u32 ob2 = OFF_O + (u32)yi * 49152 + (u32)(lane >> 4) * 4 * 1536 + (u32)w * 192 + (lane & 15) * 12;
#pragma unroll
                for (int hh = 0; hh < 2; ++hh) {
                    f32x4 d[6];
#pragma unroll
                    for (int q = 0; q < 6; ++q) { d[q] = mfma(pb[hh][0], va[q][0], f32x4{0.f, 0.f, 0.f, 0.f}); }
#pragma unroll
                    for (int q = 0; q < 6; ++q) { d[q] = mfma(pb[hh][1], va[q][1], d[q]); }
#pragma unroll
                    for (int e = 0; e < 4; ++e)
                        __builtin_amdgcn_raw_buffer_store_b96(u32x3{cvtpk(d[0][e], d[1][e]), cvtpk(d[2][e], d[3][e]), cvtpk(d[4][e], d[5][e])}, rws, ob2 + (hh * 16 + e) * 1536, 0, AUX_SC1);
                }
            }
            SB();
#pragma unroll
            for (int j = 0; j < WOE; ++j) ow[WO1 + j] = ld4(ro, wobase + (WO1 + j) * 1280, AUX_NT);
            SB();
            vmwait<WOE>();
            STAMP(8);
            asm volatile("s_barrier" ::: "memory");
            st4(rws, (t & 15) == 0 ? OFF_ML + (u32)((t >> 4) * 128 + yi) * 16 : OOB, u32x4{__float_as_uint(sm_m), __float_as_uint(sm_l), tlo, thi});
            SB();
        }
        STAMP(9);
        if (yi < 120) {
#pragma unroll
            for (int j = WO1 + FLAGO * WOE; j < 8; ++j) {
                if (WOA > 0 && j == WO1 + FLAGO * WOE) wait_until(tstart, D_WA);
                if (j == WO1 + FLAGO * WOE + WOA) { SB(); wait_until(tstart, D_WO); SB(); }
                ow[j] = ld4(ro, wobase + j * 1280, AUX_NT); }
            SB();
            f32x2 of[8][4];
#pragma unroll
            for (int j = 0; j < 8; ++j) { of[j][0] = up2(ow[j].x); of[j][1] = up2(ow[j].y); of[j][2] = up2(ow[j].z); of[j][3] = up2(ow[j].w); PIN1(of[j][0]); PIN1(of[j][1]); PIN1(of[j][2]); PIN1(of[j][3]); }
            STAMP(10);
            const int cc8 = lane & 7;
            const u32 goff = OFF_E5 + (u32)((cc8 < 4 ? 0 : 2) * 3072 + wrg * 256 + w * 32 + (lane >> 3) * 4 + (cc8 < 4 ? cc8 : 7 - cc8)) * 16;
            u32x4 g0, g1;
            wait_until(tstart, D_E5);
            for (int it = 0; it < SPIN; ++it) { CFENCE();
                g0 = ld4(rws, goff, AUX_SC1); g1 = ld4(rws, goff + 3072 * 16, AUX_SC1);
                if (!anyl(tagbad(g0, tlo, thi) | tagbad(g1, tlo, thi))) break; PSLEEP();
            }
            STAMP(11);
            float vx = __uint_as_float(g0.x) + __uint_as_float(g1.x), vy = __uint_as_float(g0.y) + __uint_as_float(g1.y);
            vx += dpp<0x141>(vx); vy += dpp<0x141>(vy);
            vx = dppb<0x1B, 0xA>(vx, vx); vy = dppb<0x1B, 0xA>(vy, vy);
            f32x2 a2[4] = {{0.f, 0.f}, {0.f, 0.f}, {0.f, 0.f}, {0.f, 0.f}};
#define WSTEP(k) { const f32x2 hx = splat(dpp<k * 0x55>(vx)), hy = splat(dpp<k * 0x55>(vy)); _Pragma("unroll") for (int i = 0; i < 4; ++i) { a2[i] += hx * of[2 * k][i]; a2[i] += hy * of[2 * k + 1][i]; } }
            WSTEP(0) WSTEP(1) WSTEP(2) WSTEP(3)
#undef WSTEP
            float a[8] = {a2[0].x, a2[0].y, a2[1].x, a2[1].y, a2[2].x, a2[2].y, a2[3].x, a2[3].y};
            const float v = rscat8(a, lane);
            const int e = ((lane >> 5) & 1) * 4 + ((lane >> 4) & 1) * 2 + ((lane >> 3) & 1);
            LAS float* red = (LAS float*)(L + L_RW);
            red[w * 64 + (lane & 7) * 8 + e] = v;
            BAR();
            if (w == 0 && lane < 16) {
                f32x4 p[8];
#pragma unroll
                for (int k = 0; k < 8; ++k) p[k] = *(LAS f32x4*)(red + k * 64 + lane * 4);
                const f32x4 s4 = ((p[0] + p[1]) + (p[2] + p[3])) + ((p[4] + p[5]) + (p[6] + p[7]));
                st4(rws, OFF_E6 + (u32)(wrg * 640 + wcs * 64) * 4 + lane * 16, u32x4{canonf(s4.x), canonf(s4.y), canonf(s4.z), canonf(s4.w)});
            }
            STAMP(12);
        }
    }
}

extern "C" void kernel_launch(void* const* d_in, const int* in_sizes, int n_in, void* d_out, int out_size, void* d_ws, size_t ws_size, hipStream_t stream) {
    (void)in_sizes; (void)n_in; (void)out_size; (void)ws_size;
    hipFuncSetAttribute((const void*)adaln_mla_mlp_kernel, hipFuncAttributeMaxDynamicSharedMemorySize, LDS_BYTES);
    adaln_mla_mlp_kernel<<<NWG, NT, LDS_BYTES, stream>>>(
        (const u16*)d_in[0], (const u16*)d_in[1], (const u16*)d_in[2], (const u16*)d_in[3], (const u16*)d_in[4], (const u16*)d_in[5],
        (const u16*)d_in[7], (const u16*)d_in[8], (const u16*)d_in[9], (const u16*)d_in[10], (u16*)d_out, (unsigned char*)d_ws);
}
```

```cpp
#include <hip/hip_runtime.h>
#include <stdint.h>

typedef unsigned u32;
typedef unsigned long long u64;
typedef unsigned short u16;
typedef u32 u32x2 __attribute__((ext_vector_type(2)));
typedef u32 u32x3 __attribute__((ext_vector_type(3)));
typedef u32 u32x4 __attribute__((ext_vector_type(4)));
typedef float f32x2 __attribute__((ext_vector_type(2)));
typedef float f32x4 __attribute__((ext_vector_type(4)));
typedef short bf16x8 __attribute__((ext_vector_type(8)));
typedef short s16x4 __attribute__((ext_vector_type(4)));
typedef __amdgpu_buffer_rsrc_t rsrc_t;
#define LAS __attribute__((address_space(3)))
#define GAS __attribute__((address_space(1)))

constexpr int NH = 32, SK = 8192, HD = 192, DM = 640, DC = 768, DFF = 1536;
constexpr int NWG = 256, NT = 512;

#pragma clang diagnostic ignored "-Winline-asm"
#define UK1 4
#define D_WQ 0
#define D_CKV 350
#define D_CKV2 350
#define CKV1 0
#define D_PO 560
#define D_UV 730
#define XW 4
#define YW 3
#define D_E1 260
#define D_E2 40
#define JPF 7
#define ML_SLEEP 4
#define WO1 0
#define FIX1 1
#define FIX2 0
#define GPOS 3
#define UV1 12
#define GY 0
#define D_S 670
#define FULLG 0
#define DIRO 2
#define VAPOS 0
#define PINAF 0
#define YPOIS 0
#define DUPKV 0
#define UVA 0
#define D_G 0
#define W1P 1
#define D_WO 1280
#define PFM 0
#define D_PW 980
#define WOA 0
#define D_WA 0
#define QLIN 0
#define D_ML 1170
#define D_E5 1440
#define D_E6 1580
#define D_H 1775
#define W2P 0
#define D_W2 0
#define W2A 2
#define W1A 2
#define F2MAP 0
#define TLBW 0
#define E6D 2
#define LEAN3 1
#define OFMA 1
#define E5P 1
#define PPC 1
#define TREF 0
#define D_HS 0
#define F2N 4
#define FLAGO 1
#define WOE 0
constexpr u32 OFF_EPOCH = 0;
constexpr u32 OFF_PF = 256;
constexpr u32 OFF_PF2 = OFF_PF + 128 * 16;
constexpr u32 OFF_E1 = OFF_PF2 + 128 * 16;
constexpr u32 OFF_E2 = OFF_E1 + 8 * 4 * 8 * 256;
constexpr u32 OFF_E3 = OFF_E2 + 4 * 32 * 96 * 16;
constexpr u32 OFF_ML = OFF_E3 + 32 * 4 * 768;
constexpr u32 OFF_E5 = OFF_ML + 32 * 128 * 16;
constexpr u32 OFF_E6 = OFF_E5 + 4 * 3072 * 16;
constexpr u32 OFF_G = OFF_E6 + 12 * 640 * 4;
constexpr u32 OFF_HID = OFF_G + 1280 * 16;
constexpr u32 OFF_O = OFF_HID + 768 * 16;
constexpr u32 OFF_PP = OFF_O + 32 * 4 * 128 * 384;
constexpr u32 WS_BYTES = OFF_PP + 48 * 320 * 16;
constexpr u32 OFF_DIAG = WS_BYTES;
static_assert(OFF_O % 128 == 0 && OFF_E3 % 128 == 0 && OFF_E5 % 128 == 0 && OFF_E2 % 256 == 0 && OFF_E6 % 128 == 0 && OFF_E1 % 256 == 0, "alignment");

constexpr int L_KV = 0;
constexpr int OSTR = 1552;
constexpr int L_SP = 98304;
constexpr int L_P = L_SP + 4 * 32 * 68 * 4;
constexpr int L_RM = L_P + 32 * 144;
constexpr int L_RW = L_RM + 8 * 128 * 4;
constexpr int L_ML = L_RW + 8 * 64 * 4;
constexpr int L_TR = L_ML + 256;
constexpr int LDS_BYTES = L_TR + 16;
constexpr int L_RQ = 0;
constexpr int L_Q = L_RQ + 6144;
constexpr int L_RA = L_Q + 768;
constexpr int L_WL = L_RA + 768;
constexpr int L_RV1 = L_WL + 1024;
constexpr int L_RV2 = L_RV1 + 6144;
constexpr int L_RG = L_RV2 + 6144;
constexpr int L_GS = L_RG + 1024;
constexpr int L_RP = L_GS + 7680;
constexpr int L_RF = L_RP + 2048;
constexpr int L_ST = L_RF + 1024;
constexpr int L_R2 = L_ST + 64;
constexpr int L_R6 = L_R2 + 512;
constexpr int SPIN = 1 << 16;
constexpr u32 POISON = 0xFFFFFFFFu;
constexpr u32 OOB = 0x80000000u;
constexpr int AUX_SC1 = 16, AUX_NT = 2;
constexpr u64 TAGMUL = 0x9E3779B97F4A7C15ull;

__device__ __forceinline__ float bflo(u32 v) { return __uint_as_float(v << 16); }
__device__ __forceinline__ float bfhi(u32 v) { return __uint_as_float(v & 0xFFFF0000u); }
__device__ __forceinline__ float bf1(u16 v) { return __uint_as_float((u32)v << 16); }
typedef __bf16 bf16x2_t __attribute__((ext_vector_type(2)));
__device__ __forceinline__ u32 cvtpk(float lo, float hi) { f32x2 v = {lo, hi}; return __builtin_bit_cast(u32, __builtin_convertvector(v, bf16x2_t)); }
__device__ __forceinline__ u32 canon(u32 v) { return v < 0xFFFFFFFEu ? v : 0xFFFFFFFEu; }
__device__ __forceinline__ u32 canonf(float v) { return canon(__float_as_uint(v)); }
__device__ __forceinline__ rsrc_t mkrs(const void* p, u32 bytes) { return __builtin_amdgcn_make_buffer_rsrc((void*)p, 0, bytes, 0x00020000); }
__device__ __forceinline__ u32x4 ld4(rsrc_t r, u32 off, int aux) { return aux == AUX_SC1 ? __builtin_amdgcn_raw_buffer_load_b128(r, off, 0, AUX_SC1) : __builtin_amdgcn_raw_buffer_load_b128(r, off, 0, AUX_NT); }
__device__ __forceinline__ u32x3 ld3(rsrc_t r, u32 off, int aux) { return aux == AUX_SC1 ? __builtin_amdgcn_raw_buffer_load_b96(r, off, 0, AUX_SC1) : __builtin_amdgcn_raw_buffer_load_b96(r, off, 0, AUX_NT); }
__device__ __forceinline__ void st4(rsrc_t r, u32 off, u32x4 v) { __builtin_amdgcn_raw_buffer_store_b128(v, r, off, 0, AUX_SC1); }
__device__ __forceinline__ void st2(rsrc_t r, u32 off, u32x2 v) { __builtin_amdgcn_raw_buffer_store_b64(v, r, off, 0, AUX_SC1); }
#define CFENCE() asm volatile("" ::: "memory")
#define PSL 0
#define PSLEEP() do { if (PSL > 0) __builtin_amdgcn_s_sleep(PSL); } while (0)
#define SB() __builtin_amdgcn_sched_barrier(0)
#define BAR() asm volatile("s_waitcnt lgkmcnt(0)\n\ts_barrier" ::: "memory")
#define LGKM0() asm volatile("s_waitcnt lgkmcnt(0)" ::: "memory")
#define DRAIN() asm volatile("s_waitcnt vmcnt(0)" ::: "memory")

template <int CTRL> __device__ __forceinline__ float dpp(float x) { return __int_as_float(__builtin_amdgcn_update_dpp(0, __float_as_int(x), CTRL, 0xf, 0xf, true)); }
__device__ __forceinline__ float rs32(float a, float b) { auto r = __builtin_amdgcn_permlane32_swap(__float_as_uint(a), __float_as_uint(b), false, false); return __uint_as_float(r[0]) + __uint_as_float(r[1]); }
__device__ __forceinline__ float rs16(float a, float b) { auto r = __builtin_amdgcn_permlane16_swap(__float_as_uint(a), __float_as_uint(b), false, false); return __uint_as_float(r[0]) + __uint_as_float(r[1]); }
__device__ __forceinline__ float row_sum(float x) { x += dpp<0x128>(x); x += dpp<0x124>(x); x += dpp<0x122>(x); x += dpp<0x121>(x); return x; }
__device__ __forceinline__ float row_max(float x) { x = fmaxf(x, dpp<0x128>(x)); x = fmaxf(x, dpp<0x124>(x)); x = fmaxf(x, dpp<0x122>(x)); x = fmaxf(x, dpp<0x121>(x)); return x; }
__device__ __forceinline__ float wave_sum(float x) { x = row_sum(x); x = rs16(x, x); x = rs32(x, x); return x; }
__device__ __forceinline__ float wave_max(float x) {
    x = row_max(x);
    { auto r = __builtin_amdgcn_permlane16_swap(__float_as_uint(x), __float_as_uint(x), false, false); x = fmaxf(__uint_as_float(r[0]), __uint_as_float(r[1])); }
    { auto r = __builtin_amdgcn_permlane32_swap(__float_as_uint(x), __float_as_uint(x), false, false); x = fmaxf(__uint_as_float(r[0]), __uint_as_float(r[1])); }
    return x;
}
__device__ __forceinline__ float rscat8(const float (&a)[8], int lane) {
    float r0 = rs32(a[0], a[4]), r1 = rs32(a[1], a[5]), r2 = rs32(a[2], a[6]), r3 = rs32(a[3], a[7]);
    float s0 = rs16(r0, r2), s1 = rs16(r1, r3);
    float t0 = s0 + dpp<0x128>(s0), t1 = s1 + dpp<0x128>(s1);
    return (lane & 8) ? t1 : t0;
}
__device__ __forceinline__ bool pois4(u32x4 v) { u32 m = max(max(v.x, v.y), max(v.z, v.w)); return m == POISON; }
__device__ __forceinline__ bool pois3(u32x3 v) { u32 m = max(max(v.x, v.y), v.z); return m == POISON; }
__device__ __forceinline__ bool anyl(bool b) { return __builtin_amdgcn_ballot_w64(b) != 0ull; }
__device__ __forceinline__ f32x4 mfma(u32x4 a, u32x4 b, f32x4 c) { return __builtin_amdgcn_mfma_f32_16x16x32_bf16(__builtin_bit_cast(bf16x8, a), __builtin_bit_cast(bf16x8, b), c, 0, 0, 0); }
__device__ __forceinline__ float silu(float v) { return v / (1.f + __expf(-v)); }
__device__ __forceinline__ void unp8(u32x4 v, float (&f)[8]) { f[0] = bflo(v.x); f[1] = bfhi(v.x); f[2] = bflo(v.y); f[3] = bfhi(v.y); f[4] = bflo(v.z); f[5] = bfhi(v.z); f[6] = bflo(v.w); f[7] = bfhi(v.w); }
__device__ __forceinline__ void unp6(u32x3 v, float (&f)[6]) { f[0] = bflo(v.x); f[1] = bfhi(v.x); f[2] = bflo(v.y); f[3] = bfhi(v.y); f[4] = bflo(v.z); f[5] = bfhi(v.z); }

__device__ __forceinline__ void wait_until(u64 t0, int ticks) {
    if (ticks > 0) { for (int i = 0; i < 4096; ++i) { if (__builtin_amdgcn_s_memrealtime() - t0 >= (u64)ticks) break; __builtin_amdgcn_s_sleep(1); } }
}
#define STAMP(i) do { } while (0)

#define STAMP2(i) do { } while (0)
__device__ __forceinline__ void ln_stats(const float (&v)[10], float& mean, float& rstd) {
    float s = 0.f;
#pragma unroll
    for (int i = 0; i < 10; ++i) s += v[i];
    mean = wave_sum(s) * (1.f / 640.f);
    float q = 0.f;
#pragma unroll
    for (int i = 0; i < 10; ++i) { float d = v[i] - mean; q += d * d; }
    float var = wave_sum(q) * (1.f / 640.f);
    rstd = 1.f / sqrtf(var + 1e-5f);
}

#define CAS __attribute__((address_space(4)))
#define PIN1(a) asm volatile("" : "+v"(a))
template <int CTRL, int BANK> __device__ __forceinline__ float dppb(float old, float x) { return __int_as_float(__builtin_amdgcn_update_dpp(__float_as_int(old), __float_as_int(x), CTRL, 0xf, BANK, false)); }
__device__ __forceinline__ u32x2 ld2(rsrc_t r, u32 off) { return __builtin_amdgcn_raw_buffer_load_b64(r, off, 0, AUX_SC1); }
__device__ __forceinline__ u32 ld1(rsrc_t r, u32 off) { return __builtin_amdgcn_raw_buffer_load_b32(r, off, 0, AUX_SC1); }
__device__ __forceinline__ bool tagbad(u32x4 g, u32 tlo, u32 thi) { return (g.z != tlo) | (g.w != thi); }
__device__ __forceinline__ f32x2 up2(u32 v) { return f32x2{bflo(v), bfhi(v)}; }
__device__ __forceinline__ f32x2 splat(float v) { return f32x2{v, v}; }

__device__ __forceinline__ void dma16nt(const void* base, u32 voff, u32 lds) {
    asm volatile("s_mov_b32 m0, %2\n\ts_nop 0\n\tglobal_load_lds_dwordx4 %0, %1 nt" ::"v"(voff), "s"(base), "s"(lds) : "memory", "m0");
}
__device__ __forceinline__ u32 ldh(rsrc_t r, u32 off) { return __builtin_amdgcn_raw_buffer_load_b16(r, off, 0, 0); }
template <int N> __device__ __forceinline__ void vmwait() { if (N < 63) asm volatile("s_waitcnt vmcnt(%0)" ::"n"(N) : "memory"); }
__device__ __forceinline__ u32x4 ald4(u32x4 rs, u32 off) { u32x4 v; asm volatile("buffer_load_dwordx4 %0, %1, %2, 0 offen sc1" : "=v"(v) : "v"(off), "s"(rs) : "memory"); return v; }
template <int N> __device__ __forceinline__ void await(u32x4& v) { asm volatile("s_waitcnt vmcnt(%1)" : "+v"(v) : "n"(N) : "memory"); }
#define GETTAG() \
    const u64 epoch = ((u64)(u32)__builtin_amdgcn_readfirstlane(epv.y) << 32) | (u64)(u32)__builtin_amdgcn_readfirstlane(epv.x); \
    const u64 tag = (epoch + 1ull) * TAGMUL; \
    const u32 tlo = (u32)tag, thi = (u32)(tag >> 32);

#define TREFRD() tstart

__global__ __launch_bounds__(NT, 2) void adaln_mla_mlp_kernel(
    const u16* __restrict__ x, const u16* __restrict__ ckv, const u16* __restrict__ wq, const u16* __restrict__ wuk,
    const u16* __restrict__ wuv, const u16* __restrict__ wo, const u16* __restrict__ w1, const u16* __restrict__ w2,
    const u16* __restrict__ cc, const u16* __restrict__ wmod, u16* __restrict__ out, unsigned char* __restrict__ ws) {
    extern __shared__ __attribute__((aligned(16))) unsigned char smem[];
    LAS unsigned char* L = (LAS unsigned char*)smem;
    STAMP(0);
    const u64 tstart = __builtin_amdgcn_s_memrealtime();
    asm volatile("" ::"s"(x), "s"(ckv), "s"(wq), "s"(wuk), "s"(wuv), "s"(wo), "s"(w1), "s"(w2), "s"(cc), "s"(wmod), "s"(out), "s"(ws));
    const int t = threadIdx.x, lane = t & 63, w = __builtin_amdgcn_readfirstlane(t >> 6);
    const int bid = blockIdx.x;
    const rsrc_t rws = mkrs(ws, WS_BYTES);
    const u32x2 epv = ld2(rws, OFF_EPOCH);
    const u32x4 PZ = {POISON, POISON, POISON, POISON};
    const u32 lbase = (u32)(uintptr_t)L;
    const u32x4 rws4 = {(u32)(uintptr_t)ws, (u32)((uintptr_t)ws >> 32), WS_BYTES, 0x00020000u};

    if (bid < 128) {
        const int xi = bid, h = xi & 31, cq = xi >> 5;
        const rsrc_t rmod = mkrs(wmod, DM * 3840 * 2);
        const u32 e3off = OFF_E3 + (u32)((cq * 12 + (lane >> 2)) * 32 + h) * 64 + (lane & 3) * 16;
        st4(rws, (w == 7 && lane < 48) ? e3off : OOB, PZ);
        const rsrc_t rq = mkrs(wq, DM * 6144 * 2);
        const rsrc_t ruk = mkrs(wuk, DC * 6144 * 2);
        const u32 wbase = (u32)(((cq * 192 + w * 24 + (lane >> 5)) * 32 + h) * 192 + 6 * (lane & 31)) * 2;
        u32x3 qw[10], uw[12];
        wait_until(tstart, D_WQ);
        {
            const u32 base = (u32)(((cq * 160 + w * 20 + (lane >> 5)) * 32 + h) * 192 + 6 * (lane & 31)) * 2;
#pragma unroll
            for (int j = 0; j < 10; ++j) { qw[j] = ld3(rq, base + j * 2 * 12288, AUX_NT); if (j >= XW) { SB(); vmwait<XW>(); SB(); } }
            SB();
#pragma unroll
            for (int j = 0; j < UK1; ++j) { uw[j] = ld3(ruk, wbase + j * 2 * 12288, AUX_NT); if (10 + j >= XW) { SB(); vmwait<XW>(); SB(); } }
            SB();
        }
        GETTAG()
        f32x2 qf2[10][3];
#pragma unroll
        for (int j = 0; j < 10; ++j) { qf2[j][0] = up2(qw[j].x); qf2[j][1] = up2(qw[j].y); qf2[j][2] = up2(qw[j].z); PIN1(qf2[j][0]); PIN1(qf2[j][1]); PIN1(qf2[j][2]); }
        STAMP(1);
        {
            const bool act = (lane & 15) < 10;
            const u32 goff = act ? OFF_E1 + (u32)(((lane >> 4) * 4 + cq) * 8 + w) * 256 + (lane & 15) * 16 : OOB;
            wait_until(tstart, D_E1);
            u32x4 g0 = ld4(rws, goff, AUX_SC1), g1 = ld4(rws, goff + 32768, AUX_SC1);
            SB();
            asm volatile("s_barrier" ::: "memory");
            SB();
#pragma unroll
            for (int j = UK1; j < 12; ++j) uw[j] = ld3(ruk, wbase + j * 2 * 12288, AUX_NT);
            SB();
            if (anyl(act & (tagbad(g0, tlo, thi) | tagbad(g1, tlo, thi)))) {
                for (int it = 0; it < SPIN; ++it) { CFENCE();
                    g0 = ld4(rws, goff, AUX_SC1); g1 = ld4(rws, goff + 32768, AUX_SC1);
                    if (!anyl(act & (tagbad(g0, tlo, thi) | tagbad(g1, tlo, thi)))) break;
                }
            }
            STAMP(2);
            st4(rws, t == 448 ? OFF_PF + xi * 16 : OOB, u32x4{TREF ? (u32)tstart : 0u, 0u, tlo, thi});
            SB();
            float s = rs32(__uint_as_float(g0.x) + __uint_as_float(g1.x), __uint_as_float(g0.y) + __uint_as_float(g1.y));
            s = rs16(s, s);
            f32x2 a[3] = {{0.f, 0.f}, {0.f, 0.f}, {0.f, 0.f}};
#define QSTEP(j) { const f32x2 hv = splat(dpp<0x150 + j>(s)); a[0] += hv * qf2[j][0]; a[1] += hv * qf2[j][1]; a[2] += hv * qf2[j][2]; }
            QSTEP(0) QSTEP(1) QSTEP(2) QSTEP(3) QSTEP(4) QSTEP(5) QSTEP(6) QSTEP(7) QSTEP(8) QSTEP(9)
#undef QSTEP
            LAS float* red = (LAS float*)(L + L_RQ);
            {
                LAS float* dst = red + w * 192 + 6 * (lane & 31) + 3 * (lane >> 5);
                dst[0] = rs32(a[0].x, a[1].y); dst[1] = rs32(a[0].y, a[2].x); dst[2] = rs32(a[1].x, a[2].y);
            }
            BAR();
            f32x2 v = {0.f, 0.f};
            if (w < 2) {
                f32x2 p[8];
#pragma unroll
                for (int k = 0; k < 8; ++k) p[k] = *(LAS f32x2*)(red + k * 192 + t * 2);
                v = ((p[0] + p[1]) + (p[2] + p[3])) + ((p[4] + p[5]) + (p[6] + p[7]));
            }
            st4(rws, t < 96 ? OFF_E2 + (u32)((cq * 32 + h) * 96 + t) * 16 : OOB, u32x4{__float_as_uint(v.x), __float_as_uint(v.y), tlo, thi});
        }
        STAMP(3);
        f32x2 wf[12][3];
        {
            LAS float* qs = (LAS float*)(L + L_Q);
            const u32 goff = w < 6 ? OFF_E2 + (u32)(((lane >> 4) * 32 + h) * 96 + 16 * w + (lane & 15)) * 16 : OOB;
            { const u64 tn = __builtin_amdgcn_s_memrealtime(); wait_until(tn, D_E2); }
            u32x4 g = ld4(rws, goff, AUX_SC1);
            SB();
#pragma unroll
            for (int j = 0; j < 12; ++j) { wf[j][0] = up2(uw[j].x); wf[j][1] = up2(uw[j].y); wf[j][2] = up2(uw[j].z); PIN1(wf[j][0]); PIN1(wf[j][1]); PIN1(wf[j][2]); }
            if (w < 6) {
                if (anyl(tagbad(g, tlo, thi))) {
                    for (int it = 0; it < SPIN; ++it) { CFENCE();
                        g = ld4(rws, goff, AUX_SC1);
                        if (!anyl(tagbad(g, tlo, thi))) break;
                    }
                }
                float s = rs32(__uint_as_float(g.x), __uint_as_float(g.y));
                s = rs16(s, s);
                if (!(lane & 16)) qs[2 * (16 * w + (lane & 15)) + (lane >> 5)] = s;
            }
            STAMP(5);
            BAR();
            const f32x2 q0 = *(LAS f32x2*)(qs + 6 * (lane & 31)), q1 = *(LAS f32x2*)(qs + 6 * (lane & 31) + 2), q2 = *(LAS f32x2*)(qs + 6 * (lane & 31) + 4);
            float p[12];
#pragma unroll
            for (int j = 0; j < 12; ++j) { const f32x2 d = wf[j][0] * q0 + wf[j][1] * q1 + wf[j][2] * q2; p[j] = d.x + d.y; }
            float r[6], s[3];
#pragma unroll
            for (int j = 0; j < 6; ++j) r[j] = rs16(p[j], p[j + 6]);
#pragma unroll
            for (int j = 0; j < 3; ++j) {
                const float own = (lane & 8) ? r[j + 3] : r[j], oth = (lane & 8) ? r[j] : r[j + 3];
                float v = own + dpp<0x128>(oth);
                v += dpp<0x141>(v); v += dpp<0x1B>(v); v += dpp<0xB1>(v);
                s[j] = v;
            }
            LAS float* red = (LAS float*)(L + L_RA);
            {
                const int j = lane & 7;
                const float v = j == 0 ? s[0] : (j == 1 ? s[1] : s[2]);
                if (j < 3) red[w * 24 + 2 * (j + 3 * ((lane >> 3) & 1) + 6 * ((lane >> 4) & 1)) + (lane >> 5)] = v;
            }
            BAR();
            u32 o[4] = {0u, 0u, 0u, 0u};
            if (w == 0) {
                const float sc = 0.07216878364870322f * 1.4426950408889634f;
                const int c0 = ((lane >> 3) * 32 + (lane & 3) * 8) & 255;
                const f32x4 p0 = *(LAS f32x4*)(red + c0) * sc, p1 = *(LAS f32x4*)(red + c0 + 4) * sc;
                const float v8[8] = {p0.x, p0.y, p0.z, p0.w, p1.x, p1.y, p1.z, p1.w};
#pragma unroll
                for (int i = 0; i < 4; ++i) {
                    const u32 hi = cvtpk(v8[2 * i], v8[2 * i + 1]);
                    const u32 lo = cvtpk(v8[2 * i] - bflo(hi), v8[2 * i + 1] - bfhi(hi));
                    o[i] = canon((lane & 4) ? lo : hi);
                }
            }
            st4(rws, t < 48 ? e3off : OOB, u32x4{o[0], o[1], o[2], o[3]});
        }
        STAMP(6);
        const rsrc_t ruv = mkrs(wuv, DC * 6144 * 2);
        u32x3 vw[12];
        SB();
#pragma unroll
        for (int j = 0; j < UVA; ++j) vw[j] = ld3(ruv, wbase + j * 2 * 12288, AUX_NT);
        SB();
        u32x4 gw[5]; u32 cv[5];
        const int rs = lane >> 2, ck = lane & 3;
        const int gi = xi - 48;
        const int g_s = 2 * ((gi >> 4) * 8 + (gi & 7)) + ((gi >> 3) & 1);
        const int f_s = W1P ? 2 * ((xi >> 4) * 8 + (xi & 7)) + ((xi >> 3) & 1) : xi;
        const int fi = xi - 48;
        const bool isf2 = xi >= 48 && xi < 88;
        const int s2 = 5 * (fi & 7) + (fi >> 3);
        const rsrc_t r2 = mkrs(w2, DFF * DM * 2);
        u32x4 fw[6];
#define W2LOADS() do { \
        const u32 w2b = isf2 ? (u32)(2 * (t >> 1) * DM + 16 * s2 + 8 * (t & 1)) * 2 : OOB; \
        _Pragma("unroll") for (int i = 0; i < 3; ++i) { fw[2 * i] = __builtin_amdgcn_raw_buffer_load_b128(r2, w2b + (u32)(512 * i * DM) * 2, 0, W2A); fw[2 * i + 1] = __builtin_amdgcn_raw_buffer_load_b128(r2, w2b + (u32)((512 * i + 1) * DM) * 2, 0, W2A); } \
        SB(); \
        } while (0)
#define W2RLOADS() do { \
        const u32 o2 = (xi < 48 && (lane & 15) < 10) ? (u32)((32 * f_s + 8 * (lane >> 4)) * DM + 80 * w + 8 * (lane & 15)) * 2 : OOB; \
        _Pragma("unroll") for (int j = 0; j < 8; ++j) w2r[j] = __builtin_amdgcn_raw_buffer_load_b128(r2, o2 + j * DM * 2, 0, W2A); \
        SB(); \
        } while (0)
#define GLOADS() do { \
        const bool isg = xi >= 48; \
        const rsrc_t rg = mkrs(isg ? wmod : w1, isg ? DM * 3840 * 2 : DM * DFF * 2); \
        const rsrc_t rc = mkrs(isg ? cc : x, DM * 2); \
        const u32 goff0 = isg ? (GY ? OOB : (u32)((t >> 2) * 3840 + 1280 + 32 * g_s + 8 * ck) * 2) : (u32)((w * 80 + (E6D ? 4 : 1) * rs) * DFF + 32 * f_s + 8 * ck) * 2; \
        const u32 gstr = isg ? (GY ? 0 : 128 * 7680) : (E6D ? 1 : 16) * DFF * 2; \
        const u32 coff0 = isg ? (u32)(t >> 2) * 2 : (u32)(w * 80 + (E6D ? 4 : 1) * rs) * 2; \
        const u32 cstr = isg ? 256 : (E6D ? 2 : 32); \
        const u32 gx4 = (E6D && !isg) ? (u32)(60 - 3 * rs) * DFF * 2 : 0u, cx4 = (E6D && !isg) ? (u32)(60 - 3 * rs) * 2 : 0u; \
        _Pragma("unroll") for (int j = 0; j < 5; ++j) gw[j] = __builtin_amdgcn_raw_buffer_load_b128(rg, goff0 + j * gstr + (j == 4 ? gx4 : 0u), 0, W1A); \
        _Pragma("unroll") for (int j = 0; j < 5; ++j) cv[j] = ldh(rc, coff0 + j * cstr + (j == 4 ? cx4 : 0u)); \
        SB(); \
        } while (0)
#define GCOMP() do { \
        if (!GY && xi >= 48) { \
            float a[8]; \
_Pragma("unroll") \
            for (int i = 0; i < 8; ++i) a[i] = 0.f; \
_Pragma("unroll") \
            for (int j = 0; j < 5; ++j) { \
                float f[8]; unp8(gw[j], f); const float s = silu(bflo(cv[j])); \
_Pragma("unroll") \
                for (int i = 0; i < 8; ++i) a[i] += s * f[i]; \
            } \
            float v = rscat8(a, lane); \
            v += dpp<0x104>(v); \
            const int e = ((lane >> 5) & 1) * 4 + ((lane >> 4) & 1) * 2 + ((lane >> 3) & 1); \
            LAS float* redg = (LAS float*)(L + L_RG); \
            if (!(lane & 4)) redg[w * 32 + ck * 8 + e] = v; \
            BAR(); \
            if (w == 0 && lane < 16) { \
                f32x2 s = {0.f, 0.f}; \
_Pragma("unroll") \
                for (int i = 0; i < 8; ++i) s += *(LAS f32x2*)(redg + i * 32 + lane * 2); \
                st4(rws, OFF_G + (g_s * 16 + lane) * 16, u32x4{__float_as_uint(s.x), __float_as_uint(s.y), tlo, thi}); \
            } \
        } \
        } while (0)
        wait_until(tstart, D_G);
        GLOADS();
        wait_until(tstart, D_UV);
#pragma unroll
        for (int j = UVA; j < 12; ++j) vw[j] = ld3(ruv, wbase + j * 2 * 12288, AUX_NT);
        SB();
        STAMP(4);
        GCOMP();
        f32x2 vf[12][3];
#pragma unroll
        for (int j = 0; j < 12; ++j) { vf[j][0] = up2(vw[j].x); vf[j][1] = up2(vw[j].y); vf[j][2] = up2(vw[j].z); PIN1(vf[j][0]); PIN1(vf[j][1]); PIN1(vf[j][2]); }
        STAMP(7);
        u32 pfd = 0;
        {
            const u32 obase = OFF_O + (u32)(w * 16 + (lane >> 5)) * 49152 + (u32)(h * 4 + cq) * 384 + (lane & 31) * 12;
            constexpr u32 OJ = 2 * 49152;
            LAS float* red1 = (LAS float*)(L + L_RV1);
            LAS float* red2 = (LAS float*)(L + L_RV2);
            LAS f32x2* wl = (LAS f32x2*)(L + L_WL);
            u32x3 oa[8];
            {
                const u32 mlA = lane < 16 ? OFF_ML + (u32)(h * 128 + w * 16 + lane) * 16 : OOB;
                { const u64 tr_ = TREFRD(); wait_until(tr_, D_ML); }
                u32x4 ma = ld4(rws, mlA, AUX_SC1);
                if (anyl((lane < 16) & tagbad(ma, tlo, thi))) {
                    for (int it = 0; it < SPIN; ++it) { CFENCE();
                        if (ML_SLEEP > 0) __builtin_amdgcn_s_sleep(ML_SLEEP);
                        ma = ld4(rws, mlA, AUX_SC1);
                        if (!anyl((lane < 16) & tagbad(ma, tlo, thi))) break;
                    }
                }
                CFENCE();
                STAMP(8);
#pragma unroll
                for (int j = 0; j < 8; ++j) oa[j] = ld3(rws, obase + j * OJ, AUX_SC1);
                SB();
                if (lane < 16) wl[w * 16 + lane] = f32x2{__uint_as_float(ma.x), __uint_as_float(ma.y)};
            }
            BAR();
            float wA;
            {
                const f32x2 a = wl[lane], b = wl[lane + 64], own = wl[16 * w + 2 * (lane & 7) + (lane >> 5)];
                const float M = wave_max(fmaxf(a.x, b.x));
                const float Lsum = wave_sum(__builtin_amdgcn_exp2f(a.x - M) * a.y + __builtin_amdgcn_exp2f(b.x - M) * b.y);
                wA = __builtin_amdgcn_exp2f(own.x - M) * __builtin_amdgcn_rcpf(Lsum);
            }
            f32x2 pr[8][3];
            f32x2 a2[3], b2[3];
#define OSTEP(j, A, first) { const f32x2 ww = splat(dpp<0x150 + j>(wA)); if (first) { A[0] = up2(oa[j].x) * ww; A[1] = up2(oa[j].y) * ww; A[2] = up2(oa[j].z) * ww; } else { A[0] += up2(oa[j].x) * ww; A[1] += up2(oa[j].y) * ww; A[2] += up2(oa[j].z) * ww; } }
            OSTEP(0, a2, true) OSTEP(1, b2, true) OSTEP(2, a2, false) OSTEP(3, b2, false) OSTEP(4, a2, false) OSTEP(5, b2, false) OSTEP(6, a2, false) OSTEP(7, b2, false)
#undef OSTEP
            (void)pr;
            STAMP(9);
#pragma unroll
            for (int k = 0; k < 3; ++k) a2[k] += b2[k];
            {
                LAS float* dst = red1 + w * 192 + 6 * (lane & 31) + 3 * (lane >> 5);
                dst[0] = rs32(a2[0].x, a2[1].y); dst[1] = rs32(a2[0].y, a2[2].x); dst[2] = rs32(a2[1].x, a2[2].y);
            }
            STAMP2(1);
            BAR();
            STAMP2(2);
            float cx = 0.f;
            {
                const int i = lane & 15, r = 2 * (i < 12 ? i : 11) + (lane >> 5), cidx = (w >> 2) * 96 + (4 * (w & 3) + (r & 3)) * 6 + 2 * (r >> 3) + (((r >> 2) & 1) ^ ((w >> 1) & 1));
                float p[8];
#pragma unroll
                for (int k = 0; k < 8; ++k) p[k] = red1[k * 192 + cidx];
                cx = ((p[0] + p[1]) + (p[2] + p[3])) + ((p[4] + p[5]) + (p[6] + p[7]));
            }
            f32x2 c2[3] = {{0.f, 0.f}, {0.f, 0.f}, {0.f, 0.f}};
#define VSTEP(j) { const f32x2 cvv = splat(dpp<0x150 + j>(cx)); c2[0] += cvv * vf[j][0]; c2[1] += cvv * vf[j][1]; c2[2] += cvv * vf[j][2]; }
            VSTEP(0) VSTEP(1) VSTEP(2) VSTEP(3) VSTEP(4) VSTEP(5) VSTEP(6) VSTEP(7) VSTEP(8) VSTEP(9) VSTEP(10) VSTEP(11)
#undef VSTEP
            {
                LAS float* dst = red2 + w * 192 + 6 * (lane & 31) + 3 * (lane >> 5);
                dst[0] = rs32(c2[0].x, c2[1].y); dst[1] = rs32(c2[0].y, c2[2].x); dst[2] = rs32(c2[1].x, c2[2].y);
            }
            STAMP2(3);
            BAR();
            f32x2 s5 = {0.f, 0.f};
            if (w < 2) {
                f32x2 p[8];
#pragma unroll
                for (int k = 0; k < 8; ++k) p[k] = *(LAS f32x2*)(red2 + k * 192 + t * 2);
                s5 = ((p[0] + p[1]) + (p[2] + p[3])) + ((p[4] + p[5]) + (p[6] + p[7]));
            }
            st4(rws, t < 96 ? OFF_E5 + (u32)(cq * 3072 + h * 96 + t) * 16 : OOB, u32x4{__float_as_uint(s5.x), __float_as_uint(s5.y), tlo, thi});
        }
        BAR();
        STAMP(10);
        if (xi < 48) {
            LAS float* gs = (LAS float*)(L + L_GS);
            u32x4 w2r[8];
            {
                u32x4 g0, g1;
                const bool two = t < 448;
                g0 = ld4(rws, OFF_G + t * 16, AUX_SC1); g1 = ld4(rws, two ? OFF_G + (t + 512) * 16 : OFF_G + t * 16, AUX_SC1);
                SB();
                wait_until(tstart, D_W2);
                W2RLOADS();
                if (anyl(tagbad(g0, tlo, thi) | tagbad(g1, tlo, thi)))
                for (int it = 0; it < SPIN; ++it) { CFENCE();
                    g0 = ld4(rws, OFF_G + t * 16, AUX_SC1); bool bad = tagbad(g0, tlo, thi);
                    if (two) { g1 = ld4(rws, OFF_G + (t + 512) * 16, AUX_SC1); bad |= tagbad(g1, tlo, thi); }
                    if (!anyl(bad)) break; PSLEEP();
                }
                *(LAS u32x2*)(gs + 2 * t) = u32x2{g0.x, g0.y};
                if (two) *(LAS u32x2*)(gs + 2 * (t + 512)) = u32x2{g1.x, g1.y};
            }
            BAR();
            f32x2 wA[5][4], u[4], v1[4], v2[4];
            float xv[5], ga[5];
#pragma unroll
            for (int k = 0; k < 4; ++k) { u[k] = f32x2{0.f, 0.f}; v1[k] = f32x2{0.f, 0.f}; v2[k] = f32x2{0.f, 0.f}; }
#pragma unroll
            for (int j = 0; j < 5; ++j) {
                const int m = E6D ? w * 80 + (j < 4 ? 4 * rs + j : 64 + rs) : w * 80 + 16 * j + rs;
                const float gav = gs[m], sbv = gs[640 + m], cbp = 1.f + gs[1280 + m];
                xv[j] = bflo(cv[j]); ga[j] = gav;
                const f32x2 A = splat(gav * cbp), X0 = splat(xv[j] * cbp), C = splat(cbp), S = splat(sbv);
                const f32x2 f[4] = {up2(gw[j].x), up2(gw[j].y), up2(gw[j].z), up2(gw[j].w)};
#pragma unroll
                for (int k = 0; k < 4; ++k) { wA[j][k] = A * f[k]; u[k] += X0 * f[k]; v1[k] += C * f[k]; v2[k] += S * f[k]; PIN1(wA[j][k]); }
            }
            const int e = ((lane >> 5) & 1) * 4 + ((lane >> 4) & 1) * 2 + ((lane >> 3) & 1);
            LAS float* rp = (LAS float*)(L + L_RP);
            {
                float a[8] = {v1[0].x, v1[0].y, v1[1].x, v1[1].y, v1[2].x, v1[2].y, v1[3].x, v1[3].y};
                float v = rscat8(a, lane); v += dpp<0x104>(v);
                if (!(lane & 4)) rp[w * 32 + ck * 8 + e] = v;
                float b[8] = {v2[0].x, v2[0].y, v2[1].x, v2[1].y, v2[2].x, v2[2].y, v2[3].x, v2[3].y};
                v = rscat8(b, lane); v += dpp<0x104>(v);
                if (!(lane & 4)) rp[256 + w * 32 + ck * 8 + e] = v;
            }
            BAR();
            f32x2 V1 = {0.f, 0.f}, V2 = {0.f, 0.f};
            const int hp = 2 * (4 * (lane >> 4) + (lane & 3));
#pragma unroll
            for (int k = 0; k < 8; ++k) { V1 += *(LAS f32x2*)(rp + k * 32 + hp); V2 += *(LAS f32x2*)(rp + 256 + k * 32 + hp); }
            PIN1(V1); PIN1(V2);
            f32x2 w2f[8][4];
#pragma unroll
            for (int j = 0; j < 8; ++j) { w2f[j][0] = up2(w2r[j].x); w2f[j][1] = up2(w2r[j].y); w2f[j][2] = up2(w2r[j].z); w2f[j][3] = up2(w2r[j].w); PIN1(w2f[j][0]); PIN1(w2f[j][1]); PIN1(w2f[j][2]); PIN1(w2f[j][3]); }
#pragma unroll
            for (int k = 0; k < 4; ++k) PIN1(u[k]);
            STAMP(11);
            LAS float* r6 = (LAS float*)(L + L_R6);
            float atv[5];
            {
                const int pg = lane / 20, qq = lane - pg * 20;
                const u32 goff = lane < 60 ? OFF_E6 + (u32)(4 * pg * 640 + w * 80 + 4 * qq) * 4 : OOB;
                u32x4 eg[4];
                { const u64 tr_ = TREFRD(); wait_until(tr_, D_E6); }
                for (int it = 0; it < SPIN; ++it) { CFENCE();
#pragma unroll
                    for (int k = 0; k < 4; ++k) eg[k] = ld4(rws, goff + k * 2560, AUX_SC1);
                    if (!anyl(pois4(eg[0]) | pois4(eg[1]) | pois4(eg[2]) | pois4(eg[3]))) break; PSLEEP();
                }
                STAMP(12);
                const f32x4 s = (__builtin_bit_cast(f32x4, eg[0]) + __builtin_bit_cast(f32x4, eg[1])) + (__builtin_bit_cast(f32x4, eg[2]) + __builtin_bit_cast(f32x4, eg[3]));
                LAS float* rw = r6 + w * 256;
                *(LAS f32x4*)(rw + 4 * lane) = s;
                CFENCE(); __builtin_amdgcn_wave_barrier(); CFENCE();
                const f32x4 a0 = *(LAS f32x4*)(rw + 4 * rs), a1 = *(LAS f32x4*)(rw + 80 + 4 * rs), a2 = *(LAS f32x4*)(rw + 160 + 4 * rs);
                const float b0 = rw[64 + rs], b1 = rw[144 + rs], b2 = rw[224 + rs];
                const f32x4 as = (a0 + a1) + a2;
                atv[0] = as.x; atv[1] = as.y; atv[2] = as.z; atv[3] = as.w; atv[4] = (b0 + b1) + b2;
            }
            (void)r6;
            float x1[5], sum = 0.f;
#pragma unroll
            for (int j = 0; j < 5; ++j) {
                const float at = atv[j];
                x1[j] = xv[j] + ga[j] * at; sum += x1[j];
                const f32x2 A = splat(at);
#pragma unroll
                for (int k = 0; k < 4; ++k) u[k] += A * wA[j][k];
            }
            sum += dpp<0x124>(sum); sum += dpp<0x128>(sum); sum = rs16(sum, sum); sum = rs32(sum, sum);
            const float mw_ = sum * (1.f / 80.f);
            float q = 0.f;
#pragma unroll
            for (int j = 0; j < 5; ++j) { const float d = x1[j] - mw_; q += d * d; }
            q += dpp<0x124>(q); q += dpp<0x128>(q); q = rs16(q, q); q = rs32(q, q);
            {
                float a[8] = {u[0].x, u[0].y, u[1].x, u[1].y, u[2].x, u[2].y, u[3].x, u[3].y};
                float v = rscat8(a, lane); v += dpp<0x104>(v);
                LAS float* rf = (LAS float*)(L + L_RF);
                if (!(lane & 4)) rf[w * 32 + ck * 8 + e] = v;
                LAS float* sst = (LAS float*)(L + L_ST);
                if (lane == 0) *(LAS f32x2*)(sst + 2 * w) = f32x2{mw_, q};
                STAMP2(4);
                BAR();
                {
                    const int kk = (lane >> 2) & 3;
                    const f32x2 pa = *(LAS f32x2*)(rf + kk * 32 + hp), pb = *(LAS f32x2*)(rf + 128 + kk * 32 + hp), s1 = *(LAS f32x2*)(sst + 2 * (lane & 7));
                    f32x2 U = pa + pb;
                    float ms = s1.x;
                    ms += dpp<0xB1>(ms); U.x += dpp<0x124>(U.x); U.y += dpp<0x124>(U.y);
                    ms += dpp<0x4E>(ms); U.x += dpp<0x128>(U.x); U.y += dpp<0x128>(U.y);
                    ms += dpp<0x141>(ms);
                    const float mean = ms * 0.125f, d = s1.x - mean;
                    float vs = s1.y + 80.f * d * d;
                    vs += dpp<0xB1>(vs); vs += dpp<0x4E>(vs); vs += dpp<0x141>(vs);
                    const float var = vs * (1.f / 640.f);
                    const float rstd = __builtin_amdgcn_rsqf(var + 1e-5f);
                    const float h0 = fmaxf(rstd * (U.x - mean * V1.x) + V2.x, 0.f), h1 = fmaxf(rstd * (U.y - mean * V1.y) + V2.y, 0.f);
                    f32x2 o2[4] = {{0.f, 0.f}, {0.f, 0.f}, {0.f, 0.f}, {0.f, 0.f}};
#define HSTEP(k) { const f32x2 ha = splat(dpp<0x150 + k>(h0)), hb = splat(dpp<0x150 + k>(h1)); _Pragma("unroll") for (int i = 0; i < 4; ++i) { o2[i] += ha * w2f[2 * k][i]; o2[i] += hb * w2f[2 * k + 1][i]; } }
                    HSTEP(0) HSTEP(1) HSTEP(2) HSTEP(3)
#undef HSTEP
                    const float r0 = rs32(o2[0].x, o2[2].x), r1 = rs32(o2[0].y, o2[2].y), r2_ = rs32(o2[1].x, o2[3].x), r3 = rs32(o2[1].y, o2[3].y);
                    const float q0 = rs16(r0, r2_), q1 = rs16(r1, r3);
                    const int srcl = ((lane >> 2) & 15) + 16 * (lane & 3);
                    const u32 z0 = (u32)__builtin_amdgcn_ds_bpermute(srcl * 4, __float_as_int(q0)), z1 = (u32)__builtin_amdgcn_ds_bpermute(srcl * 4, __float_as_int(q1));
                    st4(rws, lane < 40 ? OFF_PP + (u32)(f_s * 320 + 40 * w + lane) * 16 : OOB, u32x4{z0, z1, tlo, thi});
                }
                if (false) {
                    f32x2 p[8], s8[8];
#pragma unroll
                    for (int k = 0; k < 8; ++k) { p[k] = *(LAS f32x2*)(rf + k * 32 + lane * 2); s8[k] = *(LAS f32x2*)(sst + 2 * k); }
                    const f32x2 U = ((p[0] + p[1]) + (p[2] + p[3])) + ((p[4] + p[5]) + (p[6] + p[7]));
                    const f32x2 SS = ((s8[0] + s8[1]) + (s8[2] + s8[3])) + ((s8[4] + s8[5]) + (s8[6] + s8[7]));
                    const float mean = SS.x * 0.125f;
                    float dv = 0.f;
#pragma unroll
                    for (int k = 0; k < 8; ++k) { const float d = s8[k].x - mean; dv += d * d; }
                    const float var = (SS.y + 80.f * dv) * (1.f / 640.f);
                    const float rstd = __builtin_amdgcn_rsqf(var + 1e-5f);
                    const float h0 = fmaxf(rstd * (U.x - mean * V1.x) + V2.x, 0.f), h1 = fmaxf(rstd * (U.y - mean * V1.y) + V2.y, 0.f);
                    st4(rws, OFF_HID + (u32)(f_s * 16 + lane) * 16, u32x4{__float_as_uint(h0), __float_as_uint(h1), tlo, thi});
                }
                STAMP2(6);
            }

        } else if (isf2) {
            if (w == 0) {
                const int r = lane & 7, pq = lane >> 3;
                const u32 xx = *(const u32*)(x + 16 * s2 + 2 * r);
                u32x4 ga0, gb0;
                for (int it = 0; it < SPIN; ++it) { CFENCE();
                    ga0 = ld4(rws, OFF_G + (u32)(8 * s2 + r) * 16, AUX_SC1); gb0 = ld4(rws, OFF_G + (u32)(960 + 8 * s2 + r) * 16, AUX_SC1);
                    if (!anyl(tagbad(ga0, tlo, thi) | tagbad(gb0, tlo, thi))) break; PSLEEP();
                }
                u32x2 e0, e1;
                const u32 eo = OFF_E6 + (u32)(pq * 640 + 16 * s2 + 2 * r) * 4;
                { const u64 tr_ = TREFRD(); wait_until(tr_, D_E6); }
                for (int it = 0; it < SPIN; ++it) { CFENCE();
                    e0 = ld2(rws, eo); e1 = ld2(rws, pq < 4 ? eo + 8 * 2560 : OOB);
                    if (!anyl((max(e0.x, e0.y) == POISON) | (max(e1.x, e1.y) == POISON))) break; PSLEEP();
                }
                float ax = __uint_as_float(e0.x) + __uint_as_float(e1.x), ay = __uint_as_float(e0.y) + __uint_as_float(e1.y);
                ax += dpp<0x128>(ax); ay += dpp<0x128>(ay); ax = rs16(ax, ax); ay = rs16(ay, ay); ax = rs32(ax, ax); ay = rs32(ay, ay);
                f32x2 x1 = {bflo(xx) + __uint_as_float(ga0.x) * ax, bfhi(xx) + __uint_as_float(ga0.y) * ay};
                f32x2 gb = {__uint_as_float(gb0.x), __uint_as_float(gb0.y)};
                PIN1(x1); PIN1(gb);
                STAMP(11);
                u32x4 pg[6];
                const u32 po = OFF_PP + (u32)(pq * 320 + 8 * s2 + r) * 16;
                { const u64 tr_ = TREFRD(); wait_until(tr_, D_H); }
                for (int it = 0; it < SPIN; ++it) { CFENCE();
                    bool bad = false;
#pragma unroll
                    for (int j = 0; j < 6; ++j) pg[j] = ld4(rws, po + j * 8 * 5120, AUX_SC1);
#pragma unroll
                    for (int j = 0; j < 6; ++j) bad |= tagbad(pg[j], tlo, thi);
                    if (!anyl(bad)) break; PSLEEP();
                }
                STAMP(12);
                float sx = ((__uint_as_float(pg[0].x) + __uint_as_float(pg[1].x)) + (__uint_as_float(pg[2].x) + __uint_as_float(pg[3].x))) + (__uint_as_float(pg[4].x) + __uint_as_float(pg[5].x));
                float sy = ((__uint_as_float(pg[0].y) + __uint_as_float(pg[1].y)) + (__uint_as_float(pg[2].y) + __uint_as_float(pg[3].y))) + (__uint_as_float(pg[4].y) + __uint_as_float(pg[5].y));
                sx += dpp<0x128>(sx); sy += dpp<0x128>(sy); sx = rs16(sx, sx); sy = rs16(sy, sy); sx = rs32(sx, sx); sy = rs32(sy, sy);
                if (lane < 8) *(u32*)(out + 16 * s2 + 2 * r) = cvtpk(x1.x + gb.x * sx, x1.y + gb.y * sy);
                if (xi == 48 && lane == 0) __hip_atomic_store((GAS u64*)(ws + OFF_EPOCH), epoch + 1ull, __ATOMIC_RELAXED, __HIP_MEMORY_SCOPE_AGENT);
            }
        } else if (false) {
            const int chunk = t & 1, q = t >> 1;
            W2LOADS();
            f32x4 x1 = {0.f, 0.f, 0.f, 0.f}, gb = {0.f, 0.f, 0.f, 0.f};
            if (w == 0) {
                const int c4 = lane & 3, p = lane >> 2;
                const u32x2 xx = *(const u32x2*)(x + 16 * s2 + 4 * c4);
                u32x4 ga0, ga1, gb0, gb1;
                for (int it = 0; it < SPIN; ++it) { CFENCE();
                    ga0 = ld4(rws, OFF_G + (u32)(8 * s2 + 2 * c4) * 16, AUX_SC1); ga1 = ld4(rws, OFF_G + (u32)(8 * s2 + 2 * c4 + 1) * 16, AUX_SC1);
                    gb0 = ld4(rws, OFF_G + (u32)(960 + 8 * s2 + 2 * c4) * 16, AUX_SC1); gb1 = ld4(rws, OFF_G + (u32)(960 + 8 * s2 + 2 * c4 + 1) * 16, AUX_SC1);
                    if (!anyl(tagbad(ga0, tlo, thi) | tagbad(ga1, tlo, thi) | tagbad(gb0, tlo, thi) | tagbad(gb1, tlo, thi))) break; PSLEEP();
                }
                u32x4 g = {0u, 0u, 0u, 0u};
                { const u64 tr_ = TREFRD(); wait_until(tr_, D_E6); }
                for (int it = 0; it < SPIN; ++it) { CFENCE();
                    if (p < 12) g = ld4(rws, OFF_E6 + (u32)(p * 640 + 16 * s2 + 4 * c4) * 4, AUX_SC1);
                    if (!anyl(pois4(g))) break; PSLEEP();
                }
                f32x4 at = __builtin_bit_cast(f32x4, g);
#pragma unroll
                for (int k = 0; k < 4; ++k) { float v = at[k]; v += dpp<0x124>(v); v += dpp<0x128>(v); v = rs16(v, v); v = rs32(v, v); at[k] = v; }
                x1 = f32x4{bflo(xx.x) + __uint_as_float(ga0.x) * at.x, bfhi(xx.x) + __uint_as_float(ga0.y) * at.y, bflo(xx.y) + __uint_as_float(ga1.x) * at.z, bfhi(xx.y) + __uint_as_float(ga1.y) * at.w};
                gb = f32x4{__uint_as_float(gb0.x), __uint_as_float(gb0.y), __uint_as_float(gb1.x), __uint_as_float(gb1.y)};
                asm volatile("" : "+v"(x1), "+v"(gb));
            }
            f32x2 ff[6][4];
#pragma unroll
            for (int j = 0; j < 6; ++j) { ff[j][0] = up2(fw[j].x); ff[j][1] = up2(fw[j].y); ff[j][2] = up2(fw[j].z); ff[j][3] = up2(fw[j].w); PIN1(ff[j][0]); PIN1(ff[j][1]); PIN1(ff[j][2]); PIN1(ff[j][3]); }
            STAMP(11);
            u32x4 hg[3];
            { const u64 tr_ = TREFRD(); wait_until(tr_, D_H); }
            for (int it = 0; it < SPIN; ++it) { CFENCE();
#pragma unroll
                for (int i = 0; i < 3; ++i) hg[i] = ld4(rws, OFF_HID + (u32)(q + 256 * i) * 16, AUX_SC1);
                if (!anyl(tagbad(hg[0], tlo, thi) | tagbad(hg[1], tlo, thi) | tagbad(hg[2], tlo, thi))) break; PSLEEP();
            }
            STAMP(12);
            f32x2 acc[4] = {{0.f, 0.f}, {0.f, 0.f}, {0.f, 0.f}, {0.f, 0.f}};
#pragma unroll
            for (int i = 0; i < 3; ++i) {
                const f32x2 h0 = splat(__uint_as_float(hg[i].x)), h1 = splat(__uint_as_float(hg[i].y));
#pragma unroll
                for (int k = 0; k < 4; ++k) { acc[k] += h0 * ff[2 * i][k]; acc[k] += h1 * ff[2 * i + 1][k]; }
            }
            float a[8] = {acc[0].x, acc[0].y, acc[1].x, acc[1].y, acc[2].x, acc[2].y, acc[3].x, acc[3].y};
            float v = rscat8(a, lane);
            v += dpp<0x4E>(v); v += dpp<0x104>(v);
            const int e = ((lane >> 5) & 1) * 4 + ((lane >> 4) & 1) * 2 + ((lane >> 3) & 1);
            LAS float* r2l = (LAS float*)(L + L_R2);
            if (!(lane & 6)) r2l[w * 16 + chunk * 8 + e] = v;
            STAMP2(5);
            BAR();
            STAMP2(4);
            if (t < 4) {
                f32x4 p[8];
#pragma unroll
                for (int k = 0; k < 8; ++k) p[k] = *(LAS f32x4*)(r2l + k * 16 + t * 4);
                const f32x4 s = ((p[0] + p[1]) + (p[2] + p[3])) + ((p[4] + p[5]) + (p[6] + p[7]));
                const f32x4 o = x1 + gb * s;
                *(u32x2*)(out + 16 * s2 + 4 * t) = u32x2{cvtpk(o.x, o.y), cvtpk(o.z, o.w)};
            }
            if (xi == 48 && t == 0) __hip_atomic_store((GAS u64*)(ws + OFF_EPOCH), epoch + 1ull, __ATOMIC_RELAXED, __HIP_MEMORY_SCOPE_AGENT);

        }
    } else {
        const int yi = bid - 128;
        const bool isM = yi < 80;
        const unsigned char* src = (const unsigned char*)ckv + (size_t)yi * 64 * 1536;
        u32 koff[12];
#define KOFF() _Pragma("unroll") for (int j = 0; j < 12; ++j) { const int idx = j * 64 + lane, row = w * 8 + idx / 96, p = idx % 96; const int ch = (p & ~15) | ((p ^ row) & 15); koff[j] = (u32)(row * 1536 + ch * 16); PIN1(koff[j]); }
        u32x4 mw[3]; u32 mc[3]; u32 xr[5], xm = 0;
        const int m_rg = yi & 7, m_s = yi >> 3;
        if (isM) {
            const rsrc_t rmod = mkrs(wmod, DM * 3840 * 2);
            const int row0 = m_rg * 80 + w * 10 + (lane >> 4);
            const u32 off = (u32)(row0 * 3840 + ((lane >> 3) & 1) * 640 + m_s * 64 + (lane & 7) * 8) * 2;
            mw[0] = ld4(rmod, off, AUX_NT); mw[1] = ld4(rmod, off + 4 * 7680, AUX_NT); mw[2] = ld4(rmod, lane < 32 ? off + 8 * 7680 : OOB, AUX_NT);
            const rsrc_t rc = mkrs(cc, DM * 2);
            mc[0] = ldh(rc, row0 * 2); mc[1] = ldh(rc, row0 * 2 + 8); mc[2] = ldh(rc, row0 * 2 + (lane < 32 ? 16 : 0));
            if (w == 0) {
#pragma unroll
                for (int i = 0; i < 5; ++i) xr[i] = ((const u32*)x)[lane + 64 * i];
                xm = ((const u32*)x)[32 * m_s + (lane & 31)];
            }
        }
        SB();
        KOFF()
        if (CKV1 > 0) {
            wait_until(tstart, D_CKV);
#pragma unroll
            for (int j = 0; j < CKV1; ++j) dma16nt(src, koff[j], lbase + L_KV + (u32)(w * 12 + j) * 1024);
            SB();
        }
        GETTAG()
        if (isM) {
            f32x2 lnx = {0.f, 0.f};
            if (w == 0) {
                float xv[10];
#pragma unroll
                for (int i = 0; i < 5; ++i) { xv[2 * i] = bflo(xr[i]); xv[2 * i + 1] = bfhi(xr[i]); }
                float mean, rstd; ln_stats(xv, mean, rstd);
                lnx = f32x2{(bflo(xm) - mean) * rstd, (bfhi(xm) - mean) * rstd};
            }
            float a[8], f[8];
            const float s0 = silu(bflo(mc[0])), s1 = silu(bflo(mc[1])), s2 = silu(bflo(mc[2]));
            unp8(mw[0], f);
#pragma unroll
            for (int i = 0; i < 8; ++i) a[i] = s0 * f[i];
            unp8(mw[1], f);
#pragma unroll
            for (int i = 0; i < 8; ++i) a[i] += s1 * f[i];
            unp8(mw[2], f);
#pragma unroll
            for (int i = 0; i < 8; ++i) a[i] += s2 * f[i];
            const float r0 = rs32(a[0], a[4]), r1 = rs32(a[1], a[5]), r2 = rs32(a[2], a[6]), r3 = rs32(a[3], a[7]);
            const f32x2 sv = {rs16(r0, r2), rs16(r1, r3)};
            LAS float* red = (LAS float*)(L + L_RM);
            *(LAS f32x2*)(red + w * 128 + ((lane >> 3) & 1) * 64 + (lane & 7) * 8 + 4 * (lane >> 5) + 2 * ((lane >> 4) & 1)) = sv;
            BAR();
            if (w == 0) {
                f32x2 p[8];
#pragma unroll
                for (int k = 0; k < 8; ++k) p[k] = *(LAS f32x2*)(red + k * 128 + 2 * lane);
                const f32x2 s = ((p[0] + p[1]) + (p[2] + p[3])) + ((p[4] + p[5]) + (p[6] + p[7]));
                auto rx = __builtin_amdgcn_permlane32_swap(__float_as_uint(s.x), __float_as_uint(s.x), false, false);
                auto ry = __builtin_amdgcn_permlane32_swap(__float_as_uint(s.y), __float_as_uint(s.y), false, false);
                float hx = lnx.x * __uint_as_float(rx[1]) + __uint_as_float(rx[0]), hy = lnx.y * __uint_as_float(ry[1]) + __uint_as_float(ry[0]);
                if (m_rg == 0) { hx += lnx.x; hy += lnx.y; }
                const int g = 32 * m_s + lane, gm = g / 80, gr = g - gm * 80, gw_ = gr / 10;
                if (lane < 32) st4(rws, OFF_E1 + (u32)(((m_rg * 4 + gm) * 8 + gw_) * 16 + (gr - gw_ * 10)) * 16, u32x4{__float_as_uint(hx), __float_as_uint(hy), tlo, thi});
            }
            BAR();
        }
        STAMP(1);
        wait_until(tstart, isM ? D_CKV : D_CKV2);
        const int kq = w >> 1, ht = w & 1;
        const u32 qoff = OFF_E3 + (u32)(kq * 12 * 32 + ht * 16 + (lane & 15)) * 64 + (lane >> 4) * 16;
        const u32 pfoff = OFF_PF + (u32)(TREF && w == 0 ? lane : w * 16 + (lane & 15)) * 16;
        u32x4 pf, sent;
#pragma unroll
        for (int j = CKV1; j < 12; ++j) {
            dma16nt(src, koff[j], lbase + L_KV + (u32)(w * 12 + j) * 1024);
            if (j == JPF) pf = ald4(rws4, pfoff);
            if (j - CKV1 >= YW) vmwait<YW>();
        }
        SB();
        STAMP(2);
        await<11 - JPF>(pf);
        if (anyl(tagbad(pf, tlo, thi))) {
            for (int it = 0; it < SPIN; ++it) { CFENCE();
                pf = ald4(rws4, pfoff); await<0>(pf);
                if (!anyl(tagbad(pf, tlo, thi))) break;
            }
        }
        const u32 opz = OFF_O + (u32)yi * 49152 + t * 16;
        const int wrg = yi % 12, wcs = yi / 12;
#define YPOISON() do { \
            st4(rws, (t < 16 && yi < 120) ? OFF_E6 + (u32)(wrg * 640 + wcs * 64) * 4 + lane * 16 : OOB, PZ); \
            SB(); } while (0)
        wait_until(tstart, D_S);
        sent = ald4(rws4, qoff + 11 * 2048);
        await<0>(sent);
        BAR();
        STAMP(3);
        u32x4 ow[8];
        const rsrc_t ro = mkrs(wo, 6144 * DM * 2);
        const u32 wobase = yi < 120 ? (u32)((wrg * 512 + w * 64 + (lane >> 3) * 8) * DM + wcs * 64 + (lane & 7) * 8) * 2 : OOB;
        u32x4 pf2;
        const u32 pf2off = OFF_PF2 + (u32)(kq * 32 + (lane & 31)) * 16;
        u32x4 va[6][2];
        {
            u32x4 qf[12];
            if (anyl(pois4(sent))) {
                for (int i2 = 0; i2 < SPIN; ++i2) { CFENCE();
                    sent = ald4(rws4, qoff + 11 * 2048); await<0>(sent);
                    if (!anyl(pois4(sent))) break; PSLEEP();
                }
            }
            STAMP(5);
            qf[11] = sent;
#pragma unroll
            for (int j = 0; j < 11; ++j) qf[j] = ld4(rws, qoff + j * 2048, AUX_SC1);
            SB();
            YPOISON();
            const int r = lane & 15;
            const int keyr = 8 * (r >> 3) + 2 * (r & 3) + ((r >> 2) & 1);
            u32x4 af[6][4];
#pragma unroll
            for (int ks = 0; ks < 6; ++ks) {
                const int ch = kq * 24 + ks * 4 + (lane >> 4);
                const int p = (ch & ~15) | ((ch ^ keyr) & 15);
#pragma unroll
                for (int kt = 0; kt < 4; ++kt) { af[ks][kt] = *(LAS u32x4*)(L + L_KV + (kt * 16 + keyr) * 1536 + p * 16); }
            }
            SB();
#define VAREADS() do { \
            { \
                const int g = lane >> 4, e = (lane >> 2) & 3, pp = lane & 3; \
_Pragma("unroll") \
                for (int q = 0; q < 6; ++q) { \
                    const int ch = 12 * w + 3 * pp + (q >> 1), hf = (q ^ (pp >> 1)) & 1; \
_Pragma("unroll") \
                    for (int k2 = 0; k2 < 2; ++k2) { \
                        const int ka = k2 * 32 + 8 * g + 2 * e, kb = ka + 1; \
                        const int pa = (ch & ~15) | ((ch ^ ka) & 15), pbx = (ch & ~15) | ((ch ^ kb) & 15); \
                        const s16x4 t0 = __builtin_amdgcn_ds_read_tr16_b64_v4i16((LAS s16x4*)(L + L_KV + ka * 1536 + pa * 16 + hf * 8)); \
                        const s16x4 t1 = __builtin_amdgcn_ds_read_tr16_b64_v4i16((LAS s16x4*)(L + L_KV + kb * 1536 + pbx * 16 + hf * 8)); \
                        const u32x2 a0 = __builtin_bit_cast(u32x2, t0), a1 = __builtin_bit_cast(u32x2, t1); \
                        va[q][k2] = u32x4{a0.x, a0.y, a1.x, a1.y}; \
                    } \
                } \
            } \
            } while (0)
            VAREADS();
#pragma unroll
            for (int q = 0; q < 6; ++q) { PIN1(va[q][0]); PIN1(va[q][1]); }
            f32x4 acc[4];
            u32 pm;
#define QKPASS() do { \
            _Pragma("unroll") for (int kt = 0; kt < 4; ++kt) acc[kt] = f32x4{0.f, 0.f, 0.f, 0.f}; \
            pm = 0; \
            _Pragma("unroll") for (int j = 0; j < 12; ++j) { \
                _Pragma("unroll") for (int kt = 0; kt < 4; ++kt) acc[kt] = mfma(af[j >> 1][kt], qf[j], acc[kt]); \
                pm = max(pm, max(max(qf[j].x, qf[j].y), max(qf[j].z, qf[j].w))); \
            } } while (0)
            QKPASS();
            if (anyl(pm == POISON)) {
                for (int it = 0; it < SPIN; ++it) { CFENCE();
#pragma unroll
                    for (int j = 0; j < 12; ++j) qf[j] = ld4(rws, qoff + j * 2048, AUX_SC1);
                    SB();
                    QKPASS();
                    if (!anyl(pm == POISON)) break;
                }
            }
            STAMP(6);
#pragma unroll
            for (int j = 0; j < WO1; ++j) ow[j] = ld4(ro, wobase + j * 1280, AUX_NT);
            SB();
            LAS float* sp = (LAS float*)(L + L_SP);
#pragma unroll
            for (int kt = 0; kt < 4; ++kt) *(LAS f32x4*)(sp + (kq * 32 + ht * 16 + (lane & 15)) * 68 + kt * 16 + 4 * (lane >> 4)) = acc[kt];
        }
        BAR();
        float sm_m, sm_l;
        {
            LAS float* sp = (LAS float*)(L + L_SP);
            const int hd = t >> 4, qd = t & 15;
            f32x4 s = *(LAS f32x4*)(sp + hd * 68 + qd * 4);
#pragma unroll
            for (int k = 1; k < 4; ++k) s += *(LAS f32x4*)(sp + (k * 32 + hd) * 68 + qd * 4);
            const float m = row_max(fmaxf(fmaxf(s.x, s.y), fmaxf(s.z, s.w)));
            const float p0 = __builtin_amdgcn_exp2f(s.x - m), p1 = __builtin_amdgcn_exp2f(s.y - m), p2 = __builtin_amdgcn_exp2f(s.z - m), p3 = __builtin_amdgcn_exp2f(s.w - m);
            const u32 b01 = cvtpk(p0, p1), b23 = cvtpk(p2, p3);
            *(LAS u32x2*)(L + L_P + hd * 144 + qd * 8) = u32x2{b01, b23};
            const float l = row_sum((bflo(b01) + bfhi(b01)) + (bflo(b23) + bfhi(b23)));
            sm_m = m; sm_l = l;
        }
        BAR();
        STAMP(7);
        {
            u32x4 pb[2][2];
#pragma unroll
            for (int hh = 0; hh < 2; ++hh)
#pragma unroll
                for (int k2 = 0; k2 < 2; ++k2) pb[hh][k2] = *(LAS u32x4*)(L + L_P + (hh * 16 + (lane & 15)) * 144 + k2 * 64 + (lane >> 4) * 16);
            STAMP(4);
            {
                const u32 ob2 = OFF_O + (u32)yi * 49152 + (u32)(lane >> 4) * 4 * 1536 + (u32)w * 192 + (lane & 15) * 12;
#pragma unroll
                for (int hh = 0; hh < 2; ++hh) {
                    f32x4 d[6];
#pragma unroll
                    for (int q = 0; q < 6; ++q) { d[q] = mfma(pb[hh][0], va[q][0], f32x4{0.f, 0.f, 0.f, 0.f}); }
#pragma unroll
                    for (int q = 0; q < 6; ++q) { d[q] = mfma(pb[hh][1], va[q][1], d[q]); }
#pragma unroll
                    for (int e = 0; e < 4; ++e)
                        __builtin_amdgcn_raw_buffer_store_b96(u32x3{cvtpk(d[0][e], d[1][e]), cvtpk(d[2][e], d[3][e]), cvtpk(d[4][e], d[5][e])}, rws, ob2 + (hh * 16 + e) * 1536, 0, AUX_SC1);
                }
            }
            SB();
#pragma unroll
            for (int j = 0; j < WOE; ++j) ow[WO1 + j] = ld4(ro, wobase + (WO1 + j) * 1280, AUX_NT);
            SB();
            vmwait<WOE>();
            STAMP(8);
            asm volatile("s_barrier" ::: "memory");
            st4(rws, (t & 15) == 0 ? OFF_ML + (u32)((t >> 4) * 128 + yi) * 16 : OOB, u32x4{__float_as_uint(sm_m), __float_as_uint(sm_l), tlo, thi});
            SB();
        }
        STAMP(9);
        if (yi < 120) {
#pragma unroll
            for (int j = WO1 + FLAGO * WOE; j < 8; ++j) {
                if (WOA > 0 && j == WO1 + FLAGO * WOE) wait_until(tstart, D_WA);
                if (j == WO1 + FLAGO * WOE + WOA) { SB(); { const u64 tr_ = TREFRD(); wait_until(tr_, D_WO); } SB(); }
                ow[j] = ld4(ro, wobase + j * 1280, AUX_NT); }
            SB();
            const int cc8 = lane & 7;
            const u32 goff = OFF_E5 + (u32)((cc8 < 4 ? 0 : 2) * 3072 + wrg * 256 + w * 32 + (lane >> 3) * 4 + (cc8 < 4 ? cc8 : 7 - cc8)) * 16;
            u32x4 g0, g1;
            SB();
            { const u64 tr_ = TREFRD(); wait_until(tr_, D_E5); }
            CFENCE();
            g0 = ld4(rws, goff, AUX_SC1); g1 = ld4(rws, goff + 3072 * 16, AUX_SC1);
            SB();
            f32x2 of[8][4];
#pragma unroll
            for (int j = 0; j < 8; ++j) { of[j][0] = up2(ow[j].x); of[j][1] = up2(ow[j].y); of[j][2] = up2(ow[j].z); of[j][3] = up2(ow[j].w); PIN1(of[j][0]); PIN1(of[j][1]); PIN1(of[j][2]); PIN1(of[j][3]); }
            STAMP(10);
            if (anyl(tagbad(g0, tlo, thi) | tagbad(g1, tlo, thi)))
            for (int it = 0; it < SPIN; ++it) { CFENCE();
                g0 = ld4(rws, goff, AUX_SC1); g1 = ld4(rws, goff + 3072 * 16, AUX_SC1);
                if (!anyl(tagbad(g0, tlo, thi) | tagbad(g1, tlo, thi))) break; PSLEEP();
            }
            STAMP(11);
            float vx = __uint_as_float(g0.x) + __uint_as_float(g1.x), vy = __uint_as_float(g0.y) + __uint_as_float(g1.y);
            vx += dpp<0x141>(vx); vy += dpp<0x141>(vy);
            vx = dppb<0x1B, 0xA>(vx, vx); vy = dppb<0x1B, 0xA>(vy, vy);
            f32x2 a2[4] = {{0.f, 0.f}, {0.f, 0.f}, {0.f, 0.f}, {0.f, 0.f}};
#define WSTEP(k) { const f32x2 hx = splat(dpp<k * 0x55>(vx)), hy = splat(dpp<k * 0x55>(vy)); _Pragma("unroll") for (int i = 0; i < 4; ++i) { a2[i] += hx * of[2 * k][i]; a2[i] += hy * of[2 * k + 1][i]; } }
            WSTEP(0) WSTEP(1) WSTEP(2) WSTEP(3)
#undef WSTEP
            float a[8] = {a2[0].x, a2[0].y, a2[1].x, a2[1].y, a2[2].x, a2[2].y, a2[3].x, a2[3].y};
            const float v = rscat8(a, lane);
            const int e = ((lane >> 5) & 1) * 4 + ((lane >> 4) & 1) * 2 + ((lane >> 3) & 1);
            LAS float* red = (LAS float*)(L + L_RW);
            red[w * 64 + (lane & 7) * 8 + e] = v;
            BAR();
            if (w == 0 && lane < 16) {
                f32x4 p[8];
#pragma unroll
                for (int k = 0; k < 8; ++k) p[k] = *(LAS f32x4*)(red + k * 64 + lane * 4);
                const f32x4 s4 = ((p[0] + p[1]) + (p[2] + p[3])) + ((p[4] + p[5]) + (p[6] + p[7]));
                st4(rws, OFF_E6 + (u32)(wrg * 640 + wcs * 64) * 4 + lane * 16, u32x4{canonf(s4.x), canonf(s4.y), canonf(s4.z), canonf(s4.w)});
            }
            STAMP(12);
        }
    }
}

extern "C" void kernel_launch(void* const* d_in, const int* in_sizes, int n_in, void* d_out, int out_size, void* d_ws, size_t ws_size, hipStream_t stream) {
    (void)in_sizes; (void)n_in; (void)out_size; (void)ws_size;
    hipFuncSetAttribute((const void*)adaln_mla_mlp_kernel, hipFuncAttributeMaxDynamicSharedMemorySize, LDS_BYTES);
    adaln_mla_mlp_kernel<<<NWG, NT, LDS_BYTES, stream>>>(
        (const u16*)d_in[0], (const u16*)d_in[1], (const u16*)d_in[2], (const u16*)d_in[3], (const u16*)d_in[4], (const u16*)d_in[5],
        (const u16*)d_in[7], (const u16*)d_in[8], (const u16*)d_in[9], (const u16*)d_in[10], (u16*)d_out, (unsigned char*)d_ws);
}
```
